```python
import torch

def _softmax(x, axis=-1):
    return torch.softmax(x, dim=axis)

def _gelu(x):
    return torch.nn.functional.gelu(x, approximate='tanh')

def _silu(x):
    return torch.nn.functional.silu(x)

def _sigmoid(x):
    return torch.sigmoid(x)

def _layernorm(x, axis=-1, eps=1e-05):
    m = x.mean(dim=axis, keepdim=True)
    v = x.var(dim=axis, keepdim=True, unbiased=False)
    return (x - m) / torch.sqrt(v + eps)

def _scatter_add(tgt, idx, src, axis=0):
    return tgt.clone().index_add_(axis, idx, src)

def _segment_sum(src, seg, n):
    out = torch.zeros((n,) + src.shape[1:], dtype=src.dtype)
    return out.index_add_(0, seg, src)

def _pad(x, pw):
    flat = tuple((v for pair in reversed(pw) for v in pair))
    return torch.nn.functional.pad(x, flat)
kernel_name = 'parallel_sum__memory_aug_recurrent_mem_tr__geglu_ffn__prompt'

def setup_inputs():

    _w = lambda a: (lambda t: t.to(torch.bfloat16) if t.is_floating_point() else t)(torch.as_tensor(a))

    def _A_setup():
        B, S, Dm = (1, 2048, 576)
        H, Dh = (32, 128)
        M, seg = (8, 32)
        seg = min(seg, S)
        S = S // seg * seg
        return {'x': _w((lambda *s: torch.randn(*s, dtype=torch.bfloat16) * 0.05)(B, S, Dm)), 'mem0': _w((lambda *s: torch.randn(*s, dtype=torch.bfloat16) * 1.6)(B, M, Dm)), 'Wq': _w((lambda *s: torch.randn(*s, dtype=torch.bfloat16) * 0.8)(Dm, H, Dh) * Dm ** (-0.5)), 'Wk': _w((lambda *s: torch.randn(*s, dtype=torch.bfloat16) * 0.8)(Dm, H, Dh) * Dm ** (-0.5)), 'Wv': _w((lambda *s: torch.randn(*s, dtype=torch.bfloat16) * 1.6)(Dm, H, Dh) * Dm ** (-0.5)), 'Wo': _w((lambda *s: torch.randn(*s, dtype=torch.bfloat16) * 1.6)(H, Dh, Dm) * (H * Dh) ** (-0.5)), 'seg_len': seg}
    _A_in = _A_setup()

    def _B_setup():
        B, S, D, F = (1, 2048, 576, 4608)
        return {'x': _w((lambda *s: torch.randn(*s, dtype=torch.bfloat16) * 0.05)(B, S, D)), 'w_gate': _w((lambda *s: torch.randn(*s, dtype=torch.bfloat16) * 0.05)(D, F)), 'w_up': _w((lambda *s: torch.randn(*s, dtype=torch.bfloat16) * 0.05)(D, F)), 'w_down': _w((lambda *s: torch.randn(*s, dtype=torch.bfloat16) * 0.05)(F, D))}
    _B_in = _B_setup()
    return {'A_x': _A_in['x'], 'A_mem0': _A_in['mem0'], 'A_Wq': _A_in['Wq'], 'A_Wk': _A_in['Wk'], 'A_Wv': _A_in['Wv'], 'A_Wo': _A_in['Wo'], 'A_seg_len': _A_in['seg_len'], 'B_x': _B_in['x'], 'B_w_gate': _B_in['w_gate'], 'B_w_up': _B_in['w_up'], 'B_w_down': _B_in['w_down']}

def reference(A_x, A_mem0, A_Wq, A_Wk, A_Wv, A_Wo, A_seg_len, B_x, B_w_gate, B_w_up, B_w_down):

    def _A_ref(x, mem0, Wq, Wk, Wv, Wo, seg_len):
        B, S, Dm = x.shape
        H, Dh = (Wq.shape[1], Wq.shape[2])
        M = mem0.shape[1]
        scale = Dh ** (-0.5)
        n_seg = S // seg_len
        L = M + seg_len
        idx = torch.arange(L)
        mask = (idx[:, None] < M) | (idx[:, None] >= idx[None, :])
        mem = mem0
        outs = []
        for s in range(n_seg):
            xs = x[:, s * seg_len:(s + 1) * seg_len, :].clone()
            h = torch.concatenate([mem, xs], axis=1)
            q = torch.einsum('btd,dhe->bhte', h, Wq)
            k = torch.einsum('btd,dhe->bhte', h, Wk)
            v = torch.einsum('btd,dhe->bhte', h, Wv)
            scores = torch.einsum('bhqe,bhke->bhqk', q, k) * scale
            scores = torch.where(mask[None, None], scores, -1000000000.0)
            p = _softmax(scores, axis=-1)
            o = torch.einsum('bhqk,bhke->bhqe', p, v)
            o = torch.einsum('bhte,hed->btd', o, Wo)
            mem = o[:, :M, :].clone()
            outs.append(o[:, M:, :])
        return torch.concatenate(outs, axis=1)

    def _B_ref(x, w_gate, w_up, w_down):
        return _gelu(x @ w_gate) * (x @ w_up) @ w_down
    x = A_x
    xn = _layernorm(x, axis=-1)
    return x + _A_ref(xn, A_mem0, A_Wq, A_Wk, A_Wv, A_Wo, A_seg_len) + _B_ref(xn, B_w_gate, B_w_up, B_w_down)
```

```cpp
#include <hip/hip_runtime.h>
#include <hip/hip_bf16.h>

typedef unsigned u32;
typedef unsigned long long u64;
typedef unsigned short u16;
typedef __attribute__((ext_vector_type(8))) __bf16 bf16x8;
typedef __attribute__((ext_vector_type(2))) __bf16 bf16x2;
typedef __attribute__((ext_vector_type(4))) float f32x4;
typedef __attribute__((ext_vector_type(2))) float f32x2;
typedef __attribute__((ext_vector_type(4))) unsigned u32x4;
typedef __attribute__((ext_vector_type(2))) unsigned u32x2;

constexpr int S_ = 2048, DM = 576, NH = 32, DH = 128, FF = 4608, NSEG = 64, KS = 18;
constexpr int NB = 72, NF = 144;
constexpr u32 HO_OFF = 0;
constexpr u32 PT_OFF = HO_OFF + NSEG * 65536;
constexpr u32 PT_STEP = 4 * 20480;
constexpr u32 POISON_BYTES = PT_OFF + NSEG * PT_STEP;
constexpr u32 CTL_OFF = POISON_BYTES;
constexpr u32 M0_OFF = CTL_OFF + 4096;
constexpr u32 XF_OFF = M0_OFF + 12288;
constexpr u32 XF_SEG = 2 * KS * 1024;
constexpr u32 WVO_OFF = XF_OFF + NSEG * XF_SEG;
constexpr u32 WVO_J = 16 * KS * 1024;
constexpr u32 H_OFF = WVO_OFF + NB * WVO_J;
constexpr u32 WDF_OFF = H_OFF + 128 * 144 * 1024;
constexpr u32 FO_OFF = WDF_OFF + 9 * 144 * 4096;
constexpr u32 AO_OFF = FO_OFF + S_ * DM * 2;
constexpr u32 WS_END = AO_OFF + S_ * DM * 2;
constexpr u32 POISON = 0xFFFFFFFFu;
constexpr int SC1 = 16;
constexpr u32 SPIN_CAP = 400000u;
constexpr int LDS_BYTES = 124 * 1024;

__device__ __forceinline__ u32 pk(float lo, float hi) {
  f32x2 v = {lo, hi};
  return __builtin_bit_cast(u32, __builtin_convertvector(v, bf16x2));
}
__device__ __forceinline__ u32 pks(float lo, float hi) { u32 r = pk(lo, hi); return r == POISON ? 0x7FC07FC0u : r; }
__device__ __forceinline__ float bfr(float x) { return (float)(__bf16)x; }
__device__ __forceinline__ float bflo(u32 w) { return __uint_as_float(w << 16); }
__device__ __forceinline__ float bfhi(u32 w) { return __uint_as_float(w & 0xFFFF0000u); }
__device__ __forceinline__ f32x4 mfma(u32x4 a, u32x4 b, f32x4 c) {
  return __builtin_amdgcn_mfma_f32_16x16x32_bf16(__builtin_bit_cast(bf16x8, a), __builtin_bit_cast(bf16x8, b), c, 0, 0, 0);
}
__device__ __forceinline__ u32 plo(u32 a, u32 b) { return __builtin_amdgcn_perm(b, a, 0x05040100u); }
__device__ __forceinline__ u32 phi(u32 a, u32 b) { return __builtin_amdgcn_perm(b, a, 0x07060302u); }
__device__ __forceinline__ float xmax(float x) {
  auto s = __builtin_amdgcn_permlane16_swap(__float_as_uint(x), __float_as_uint(x), false, false);
  x = fmaxf(__uint_as_float(s[0]), __uint_as_float(s[1]));
  auto t = __builtin_amdgcn_permlane32_swap(__float_as_uint(x), __float_as_uint(x), false, false);
  return fmaxf(__uint_as_float(t[0]), __uint_as_float(t[1]));
}
__device__ __forceinline__ float xsum(float x) {
  auto s = __builtin_amdgcn_permlane16_swap(__float_as_uint(x), __float_as_uint(x), false, false);
  x = __uint_as_float(s[0]) + __uint_as_float(s[1]);
  auto t = __builtin_amdgcn_permlane32_swap(__float_as_uint(x), __float_as_uint(x), false, false);
  return __uint_as_float(t[0]) + __uint_as_float(t[1]);
}
__device__ __forceinline__ bool ok4(u32x4 v) { return v.x != POISON && v.y != POISON && v.z != POISON && v.w != POISON; }
__device__ __forceinline__ bool ok2(u32x2 v) { return v.x != POISON && v.y != POISON; }
__device__ __forceinline__ void glds16(const void* g, u32 lds) {
  u32 keep;
  asm volatile("s_mov_b32 %0, m0\n\ts_mov_b32 m0, %2\n\ts_nop 0\n\tglobal_load_lds_dwordx4 %1, off\n\ts_mov_b32 m0, %0"
               : "=&s"(keep) : "v"(g), "s"(lds) : "memory");
}
__device__ __forceinline__ void wait_vm() { asm volatile("s_waitcnt vmcnt(0)" ::: "memory"); }
__device__ __forceinline__ void bar() {
  asm volatile("s_waitcnt lgkmcnt(0)" ::: "memory");
  __builtin_amdgcn_s_barrier();
  asm volatile("" ::: "memory");
}
__device__ __forceinline__ bool spin_fail(u32& spins, u32* tmo) {
  ++spins;
  if (spins > SPIN_CAP) { __hip_atomic_store(tmo, 1u, __ATOMIC_RELAXED, __HIP_MEMORY_SCOPE_AGENT); return true; }
  if ((spins & 1023u) == 0u && __hip_atomic_load(tmo, __ATOMIC_RELAXED, __HIP_MEMORY_SCOPE_AGENT) != 0u) return true;
  return false;
}
__device__ __forceinline__ u32 homap(u32 lo, u32 flip) { return ((lo & ~8191u) << 1) + (lo & 8191u) + flip; }
constexpr u32 HO_PM = 16384, HO_M = 20480;

struct Args {
  const u16* x; const u16* mem0; const u16* wq; const u16* wk; const u16* wv; const u16* wo;
  const u16* wg; const u16* wu; const u16* wd; unsigned char* ws; u16* out;
};

constexpr int K1_WVO = 288, K1_LN = 64, K1_GRID = 360;
__global__ void __launch_bounds__(576) k1_prep(Args a) {
  const int tid = threadIdx.x, lane = tid & 63, wave = __builtin_amdgcn_readfirstlane(tid >> 6);
  const int b = blockIdx.x;
  unsigned char* ws = a.ws;
  const int c16 = lane & 15, g = lane >> 4;
  if (b < 64 && (b & 7) == 0) {
    if (wave < 8) {
      const int blk = (b >> 3) * 8 + wave;
      u32 best[2] = {~0u, ~0u};
      for (int rep = 0; rep < 3; ++rep)
        for (int f = 0; f < 2; ++f) {
          u32* p = (u32*)(ws + HO_OFF + blk * 65536 + homap(32768 - 128 * (rep + 1), f * 8192));
          __hip_atomic_store(p, POISON, __ATOMIC_RELAXED, __HIP_MEMORY_SCOPE_AGENT);
          wait_vm();
          u64 t0, t1;
          asm volatile("s_memtime %0\n\ts_waitcnt lgkmcnt(0)" : "=s"(t0) :: "memory");
          u32 v = __hip_atomic_load(p, __ATOMIC_RELAXED, __HIP_MEMORY_SCOPE_AGENT);
          asm volatile("s_waitcnt vmcnt(0)" : "+v"(v) :: "memory");
          asm volatile("s_memtime %0\n\ts_waitcnt lgkmcnt(0)" : "=s"(t1) :: "memory");
          const u32 d = (u32)(t1 - t0);
          best[f] = d < best[f] ? d : best[f];
        }
      if (lane == 0) ((u32*)(ws + CTL_OFF))[blk] = best[1] < best[0] ? 8192u : 0u;
    } else if (b == 0) {
      if (lane == 0) { ((u32*)(ws + CTL_OFF))[64] = 0u; ((u32*)(ws + CTL_OFF))[128] = 0u; }
      for (int i = lane; i < 576; i += 64) {
        u32x4 v = *(const u32x4*)(a.mem0 + (i & 7) * DM + (i >> 3) * 8);
        if (v.x == POISON) v.x = 0x7FC07FC0u; if (v.y == POISON) v.y = 0x7FC07FC0u;
        if (v.z == POISON) v.z = 0x7FC07FC0u; if (v.w == POISON) v.w = 0x7FC07FC0u;
        *(u32x4*)(ws + M0_OFF + i * 16) = v;
      }
    }
    return;
  }
  const int job = b < 64 ? b - 1 - (b >> 3) : b - 8;
  {
    const u32x4 pv = {POISON, POISON, POISON, POISON};
    for (u32 o = (job * 576 + tid) * 16; o < POISON_BYTES; o += 352 * 576 * 16) *(u32x4*)(ws + o) = pv;
  }
  if (job < K1_WVO) {
    const int h = job / 9, kp = job % 9, bb = wave;
    u32x4 bf[4][4];
#pragma unroll
    for (int es = 0; es < 4; ++es) {
      u32x2 r[8];
#pragma unroll
      for (int jj = 0; jj < 8; ++jj)
        r[jj] = *(const u32x2*)(a.wo + (size_t)(h * DH + es * 32 + g * 8 + jj) * DM + bb * 64 + c16 * 4);
#pragma unroll
      for (int p = 0; p < 4; ++p) {
        bf[es][0][p] = plo(r[2 * p].x, r[2 * p + 1].x); bf[es][1][p] = phi(r[2 * p].x, r[2 * p + 1].x);
        bf[es][2][p] = plo(r[2 * p].y, r[2 * p + 1].y); bf[es][3][p] = phi(r[2 * p].y, r[2 * p + 1].y);
      }
    }
#pragma unroll
    for (int tt = 0; tt < 2; ++tt) {
      const int t = kp * 2 + tt;
      f32x4 acc[2][4];
#pragma unroll
      for (int rt = 0; rt < 2; ++rt) {
        const int k = t * 32 + (c16 >> 2) * 8 + (c16 & 3) + rt * 4;
#pragma unroll
        for (int i = 0; i < 4; ++i) acc[rt][i] = f32x4{0.f, 0.f, 0.f, 0.f};
#pragma unroll
        for (int es = 0; es < 4; ++es) {
          const u32x4 af = *(const u32x4*)(a.wv + (size_t)k * (NH * DH) + h * DH + es * 32 + g * 8);
#pragma unroll
          for (int i = 0; i < 4; ++i) acc[rt][i] = mfma(af, bf[es][i], acc[rt][i]);
        }
      }
      const int j = bb * 8 + (c16 >> 1);
      unsigned char* dst = ws + WVO_OFF + (size_t)j * WVO_J + ((h >> 1) * KS + t) * 1024 + (g * 16 + (h & 1) * 8 + (c16 & 1) * 4) * 16;
#pragma unroll
      for (int i = 0; i < 4; ++i) {
        u32x4 v = {pk(acc[0][i][0], acc[0][i][1]), pk(acc[0][i][2], acc[0][i][3]), pk(acc[1][i][0], acc[1][i][1]), pk(acc[1][i][2], acc[1][i][3])};
        *(u32x4*)(dst + i * 16) = v;
      }
    }
  } else if (wave < 8) {
    const int seg = job - K1_WVO;
#pragma unroll
    for (int rr = 0; rr < 4; ++rr) {
      const int row = wave * 4 + rr;
      const bool act = lane < 36;
      u32x4 v[2] = {{0, 0, 0, 0}, {0, 0, 0, 0}};
      if (act) { const u32x4* p = (const u32x4*)(a.x + (size_t)(seg * 32 + row) * DM + lane * 16); v[0] = p[0]; v[1] = p[1]; }
      float f[16];
#pragma unroll
      for (int i = 0; i < 8; ++i) { f[2 * i] = bflo(v[i >> 2][i & 3]); f[2 * i + 1] = bfhi(v[i >> 2][i & 3]); }
      float s = 0.f;
#pragma unroll
      for (int i = 0; i < 16; ++i) s += f[i];
#pragma unroll
      for (int o = 1; o < 64; o <<= 1) s += __shfl_xor(s, o);
      const float mean = s * (1.f / DM);
      float q = 0.f;
#pragma unroll
      for (int i = 0; i < 16; ++i) { const float d = f[i] - mean; q += act ? d * d : 0.f; }
#pragma unroll
      for (int o = 1; o < 64; o <<= 1) q += __shfl_xor(q, o);
      const float mb = bfr(mean), den = bfr(sqrtf(bfr(bfr(q * (1.f / DM)) + 1e-5f)));
      if (act) {
#pragma unroll
        for (int c = 0; c < 2; ++c) {
          u32x4 o;
#pragma unroll
          for (int i = 0; i < 4; ++i) o[i] = pk(bfr(f[c * 8 + 2 * i] - mb) / den, bfr(f[c * 8 + 2 * i + 1] - mb) / den);
          const int ch = lane * 2 + c;
          *(u32x4*)(ws + XF_OFF + seg * XF_SEG + ((row >> 4) * KS + (ch >> 2)) * 1024 + ((ch & 3) * 16 + (row & 15)) * 16) = o;
        }
      }
    }
  }
}

constexpr u32 L_X = 0;
constexpr u32 L_M = 2 * XF_SEG;
constexpr u32 L_QM = L_M + 9216;
constexpr u32 L_QX = L_QM + 8192;

__device__ __forceinline__ void stage_x(const unsigned char* ws, int seg, int wave, int lane) {
  const unsigned char* src = ws + XF_OFF + seg * XF_SEG + lane * 16;
  const u32 dst = L_X + (seg & 1) * XF_SEG;
#pragma unroll
  for (int i = 0; i < 5; ++i) {
    const int p = i * 8 + wave;
    if (p < 36) glds16(src + p * 1024, dst + p * 1024);
  }
}
__device__ __forceinline__ void gather_m(__amdgpu_buffer_rsrc_t r, u32 base, u32 flip, bool first, char* smem, int wave, int lane, u32* tmo) {
  const u32 l0 = (wave * 64 + lane) * 16, l1 = (512 + wave * 8 + (lane & 7)) * 16;
  const u32 o0 = first ? M0_OFF + l0 : base + homap(HO_M + l0, flip), o1 = first ? M0_OFF + l1 : base + homap(HO_M + l1, flip);
  u32 spins = 0;
  u32x4 v, w;
  for (;;) {
    v = __builtin_amdgcn_raw_buffer_load_b128(r, o0, 0, SC1);
    w = __builtin_amdgcn_raw_buffer_load_b128(r, o1, 0, SC1);
    if (__all(ok4(v) && ok4(w))) break;
    if (spin_fail(spins, tmo)) break;
  }
  *(u32x4*)(smem + L_M + l0) = v;
  if (lane < 8) *(u32x4*)(smem + L_M + l1) = w;
  wait_vm();
}

__device__ __forceinline__ void role_a(const Args& a, int h, char* smem) {
  const int tid = threadIdx.x, lane = tid & 63, wave = __builtin_amdgcn_readfirstlane(tid >> 6);
  const int c16 = lane & 15, g = lane >> 4;
  unsigned char* ws = a.ws;
  u32* tmo = (u32*)(ws + CTL_OFF) + 64;
  __amdgpu_buffer_rsrc_t r = __builtin_amdgcn_make_buffer_rsrc(ws, 0, WS_END, 0x00020000);
  stage_x(ws, 0, wave, lane);
  stage_x(ws, 1, wave, lane);
  const u32 flips = ((const u32*)(ws + CTL_OFF))[lane];
  u32x4 W[2][KS];
  {
    const u16* wsrc = (wave < 4 ? a.wq : a.wk) + h * DH + (wave & 3) * 32 + c16 * 2 + (size_t)g * 8 * (NH * DH);
#pragma unroll
    for (int t = 0; t < KS; ++t) {
      u32 d[8];
#pragma unroll
      for (int jj = 0; jj < 8; ++jj) d[jj] = *(const u32*)(wsrc + (size_t)(t * 32 + jj) * (NH * DH));
#pragma unroll
      for (int p = 0; p < 4; ++p) { W[0][t][p] = plo(d[2 * p], d[2 * p + 1]); W[1][t][p] = phi(d[2 * p], d[2 * p + 1]); }
    }
  }
  wait_vm();
  bar();
  const u32 qsel = (wave >> 2) * 8192 + (wave & 3) * 1024 + lane * 16;
  auto proj_x = [&](int seg) {
    const char* xb = smem + L_X + (seg & 1) * XF_SEG + lane * 16;
#pragma unroll
    for (int rt = 0; rt < 2; ++rt) {
      f32x4 c0 = {0.f, 0.f, 0.f, 0.f}, c1 = {0.f, 0.f, 0.f, 0.f};
#pragma unroll
      for (int t = 0; t < KS; ++t) {
        const u32x4 xf = *(const u32x4*)(xb + (rt * KS + t) * 1024);
        c0 = mfma(W[0][t], xf, c0); c1 = mfma(W[1][t], xf, c1);
      }
      u32x4 o = {pk(c0[0], c1[0]), pk(c0[1], c1[1]), pk(c0[2], c1[2]), pk(c0[3], c1[3])};
      *(u32x4*)(smem + L_QX + (seg & 1) * 16384 + qsel + rt * 4096) = o;
    }
  };
  proj_x(0);
  for (int s = 0; s < NSEG; ++s) {
    const u32 flip = __builtin_amdgcn_readlane(flips, s), base = HO_OFF + s * 65536;
    gather_m(r, base, flip, s == 0, smem, wave, lane, tmo);
    bar();
    {
      const char* mb = smem + L_M + g * 128 + (lane & 7) * 16;
      f32x4 c0 = {0.f, 0.f, 0.f, 0.f}, c1 = {0.f, 0.f, 0.f, 0.f};
#pragma unroll
      for (int t = 0; t < KS; ++t) {
        const u32x4 xf = *(const u32x4*)(mb + t * 512);
        c0 = mfma(W[0][t], xf, c0); c1 = mfma(W[1][t], xf, c1);
      }
      u32x4 o = {pk(c0[0], c1[0]), pk(c0[1], c1[1]), pk(c0[2], c1[2]), pk(c0[3], c1[3])};
      *(u32x4*)(smem + L_QM + wave * 1024 + lane * 16) = o;
    }
    bar();
    if (wave < 3 && (wave > 0 || s < NSEG - 1)) {
      const char* qx = smem + L_QX + (s & 1) * 16384 + lane * 16;
      const char* qb = wave == 0 ? smem + L_QM + lane * 16 : qx + (wave - 1) * 4096;
      const char* km = smem + L_QM + 4096 + lane * 16;
      f32x4 s0 = {0.f, 0.f, 0.f, 0.f}, s1 = s0, s2 = s0;
#pragma unroll
      for (int w = 0; w < 4; ++w) {
        const u32x4 qf = *(const u32x4*)(qb + w * 1024);
        s2 = mfma(*(const u32x4*)(km + w * 1024), qf, s2);
        s0 = mfma(*(const u32x4*)(qx + 8192 + w * 1024), qf, s0);
        s1 = mfma(*(const u32x4*)(qx + 12288 + w * 1024), qf, s1);
      }
      const int qi = wave == 0 ? 64 : (wave - 1) * 16 + c16;
      const float scale = 0.08838834764831845f, NEG = -1.0e9f;
      float v[12];
#pragma unroll
      for (int i = 0; i < 4; ++i) {
        v[i] = (4 * g + i <= qi) ? bfr(bfr(s0[i]) * scale) : NEG;
        v[4 + i] = (16 + 4 * g + i <= qi) ? bfr(bfr(s1[i]) * scale) : NEG;
        v[8 + i] = g < 2 ? bfr(bfr(s2[i]) * scale) : -INFINITY;
      }
      float m = v[0];
#pragma unroll
      for (int i = 1; i < 12; ++i) m = fmaxf(m, v[i]);
      m = xmax(m);
      float sum = 0.f;
#pragma unroll
      for (int i = 0; i < 12; ++i) { v[i] = __builtin_amdgcn_exp2f((v[i] - m) * 1.4426950408889634f); sum += v[i]; }
      sum = xsum(sum);
      const float inv = 1.0f / sum;
      const u32x4 px = {pks(v[0] * inv, v[1] * inv), pks(v[2] * inv, v[3] * inv), pks(v[4] * inv, v[5] * inv), pks(v[6] * inv, v[7] * inv)};
      const u32x2 pm = {pks(v[8] * inv, v[9] * inv), pks(v[10] * inv, v[11] * inv)};
      const u32 lx = (((h >> 1) * 4 + g) * 16 + (h & 1) * 8 + (lane & 7)) * 16, lm = HO_PM + h * 128 + g * 64 + (lane & 7) * 8;
      if (wave == 0) {
        if (c16 < 8) {
          __builtin_amdgcn_raw_buffer_store_b128(px, r, base + homap(lx, flip), 0, SC1);
          if (g < 2) __builtin_amdgcn_raw_buffer_store_b64(pm, r, base + homap(lm, flip), 0, SC1);
        }
      } else {
        const u32 tb = PT_OFF + s * PT_STEP + ((wave - 1) * 2 + (c16 >> 3)) * 20480;
        __builtin_amdgcn_raw_buffer_store_b128(px, r, tb + lx, 0, SC1);
        if (g < 2) __builtin_amdgcn_raw_buffer_store_b64(pm, r, tb + lm, 0, SC1);
      }
    }
    if (s + 2 < NSEG) stage_x(ws, s + 2, wave, lane);
    if (s + 1 < NSEG) proj_x(s + 1);
  }
}

__device__ __forceinline__ void role_b(const Args& a, int j, char* smem) {
  const int tid = threadIdx.x, lane = tid & 63, wave = __builtin_amdgcn_readfirstlane(tid >> 6);
  const int c16 = lane & 15, g = lane >> 4;
  unsigned char* ws = a.ws;
  u32* tmo = (u32*)(ws + CTL_OFF) + 64;
  __amdgpu_buffer_rsrc_t r = __builtin_amdgcn_make_buffer_rsrc(ws, 0, WS_END, 0x00020000);
  stage_x(ws, 0, wave, lane);
  stage_x(ws, 1, wave, lane);
  const u32 flips = ((const u32*)(ws + CTL_OFF))[lane];
  u32x4 W[2][KS];
  {
    const unsigned char* wsrc = ws + WVO_OFF + (size_t)j * WVO_J + wave * 2 * KS * 1024 + lane * 16;
#pragma unroll
    for (int tt = 0; tt < 2; ++tt)
#pragma unroll
      for (int t = 0; t < KS; ++t) W[tt][t] = *(const u32x4*)(wsrc + (tt * KS + t) * 1024);
  }
  wait_vm();
  bar();
  auto proj_x = [&](int seg, int rt, u32x2& o0, u32x2& o1) {
    const char* xb = smem + L_X + (seg & 1) * XF_SEG + rt * KS * 1024 + lane * 16;
    f32x4 c0 = {0.f, 0.f, 0.f, 0.f}, c1 = {0.f, 0.f, 0.f, 0.f};
#pragma unroll
    for (int t = 0; t < KS; ++t) {
      const u32x4 xf = *(const u32x4*)(xb + t * 1024);
      c0 = mfma(xf, W[0][t], c0); c1 = mfma(xf, W[1][t], c1);
    }
    o0 = u32x2{pk(c0[0], c0[1]), pk(c0[2], c0[3])};
    o1 = u32x2{pk(c1[0], c1[1]), pk(c1[2], c1[3])};
  };
  u32x2 n00, n01, n10, n11;
  proj_x(0, 0, n00, n01);
  proj_x(0, 1, n10, n11);
  const u32 lx = wave * 2048 + lane * 16;
  const u32 lm = HO_PM + (wave * 4 + (c16 >> 3)) * 128 + (g & 1) * 64 + (lane & 7) * 8;
  const bool diag = (c16 >> 3) == (g >> 1);
  const u32 redo = ((wave * 2 + (g >> 1)) * 64 + (lane & 7) * 8 + (g & 1) * 4) * 4;
  for (int s = 0; s < NSEG; ++s) {
    const u32 flip = __builtin_amdgcn_readlane(flips, s), base = HO_OFF + s * 65536;
    const u32x4 ux0 = {n00.x, n00.y, n10.x, n10.y}, ux1 = {n01.x, n01.y, n11.x, n11.y};
    gather_m(r, base, flip, s == 0, smem, wave, lane, tmo);
    bar();
    u32x4 um;
    {
      const char* mb = smem + L_M + g * 128 + (lane & 7) * 16;
      f32x4 c0 = {0.f, 0.f, 0.f, 0.f}, c1 = {0.f, 0.f, 0.f, 0.f};
#pragma unroll
      for (int t = 0; t < KS; ++t) {
        const u32x4 xf = *(const u32x4*)(mb + t * 512);
        c0 = mfma(xf, W[0][t], c0); c1 = mfma(xf, W[1][t], c1);
      }
      um = u32x4{pk(c0[0], c0[1]), pk(c0[2], c0[3]), pk(c1[0], c1[1]), pk(c1[2], c1[3])};
      if (g >= 2) um = u32x4{0, 0, 0, 0};
    }
    if (s + 1 < NSEG) {
      proj_x(s + 1, 0, n00, n01);
      const u32 o0 = base + homap(lx, flip), o1 = o0 + 1024, o2 = base + homap(lm, flip), o3 = o2 + 256;
      u32x4 p0, p1; u32x2 q0, q1;
      u32 spins = 0;
      for (;;) {
        p0 = __builtin_amdgcn_raw_buffer_load_b128(r, o0, 0, SC1);
        p1 = __builtin_amdgcn_raw_buffer_load_b128(r, o1, 0, SC1);
        q0 = __builtin_amdgcn_raw_buffer_load_b64(r, o2, 0, SC1);
        q1 = __builtin_amdgcn_raw_buffer_load_b64(r, o3, 0, SC1);
        if (__all(ok4(p0) && ok4(p1) && ok2(q0) && ok2(q1))) break;
        if (spin_fail(spins, tmo)) break;
      }
      f32x4 d = {0.f, 0.f, 0.f, 0.f}, e = d;
      u32x4 pmv = {q0.x, q0.y, q1.x, q1.y};
      if (g >= 2) pmv = u32x4{0, 0, 0, 0};
      d = mfma(ux0, p0, d); e = mfma(ux1, p1, e); d = mfma(um, pmv, d);
      d += e;
      if (diag) *(f32x4*)(smem + L_QM + redo) = d;
      bar();
      if (wave == 0) {
        const char* rp = smem + L_QM + (lane >> 5) * 2048 + (lane & 31) * 8;
        f32x2 acc = *(const f32x2*)rp;
#pragma unroll
        for (int p = 1; p < 8; ++p) acc += *(const f32x2*)(rp + p * 256);
        auto t0 = __builtin_amdgcn_permlane32_swap(__float_as_uint(acc.x), __float_as_uint(acc.x), false, false);
        auto t1 = __builtin_amdgcn_permlane32_swap(__float_as_uint(acc.y), __float_as_uint(acc.y), false, false);
        const u32 o = pks(__uint_as_float(t0[0]) + __uint_as_float(t0[1]), __uint_as_float(t1[0]) + __uint_as_float(t1[1]));
        const u32 nflip = __builtin_amdgcn_readlane(flips, s + 1);
        if (lane < 32) __builtin_amdgcn_raw_buffer_store_b32(o, r, base + 65536 + homap(HO_M + j * 128 + lane * 4, nflip), 0, SC1);
      }
    }
    if (s + 2 < NSEG) stage_x(ws, s + 2, wave, lane);
#pragma unroll
    for (int hf = 0; hf < 2; ++hf) {
      const u32 tb = PT_OFF + s * PT_STEP + hf * 40960;
      u32x4 p0[2], p1[2]; u32x2 q0[2], q1[2];
      u32 spins = 0;
      bool firstpass = true;
      for (;;) {
#pragma unroll
        for (int u = 0; u < 2; ++u) {
          p0[u] = __builtin_amdgcn_raw_buffer_load_b128(r, tb + u * 20480 + lx, 0, SC1);
          p1[u] = __builtin_amdgcn_raw_buffer_load_b128(r, tb + u * 20480 + lx + 1024, 0, SC1);
          q0[u] = __builtin_amdgcn_raw_buffer_load_b64(r, tb + u * 20480 + lm, 0, SC1);
          q1[u] = __builtin_amdgcn_raw_buffer_load_b64(r, tb + u * 20480 + lm + 256, 0, SC1);
        }
        if (hf == 0 && firstpass && s + 1 < NSEG) proj_x(s + 1, 1, n10, n11);
        firstpass = false;
        if (__all(ok4(p0[0]) && ok4(p1[0]) && ok2(q0[0]) && ok2(q1[0]) && ok4(p0[1]) && ok4(p1[1]) && ok2(q0[1]) && ok2(q1[1]))) break;
        if (spin_fail(spins, tmo)) break;
      }
#pragma unroll
      for (int u = 0; u < 2; ++u) {
        f32x4 d = {0.f, 0.f, 0.f, 0.f};
        u32x4 pmv = {q0[u].x, q0[u].y, q1[u].x, q1[u].y};
        if (g >= 2) pmv = u32x4{0, 0, 0, 0};
        d = mfma(ux0, p0[u], d); d = mfma(ux1, p1[u], d); d = mfma(um, pmv, d);
        if (diag) *(f32x4*)(smem + L_QX + (hf * 2 + u) * 4096 + redo) = d;
      }
    }
    bar();
    if (wave >= 6) {
      const int t2 = tid - 384;
      const char* rp = smem + L_QX + (t2 >> 5) * 4096 + (t2 & 31) * 8;
      f32x2 acc = *(const f32x2*)rp;
#pragma unroll
      for (int p = 1; p < 16; ++p) acc += *(const f32x2*)(rp + p * 256);
      *(u32*)(ws + AO_OFF + ((size_t)(s * 32 + (t2 >> 2)) * DM + j * 8 + (t2 & 3) * 2) * 2) = pk(acc.x, acc.y);
    }
  }
}

__device__ __forceinline__ float gelu_tanh(float x) {
  const float u = 0.7978845608028654f * (x + 0.044715f * x * x * x);
  const float e = __expf(2.f * u);
  return 0.5f * x * (2.f - 2.f / (e + 1.f));
}

__device__ __forceinline__ void role_f(const Args& a, int f, char* smem) {
  const int tid = threadIdx.x, lane = tid & 63, wave = __builtin_amdgcn_readfirstlane(tid >> 6);
  const int c16 = lane & 15, g = lane >> 4;
  unsigned char* ws = a.ws;
  u32* tmo = (u32*)(ws + CTL_OFF) + 64;
  u32* cnt = (u32*)(ws + CTL_OFF) + 128;
  __amdgpu_buffer_rsrc_t r = __builtin_amdgcn_make_buffer_rsrc(ws, 0, WS_END, 0x00020000);
  for (int bb = wave; bb < 9; bb += 8) {
    u32x2 d[8];
#pragma unroll
    for (int jj = 0; jj < 8; ++jj)
      d[jj] = *(const u32x2*)(a.wd + (size_t)(f * 32 + (jj >> 2) * 16 + g * 4 + (jj & 3)) * DM + bb * 64 + c16 * 4);
    u32x4 o[4];
#pragma unroll
    for (int p = 0; p < 4; ++p) {
      o[0][p] = plo(d[2 * p].x, d[2 * p + 1].x); o[1][p] = phi(d[2 * p].x, d[2 * p + 1].x);
      o[2][p] = plo(d[2 * p].y, d[2 * p + 1].y); o[3][p] = phi(d[2 * p].y, d[2 * p + 1].y);
    }
#pragma unroll
    for (int i = 0; i < 4; ++i) __builtin_amdgcn_raw_buffer_store_b128(o[i], r, WDF_OFF + ((bb * 144 + f) * 4 + i) * 1024 + lane * 16, 0, SC1);
  }
  {
    const int hv = wave & 1, tq = wave >> 1;
    u32x4 WG[KS], WU[KS];
    const size_t wo = (size_t)g * 8 * FF + f * 32 + hv * 16 + (c16 & ~1);
    const u32 sel = (c16 & 1) ? 0x07060302u : 0x05040100u;
#pragma unroll
    for (int t = 0; t < KS; ++t) {
      u32 dg[8], du[8];
#pragma unroll
      for (int jj = 0; jj < 8; ++jj) {
        dg[jj] = *(const u32*)(a.wg + wo + (size_t)(t * 32 + jj) * FF);
        du[jj] = *(const u32*)(a.wu + wo + (size_t)(t * 32 + jj) * FF);
      }
#pragma unroll
      for (int p = 0; p < 4; ++p) {
        WG[t][p] = __builtin_amdgcn_perm(dg[2 * p + 1], dg[2 * p], sel);
        WU[t][p] = __builtin_amdgcn_perm(du[2 * p + 1], du[2 * p], sel);
      }
    }
    for (int tile = tq; tile < 128; tile += 4) {
      const unsigned char* xb = ws + XF_OFF + tile * (KS * 1024) + lane * 16;
      f32x4 cg = {0.f, 0.f, 0.f, 0.f}, cu = {0.f, 0.f, 0.f, 0.f};
#pragma unroll
      for (int t = 0; t < KS; ++t) {
        const u32x4 xf = *(const u32x4*)(xb + t * 1024);
        cg = mfma(WG[t], xf, cg); cu = mfma(WU[t], xf, cu);
      }
      float hh[4];
#pragma unroll
      for (int i = 0; i < 4; ++i) hh[i] = bfr(gelu_tanh(bfr(cg[i]))) * bfr(cu[i]);
      const u32x2 o = {pk(hh[0], hh[1]), pk(hh[2], hh[3])};
      __builtin_amdgcn_raw_buffer_store_b64(o, r, H_OFF + (tile * 144 + f) * 1024 + lane * 16 + hv * 8, 0, SC1);
    }
  }
  wait_vm();
  __syncthreads();
  if (tid == 0) {
    __hip_atomic_fetch_add(cnt, 1u, __ATOMIC_RELAXED, __HIP_MEMORY_SCOPE_AGENT);
    u32 spins = 0;
    while (__hip_atomic_load(cnt, __ATOMIC_RELAXED, __HIP_MEMORY_SCOPE_AGENT) < (u32)NF) {
      __builtin_amdgcn_s_sleep(8);
      if (spin_fail(spins, tmo)) break;
    }
    __builtin_amdgcn_fence(__ATOMIC_ACQUIRE, "agent");
    wait_vm();
  }
  __syncthreads();
  {
    const int tm = f / 9, bb = f % 9, tg = wave & 3, kh = wave >> 2;
    const unsigned char* hb = ws + H_OFF + ((size_t)(tm * 8 + tg * 2) * 144 + kh * 72) * 1024 + lane * 16;
    const unsigned char* wb = ws + WDF_OFF + (size_t)(bb * 144 + kh * 72) * 4096 + lane * 16;
    f32x4 acc[2][4];
#pragma unroll
    for (int u = 0; u < 2; ++u)
#pragma unroll
      for (int i = 0; i < 4; ++i) acc[u][i] = f32x4{0.f, 0.f, 0.f, 0.f};
#pragma unroll 4
    for (int t = 0; t < 72; ++t) {
      const u32x4 h0 = *(const u32x4*)(hb + t * 1024), h1 = *(const u32x4*)(hb + 144 * 1024 + t * 1024);
#pragma unroll
      for (int i = 0; i < 4; ++i) {
        const u32x4 wf = *(const u32x4*)(wb + t * 4096 + i * 1024);
        acc[0][i] = mfma(wf, h0, acc[0][i]); acc[1][i] = mfma(wf, h1, acc[1][i]);
      }
    }
    if (kh == 1) {
#pragma unroll
      for (int u = 0; u < 2; ++u)
#pragma unroll
        for (int i = 0; i < 4; ++i) *(f32x4*)(smem + ((tg * 8 + u * 4 + i) * 64 + lane) * 16) = acc[u][i];
    }
    __syncthreads();
    if (kh == 0) {
#pragma unroll
      for (int u = 0; u < 2; ++u) {
#pragma unroll
        for (int i = 0; i < 4; ++i) acc[u][i] += *(const f32x4*)(smem + ((tg * 8 + u * 4 + i) * 64 + lane) * 16);
        u32x4 o0 = {pk(acc[u][0][0], acc[u][1][0]), pk(acc[u][2][0], acc[u][3][0]), pk(acc[u][0][1], acc[u][1][1]), pk(acc[u][2][1], acc[u][3][1])};
        u32x4 o1 = {pk(acc[u][0][2], acc[u][1][2]), pk(acc[u][2][2], acc[u][3][2]), pk(acc[u][0][3], acc[u][1][3]), pk(acc[u][2][3], acc[u][3][3])};
        u32x4* dst = (u32x4*)(ws + FO_OFF + ((size_t)(tm * 128 + tg * 32 + u * 16 + c16) * DM + bb * 64 + g * 16) * 2);
        dst[0] = o0; dst[1] = o1;
      }
    }
  }
}

__global__ void __launch_bounds__(512, 2) k2_main(Args a) {
  extern __shared__ __attribute__((aligned(16))) char smem[];
  const int b = blockIdx.x, rr = b & 7, q = b >> 3;
  if (rr == 0) role_a(a, q, smem);
  else if (rr < 4 && q < 24) role_b(a, (rr - 1) * 24 + q, smem);
  else {
    const int f = rr >= 4 ? (rr - 4) * 32 + q : 128 + (rr - 1) * 8 + (q - 24);
    if (f < NF) role_f(a, f, smem);
  }
}

__global__ void __launch_bounds__(256) k3_final(Args a) {
  const int i = blockIdx.x * 256 + threadIdx.x;
  const u32x4 x = ((const u32x4*)a.x)[i], p = ((const u32x4*)(a.ws + AO_OFF))[i], f = ((const u32x4*)(a.ws + FO_OFF))[i];
  u32x4 o;
#pragma unroll
  for (int k = 0; k < 4; ++k)
    o[k] = pk(bfr(bflo(x[k]) + bflo(p[k])) + bflo(f[k]), bfr(bfhi(x[k]) + bfhi(p[k])) + bfhi(f[k]));
  ((u32x4*)a.out)[i] = o;
}

extern "C" void kernel_launch(void* const* d_in, const int* in_sizes, int n_in, void* d_out, int out_size, void* d_ws,
                              size_t ws_size, hipStream_t stream) {
  Args a;
  a.x = (const u16*)d_in[0]; a.mem0 = (const u16*)d_in[1]; a.wq = (const u16*)d_in[2]; a.wk = (const u16*)d_in[3];
  a.wv = (const u16*)d_in[4]; a.wo = (const u16*)d_in[5]; a.wg = (const u16*)d_in[8]; a.wu = (const u16*)d_in[9];
  a.wd = (const u16*)d_in[10];
  a.ws = (unsigned char*)(((uintptr_t)d_ws + 65535) & ~(uintptr_t)65535);
  a.out = (u16*)d_out;
  (void)hipFuncSetAttribute((const void*)k2_main, hipFuncAttributeMaxDynamicSharedMemorySize, LDS_BYTES);
  k1_prep<<<K1_GRID, 576, 0, stream>>>(a);
  k2_main<<<256, 512, LDS_BYTES, stream>>>(a);
  k3_final<<<S_ * DM / 8 / 256, 256, 0, stream>>>(a);
}
```

```cpp
#include <hip/hip_runtime.h>
#include <hip/hip_bf16.h>

typedef unsigned u32;
typedef unsigned long long u64;
typedef unsigned short u16;
typedef __attribute__((ext_vector_type(8))) __bf16 bf16x8;
typedef __attribute__((ext_vector_type(2))) __bf16 bf16x2;
typedef __attribute__((ext_vector_type(4))) float f32x4;
typedef __attribute__((ext_vector_type(2))) float f32x2;
typedef __attribute__((ext_vector_type(4))) unsigned u32x4;
typedef __attribute__((ext_vector_type(2))) unsigned u32x2;

constexpr int S_ = 2048, DM = 576, NH = 32, DH = 128, FF = 4608, NSEG = 64, KS = 18;
constexpr int NB = 72, NF = 96;
constexpr u32 HO_OFF = 0;
constexpr u32 PT_OFF = HO_OFF + NSEG * 65536;
constexpr u32 PT_STEP = 4 * 20480;
constexpr u32 FLG_OFF = PT_OFF + NSEG * PT_STEP;
constexpr u32 POISON_BYTES = FLG_OFF + NSEG * 128;
constexpr u32 CTL_OFF = POISON_BYTES;
constexpr u32 M0_OFF = CTL_OFF + 4096;
constexpr u32 XF_OFF = M0_OFF + 12288;
constexpr u32 XF_SEG = 2 * KS * 1024;
constexpr u32 WVO_OFF = XF_OFF + NSEG * XF_SEG;
constexpr u32 WVO_J = 16 * KS * 1024;
constexpr u32 WQK_OFF = WVO_OFF + NB * WVO_J;
constexpr u32 WQK_H = 24 * KS * 1024;
constexpr u32 UX_OFF = WQK_OFF + NH * WQK_H;
constexpr u32 H_OFF = UX_OFF + NSEG * NB * 16384;
constexpr u32 WDF_OFF = H_OFF + 128 * 144 * 1024;
constexpr u32 FO_OFF = WDF_OFF + 9 * 144 * 4096;
constexpr u32 AO_OFF = FO_OFF + S_ * DM * 2;
constexpr u32 WS_END = AO_OFF + S_ * DM * 2;
constexpr u32 POISON = 0xFFFFFFFFu;
constexpr int SC1 = 16;
constexpr u32 SPIN_CAP = 400000u;
constexpr int LDS_BYTES = 160 * 1024;

__device__ __forceinline__ u32 pk(float lo, float hi) {
  f32x2 v = {lo, hi};
  return __builtin_bit_cast(u32, __builtin_convertvector(v, bf16x2));
}
__device__ __forceinline__ u32 pks(float lo, float hi) { u32 r = pk(lo, hi); return r == POISON ? 0x7FC07FC0u : r; }
__device__ __forceinline__ float bfr(float x) { return (float)(__bf16)x; }
__device__ __forceinline__ float bflo(u32 w) { return __uint_as_float(w << 16); }
__device__ __forceinline__ float bfhi(u32 w) { return __uint_as_float(w & 0xFFFF0000u); }
__device__ __forceinline__ f32x4 mfma(u32x4 a, u32x4 b, f32x4 c) {
  return __builtin_amdgcn_mfma_f32_16x16x32_bf16(__builtin_bit_cast(bf16x8, a), __builtin_bit_cast(bf16x8, b), c, 0, 0, 0);
}
__device__ __forceinline__ u32 plo(u32 a, u32 b) { return __builtin_amdgcn_perm(b, a, 0x05040100u); }
__device__ __forceinline__ u32 phi(u32 a, u32 b) { return __builtin_amdgcn_perm(b, a, 0x07060302u); }
__device__ __forceinline__ float xmax(float x) {
  auto s = __builtin_amdgcn_permlane16_swap(__float_as_uint(x), __float_as_uint(x), false, false);
  x = fmaxf(__uint_as_float(s[0]), __uint_as_float(s[1]));
  auto t = __builtin_amdgcn_permlane32_swap(__float_as_uint(x), __float_as_uint(x), false, false);
  return fmaxf(__uint_as_float(t[0]), __uint_as_float(t[1]));
}
__device__ __forceinline__ float xsum(float x) {
  auto s = __builtin_amdgcn_permlane16_swap(__float_as_uint(x), __float_as_uint(x), false, false);
  x = __uint_as_float(s[0]) + __uint_as_float(s[1]);
  auto t = __builtin_amdgcn_permlane32_swap(__float_as_uint(x), __float_as_uint(x), false, false);
  return __uint_as_float(t[0]) + __uint_as_float(t[1]);
}
__device__ __forceinline__ float ror8(float x) {
  return __builtin_bit_cast(float, __builtin_amdgcn_mov_dpp(__builtin_bit_cast(int, x), 0x128, 0xf, 0xf, true));
}
__device__ __forceinline__ u32 mx(u32 a, u32 b) { return a > b ? a : b; }
__device__ __forceinline__ u32 mx4(u32x4 v) { return mx(mx(v.x, v.y), mx(v.z, v.w)); }
__device__ __forceinline__ u32 mx2(u32x2 v) { return mx(v.x, v.y); }
__device__ __forceinline__ bool allok(u32 m) { return __all(m != POISON); }
__device__ __forceinline__ void glds16(const void* g, u32 lds) {
  u32 keep;
  asm volatile("s_mov_b32 %0, m0\n\ts_mov_b32 m0, %2\n\ts_nop 0\n\tglobal_load_lds_dwordx4 %1, off\n\ts_mov_b32 m0, %0"
               : "=&s"(keep) : "v"(g), "s"(lds) : "memory");
}
__device__ __forceinline__ void wait_vm() { asm volatile("s_waitcnt vmcnt(0)" ::: "memory"); }
__device__ __forceinline__ void bar() {
  asm volatile("s_waitcnt lgkmcnt(0)" ::: "memory");
  __builtin_amdgcn_s_barrier();
  asm volatile("" ::: "memory");
}
__device__ __forceinline__ bool spin_fail(u32& spins, u32* tmo) {
  ++spins;
  if (spins > SPIN_CAP) { __hip_atomic_store(tmo, 1u, __ATOMIC_RELAXED, __HIP_MEMORY_SCOPE_AGENT); return true; }
  if ((spins & 1023u) == 0u && __hip_atomic_load(tmo, __ATOMIC_RELAXED, __HIP_MEMORY_SCOPE_AGENT) != 0u) return true;
  return false;
}
__device__ __forceinline__ u32 homap(u32 lo, u32 flip) { return ((lo & ~8191u) << 1) + (lo & 8191u) + flip; }
constexpr u32 HO_PM = 16384, HO_M = 20480;

#define STAMP(step, k) do {} while (0)
#define STAMPC(step, k) do {} while (0)
#define DUMP() do {} while (0)
struct Args {
  const u16* x; const u16* mem0; const u16* wq; const u16* wk; const u16* wv; const u16* wo;
  const u16* wg; const u16* wu; const u16* wd; unsigned char* ws; u16* out;
};

constexpr int K1_WVO = 288, K1_LN = 64, K1_QK = 192, K1_JOBS = K1_WVO + K1_LN + K1_QK, K1_GRID = K1_JOBS + 8;
__global__ void __launch_bounds__(576) k1_prep(Args a) {
  const int tid = threadIdx.x, lane = tid & 63, wave = __builtin_amdgcn_readfirstlane(tid >> 6);
  const int b = blockIdx.x;
  unsigned char* ws = a.ws;
  const int c16 = lane & 15, g = lane >> 4;
  if (b < 64 && (b & 7) == 0) {
    if (wave < 8) {
      const int blk = (b >> 3) * 8 + wave;
      u32 best[2] = {~0u, ~0u};
      for (int rep = 0; rep < 3; ++rep)
        for (int f = 0; f < 2; ++f) {
          u32* p = (u32*)(ws + HO_OFF + blk * 65536 + homap(32768 - 128 * (rep + 1), f * 8192));
          __hip_atomic_store(p, POISON, __ATOMIC_RELAXED, __HIP_MEMORY_SCOPE_AGENT);
          wait_vm();
          u64 t0, t1;
          asm volatile("s_memtime %0\n\ts_waitcnt lgkmcnt(0)" : "=s"(t0) :: "memory");
          u32 v = __hip_atomic_load(p, __ATOMIC_RELAXED, __HIP_MEMORY_SCOPE_AGENT);
          asm volatile("s_waitcnt vmcnt(0)" : "+v"(v) :: "memory");
          asm volatile("s_memtime %0\n\ts_waitcnt lgkmcnt(0)" : "=s"(t1) :: "memory");
          const u32 d = (u32)(t1 - t0);
          best[f] = d < best[f] ? d : best[f];
        }
      if (lane == 0) ((u32*)(ws + CTL_OFF))[blk] = best[1] < best[0] ? 8192u : 0u;
    } else if (b == 0) {
      if (lane == 0) { ((u32*)(ws + CTL_OFF))[64] = 0u; ((u32*)(ws + CTL_OFF))[128] = 0u; }
      for (int i = lane; i < 576; i += 64) {
        u32x4 v = *(const u32x4*)(a.mem0 + (i & 7) * DM + (i >> 3) * 8);
        if (v.x == POISON) v.x = 0x7FC07FC0u; if (v.y == POISON) v.y = 0x7FC07FC0u;
        if (v.z == POISON) v.z = 0x7FC07FC0u; if (v.w == POISON) v.w = 0x7FC07FC0u;
        *(u32x4*)(ws + M0_OFF + i * 16) = v;
      }
    }
    return;
  }
  const int job = b < 64 ? b - 1 - (b >> 3) : b - 8;
  {
    const u32x4 pv = {POISON, POISON, POISON, POISON};
    for (u32 o = (job * 576 + tid) * 16; o < POISON_BYTES; o += K1_JOBS * 576 * 16) *(u32x4*)(ws + o) = pv;
  }
  if (job < K1_WVO) {
    const int h = job / 9, kp = job % 9, bb = wave;
    u32x4 bf[4][4];
#pragma unroll
    for (int es = 0; es < 4; ++es) {
      u32x2 r[8];
#pragma unroll
      for (int jj = 0; jj < 8; ++jj)
        r[jj] = *(const u32x2*)(a.wo + (size_t)(h * DH + es * 32 + g * 8 + jj) * DM + bb * 64 + c16 * 4);
#pragma unroll
      for (int p = 0; p < 4; ++p) {
        bf[es][0][p] = plo(r[2 * p].x, r[2 * p + 1].x); bf[es][1][p] = phi(r[2 * p].x, r[2 * p + 1].x);
        bf[es][2][p] = plo(r[2 * p].y, r[2 * p + 1].y); bf[es][3][p] = phi(r[2 * p].y, r[2 * p + 1].y);
      }
    }
#pragma unroll
    for (int tt = 0; tt < 2; ++tt) {
      const int t = kp * 2 + tt;
      f32x4 acc[2][4];
#pragma unroll
      for (int rt = 0; rt < 2; ++rt) {
        const int k = t * 32 + (c16 >> 2) * 8 + (c16 & 3) + rt * 4;
#pragma unroll
        for (int i = 0; i < 4; ++i) acc[rt][i] = f32x4{0.f, 0.f, 0.f, 0.f};
#pragma unroll
        for (int es = 0; es < 4; ++es) {
          const u32x4 af = *(const u32x4*)(a.wv + (size_t)k * (NH * DH) + h * DH + es * 32 + g * 8);
#pragma unroll
          for (int i = 0; i < 4; ++i) acc[rt][i] = mfma(af, bf[es][i], acc[rt][i]);
        }
      }
      const int j = bb * 8 + (c16 >> 1);
      unsigned char* dst = ws + WVO_OFF + (size_t)j * WVO_J + ((h >> 1) * KS + t) * 1024 + (g * 16 + (h & 1) * 8 + (c16 & 1) * 4) * 16;
#pragma unroll
      for (int i = 0; i < 4; ++i) {
        u32x4 v = {pk(acc[0][i][0], acc[0][i][1]), pk(acc[0][i][2], acc[0][i][3]), pk(acc[1][i][0], acc[1][i][1]), pk(acc[1][i][2], acc[1][i][3])};
        *(u32x4*)(dst + i * 16) = v;
      }
    }
  } else if (job >= K1_WVO + K1_LN) {
    const int it = (job - K1_WVO - K1_LN) * 9 + wave, mat = it / (NH * KS), h = (it / KS) % NH, t = it % KS;
    const u16* src = (mat == 0 ? a.wq : mat == 1 ? a.wk : a.wv) + (size_t)(t * 32 + g * 8) * (NH * DH) + h * DH + c16 * 8;
    u32x4 d[8];
#pragma unroll
    for (int jj = 0; jj < 8; ++jj) d[jj] = *(const u32x4*)(src + (size_t)jj * (NH * DH));
    unsigned char* dst = ws + WQK_OFF + (size_t)h * WQK_H + (mat * 8 * KS + t) * 1024 + lane * 16;
#pragma unroll
    for (int i = 0; i < 4; ++i) {
      const u32x4 lo = {plo(d[0][i], d[1][i]), plo(d[2][i], d[3][i]), plo(d[4][i], d[5][i]), plo(d[6][i], d[7][i])};
      const u32x4 hi = {phi(d[0][i], d[1][i]), phi(d[2][i], d[3][i]), phi(d[4][i], d[5][i]), phi(d[6][i], d[7][i])};
      *(u32x4*)(dst + (2 * i) * KS * 1024) = lo;
      *(u32x4*)(dst + (2 * i + 1) * KS * 1024) = hi;
    }
  } else if (wave < 8) {
    const int seg = job - K1_WVO;
#pragma unroll
    for (int rr = 0; rr < 4; ++rr) {
      const int row = wave * 4 + rr;
      const bool act = lane < 36;
      u32x4 v[2] = {{0, 0, 0, 0}, {0, 0, 0, 0}};
      if (act) { const u32x4* p = (const u32x4*)(a.x + (size_t)(seg * 32 + row) * DM + lane * 16); v[0] = p[0]; v[1] = p[1]; }
      float f[16];
#pragma unroll
      for (int i = 0; i < 8; ++i) { f[2 * i] = bflo(v[i >> 2][i & 3]); f[2 * i + 1] = bfhi(v[i >> 2][i & 3]); }
      float s = 0.f;
#pragma unroll
      for (int i = 0; i < 16; ++i) s += f[i];
#pragma unroll
      for (int o = 1; o < 64; o <<= 1) s += __shfl_xor(s, o);
      const float mean = s * (1.f / DM);
      float q = 0.f;
#pragma unroll
      for (int i = 0; i < 16; ++i) { const float d = f[i] - mean; q += act ? d * d : 0.f; }
#pragma unroll
      for (int o = 1; o < 64; o <<= 1) q += __shfl_xor(q, o);
      const float mb = bfr(mean), den = bfr(sqrtf(bfr(bfr(q * (1.f / DM)) + 1e-5f)));
      if (act) {
#pragma unroll
        for (int c = 0; c < 2; ++c) {
          u32x4 o;
#pragma unroll
          for (int i = 0; i < 4; ++i) o[i] = pk(bfr(f[c * 8 + 2 * i] - mb) / den, bfr(f[c * 8 + 2 * i + 1] - mb) / den);
          const int ch = lane * 2 + c;
          *(u32x4*)(ws + XF_OFF + seg * XF_SEG + ((row >> 4) * KS + (ch >> 2)) * 1024 + ((ch & 3) * 16 + (row & 15)) * 16) = o;
        }
      }
    }
  }
}

constexpr u32 L_X = 0;
constexpr u32 L_M = 2 * XF_SEG;
constexpr u32 L_QM = L_M + 9216;
constexpr u32 L_QX = L_QM + 8192;

template <bool WA, int ST>
__device__ __forceinline__ void proj2(const u32x4 (&W0)[KS], const u32x4 (&W1)[KS], const char* xb, f32x4& c0, f32x4& c1) {
  u32x4 xf[KS];
#pragma unroll
  for (int t = 0; t < KS; ++t) xf[t] = *(const u32x4*)(xb + t * ST);
  c0 = f32x4{0.f, 0.f, 0.f, 0.f}; c1 = c0;
#pragma unroll
  for (int t = 0; t < KS; ++t) {
    c0 = WA ? mfma(W0[t], xf[t], c0) : mfma(xf[t], W0[t], c0);
    c1 = WA ? mfma(W1[t], xf[t], c1) : mfma(xf[t], W1[t], c1);
  }
  __builtin_amdgcn_sched_group_barrier(0x100, 6, 0);
#pragma unroll
  for (int t = 0; t < KS - 6; ++t) { __builtin_amdgcn_sched_group_barrier(0x008, 2, 0); __builtin_amdgcn_sched_group_barrier(0x100, 1, 0); }
  __builtin_amdgcn_sched_group_barrier(0x008, 12, 0);
}
__device__ __forceinline__ void stage_x(const unsigned char* ws, int seg, int wave, int lane) {
  const unsigned char* src = ws + XF_OFF + seg * XF_SEG + lane * 16;
  const u32 dst = L_X + (seg & 1) * XF_SEG;
#pragma unroll
  for (int i = 0; i < 5; ++i) {
    const int p = i * 8 + wave;
    if (p < 36) glds16(src + p * 1024, dst + p * 1024);
  }
}
__device__ __forceinline__ void stage_x5(const unsigned char* ws, int seg, int w5, int lane, int part) {
  const unsigned char* src = ws + XF_OFF + seg * XF_SEG + lane * 16;
  const u32 dst = L_X + (seg & 1) * XF_SEG;
#pragma unroll
  for (int i = 0; i < 4; ++i) {
    const int p = (part * 4 + i) * 5 + w5;
    if (p < 36) glds16(src + p * 1024, dst + p * 1024);
  }
}
__device__ __forceinline__ void gather_m(__amdgpu_buffer_rsrc_t r, u32 base, u32 flip, bool first, char* smem, int wave, int lane, u32* tmo) {
  const u32 l0 = (wave * 64 + lane) * 16, l1 = (512 + wave * 8 + (lane & 7)) * 16;
  const u32 o0 = first ? M0_OFF + l0 : base + homap(HO_M + l0, flip), o1 = first ? M0_OFF + l1 : base + homap(HO_M + l1, flip);
  u32 spins = 0;
  u32x4 v, w;
  for (;;) {
    v = __builtin_amdgcn_raw_buffer_load_b128(r, o0, 0, SC1);
    w = __builtin_amdgcn_raw_buffer_load_b128(r, o1, 0, SC1);
    if (allok(mx(mx4(v), mx4(w)))) break;
    if (spin_fail(spins, tmo)) break;
  }
  *(u32x4*)(smem + L_M + l0) = v;
  if (lane < 8) *(u32x4*)(smem + L_M + l1) = w;
  wait_vm();
}

__device__ __forceinline__ void role_a(const Args& a, int h, char* smem) {
  const int tid = threadIdx.x, lane = tid & 63, wave = __builtin_amdgcn_readfirstlane(tid >> 6);
  const int c16 = lane & 15, g = lane >> 4;
  unsigned char* ws = a.ws;
  u32* tmo = (u32*)(ws + CTL_OFF) + 64;
  __amdgpu_buffer_rsrc_t r = __builtin_amdgcn_make_buffer_rsrc(ws, 0, WS_END, 0x00020000);
  STAMP(64, 0);
  stage_x(ws, 0, wave, lane);
  stage_x(ws, 1, wave, lane);
  const u32 flips = ((const u32*)(ws + CTL_OFF))[lane];
  u32x4 W[2][KS];
  {
    const unsigned char* wsrc = ws + WQK_OFF + (size_t)h * WQK_H + wave * 2 * KS * 1024 + lane * 16;
#pragma unroll
    for (int tt = 0; tt < 2; ++tt)
#pragma unroll
      for (int t = 0; t < KS; ++t) W[tt][t] = *(const u32x4*)(wsrc + (tt * KS + t) * 1024);
  }
  wait_vm();
  bar();
  const u32 qsel = (wave >> 2) * 8192 + (wave & 3) * 1024 + lane * 16;
  auto proj_x = [&](int seg, int rt) {
    f32x4 c0, c1;
    proj2<true, 1024>(W[0], W[1], smem + L_X + (seg & 1) * XF_SEG + rt * KS * 1024 + lane * 16, c0, c1);
    u32x4 o = {pk(c0[0], c1[0]), pk(c0[1], c1[1]), pk(c0[2], c1[2]), pk(c0[3], c1[3])};
    *(u32x4*)(smem + L_QX + (seg & 1) * 16384 + qsel + rt * 4096) = o;
  };
  const u32 lx = (((h >> 1) * 4 + g) * 16 + (h & 1) * 8 + (lane & 7)) * 16, lm = HO_PM + h * 128 + g * 64 + (lane & 7) * 8;
  STAMP(64, 1); STAMPC(65, 0); STAMP(65, 1);
  proj_x(0, 0);
  proj_x(0, 1);
  for (int s = 0; s < NSEG; ++s) {
    const u32 flip = __builtin_amdgcn_readlane(flips, s), base = HO_OFF + s * 65536;
    STAMP(s, 0);
    gather_m(r, base, flip, s == 0, smem, wave, lane, tmo);
    STAMP(s, 1);
    bar();
    STAMP(s, 2);
    {
      f32x4 c0, c1;
      proj2<true, 512>(W[0], W[1], smem + L_M + g * 128 + (lane & 7) * 16, c0, c1);
      u32x4 o = {pk(c0[0], c1[0]), pk(c0[1], c1[1]), pk(c0[2], c1[2]), pk(c0[3], c1[3])};
      *(u32x4*)(smem + L_QM + wave * 1024 + lane * 16) = o;
    }
    u32x4 kf[8];
    if (wave == 0) {
      __builtin_amdgcn_s_setprio(3);
#pragma unroll
      for (int w = 0; w < 8; ++w) kf[w] = *(const u32x4*)(smem + L_QX + (s & 1) * 16384 + 8192 + lane * 16 + w * 1024);
    }
    bar();
    STAMP(s, 3);
    const float C2 = 0.08838834764831845f * 1.4426950408889634f;
    if (wave == 0) {
      if (s < NSEG - 1) {
        const char* qb = smem + L_QM + lane * 16;
        f32x4 s0 = {0.f, 0.f, 0.f, 0.f}, s1 = s0, s2 = s0;
#pragma unroll
        for (int w = 0; w < 4; ++w) {
          const u32x4 qf = *(const u32x4*)(qb + w * 1024);
          s2 = mfma(*(const u32x4*)(qb + 4096 + w * 1024), qf, s2);
          s0 = mfma(kf[w], qf, s0);
          s1 = mfma(kf[4 + w], qf, s1);
        }
        const bool hi = c16 >= 8;
        float v[6];
#pragma unroll
        for (int i = 0; i < 4; ++i) v[i] = hi ? s1[i] : s0[i];
        v[4] = g < 2 ? (hi ? s2[2] : s2[0]) : -INFINITY;
        v[5] = g < 2 ? (hi ? s2[3] : s2[1]) : -INFINITY;
        float m = fmaxf(fmaxf(fmaxf(v[0], v[1]), fmaxf(v[2], v[3])), fmaxf(v[4], v[5]));
        m = fmaxf(m, ror8(m));
        m = xmax(m) * C2;
        float sum = 0.f;
#pragma unroll
        for (int i = 0; i < 6; ++i) { v[i] = __builtin_amdgcn_exp2f(__builtin_fmaf(v[i], C2, -m)); sum += v[i]; }
        sum += ror8(sum);
        const float inv = __builtin_amdgcn_rcpf(xsum(sum));
        const u32x2 px = {pk(v[0] * inv, v[1] * inv) & 0x7FFF7FFFu, pk(v[2] * inv, v[3] * inv) & 0x7FFF7FFFu};
        const u32 pm = pk(v[4] * inv, v[5] * inv) & 0x7FFF7FFFu;
        __builtin_amdgcn_raw_buffer_store_b64(px, r, base + homap(lx + (c16 >> 3) * 8, flip), 0, SC1);
        if (g < 2) __builtin_amdgcn_raw_buffer_store_b32(pm, r, base + homap(lm + (c16 >> 3) * 4, flip), 0, SC1);
      }
      __builtin_amdgcn_s_setprio(0);
    } else if (wave < 3) {
      const char* qx = smem + L_QX + (s & 1) * 16384 + lane * 16;
      f32x4 s0 = {0.f, 0.f, 0.f, 0.f}, s1 = s0, s2 = s0;
#pragma unroll
      for (int w = 0; w < 4; ++w) {
        const u32x4 qf = *(const u32x4*)(qx + (wave - 1) * 4096 + w * 1024);
        s2 = mfma(*(const u32x4*)(smem + L_QM + 4096 + lane * 16 + w * 1024), qf, s2);
        s0 = mfma(*(const u32x4*)(qx + 8192 + w * 1024), qf, s0);
        s1 = mfma(*(const u32x4*)(qx + 12288 + w * 1024), qf, s1);
      }
      const int qi = (wave - 1) * 16 + c16;
      const float NEG = -1.0e9f * 1.4426950408889634f;
      float v[12];
#pragma unroll
      for (int i = 0; i < 4; ++i) {
        v[i] = (4 * g + i <= qi) ? s0[i] * C2 : NEG;
        v[4 + i] = (16 + 4 * g + i <= qi) ? s1[i] * C2 : NEG;
        v[8 + i] = g < 2 ? s2[i] * C2 : -INFINITY;
      }
      float m = v[0];
#pragma unroll
      for (int i = 1; i < 12; ++i) m = fmaxf(m, v[i]);
      m = xmax(m);
      float sum = 0.f;
#pragma unroll
      for (int i = 0; i < 12; ++i) { v[i] = __builtin_amdgcn_exp2f(v[i] - m); sum += v[i]; }
      const float inv = __builtin_amdgcn_rcpf(xsum(sum));
      u32x4 px; u32x2 pm;
#pragma unroll
      for (int i = 0; i < 4; ++i) px[i] = pk(v[2 * i] * inv, v[2 * i + 1] * inv) & 0x7FFF7FFFu;
#pragma unroll
      for (int i = 0; i < 2; ++i) pm[i] = pk(v[8 + 2 * i] * inv, v[9 + 2 * i] * inv) & 0x7FFF7FFFu;
      const u32 tb = PT_OFF + s * PT_STEP + ((wave - 1) * 2 + (c16 >> 3)) * 20480;
      __builtin_amdgcn_raw_buffer_store_b128(px, r, tb + lx, 0, SC1);
      if (g < 2) __builtin_amdgcn_raw_buffer_store_b64(pm, r, tb + lm, 0, SC1);
    }
    STAMP(s, 4);
    if (s + 1 < NSEG) {
      if (wave >= 3 && s + 2 < NSEG) stage_x5(ws, s + 2, wave - 3, lane, 0);
      proj_x(s + 1, 0);
      if (wave >= 3 && s + 2 < NSEG) stage_x5(ws, s + 2, wave - 3, lane, 1);
      proj_x(s + 1, 1);
    }
  }
  STAMPC(65, 2); STAMP(65, 3);
}

__device__ __forceinline__ void role_b(const Args& a, int j, char* smem) {
  const int tid = threadIdx.x, lane = tid & 63, wave = __builtin_amdgcn_readfirstlane(tid >> 6);
  const int c16 = lane & 15, g = lane >> 4;
  unsigned char* ws = a.ws;
  u32* tmo = (u32*)(ws + CTL_OFF) + 64;
  __amdgpu_buffer_rsrc_t r = __builtin_amdgcn_make_buffer_rsrc(ws, 0, WS_END, 0x00020000);
  STAMP(64, 0);
  const u32 flips = ((const u32*)(ws + CTL_OFF))[lane];
  u32x4 W[2][KS];
  {
    const unsigned char* wsrc = ws + WVO_OFF + (size_t)j * WVO_J + wave * 2 * KS * 1024 + lane * 16;
#pragma unroll
    for (int tt = 0; tt < 2; ++tt)
#pragma unroll
      for (int t = 0; t < KS; ++t) W[tt][t] = *(const u32x4*)(wsrc + (tt * KS + t) * 1024);
  }
  STAMP(64, 1);
  u32x4 nx0, nx1;
  u32 fl = POISON;
  auto ux_load = [&](int seg) {
    const u32 fo = FLG_OFF + seg * 128 + wave * 16 + (lane & 3) * 4;
    u32 spins = 0;
    while (!allok(fl)) {
      fl = __builtin_amdgcn_raw_buffer_load_b32(r, fo, 0, SC1);
      if (spin_fail(spins, tmo)) break;
    }
    asm volatile("" ::: "memory");
    const u32 uo = UX_OFF + ((seg * NB + j) * 16 + wave * 2) * 1024 + lane * 16;
    nx0 = __builtin_amdgcn_raw_buffer_load_b128(r, uo, 0, SC1);
    nx1 = __builtin_amdgcn_raw_buffer_load_b128(r, uo + 1024, 0, SC1);
    fl = seg + 1 < NSEG ? __builtin_amdgcn_raw_buffer_load_b32(r, fo + 128, 0, SC1) : 0u;
  };
  const u32 lx = wave * 2048 + lane * 16;
  const u32 lm = HO_PM + (wave * 4 + (c16 >> 3)) * 128 + (g & 1) * 64 + (lane & 7) * 8;
  const bool diag = (c16 >> 3) == (g >> 1);
  const u32 redo = ((wave * 2 + (g >> 1)) * 64 + (lane & 7) * 8 + (g & 1) * 4) * 4;
  u32x4 tp0[2], tp1[2]; u32x2 tq0[2], tq1[2];
  auto tok_load = [&](int seg, int hf) {
    const u32 tb = PT_OFF + seg * PT_STEP + hf * 40960;
#pragma unroll
    for (int u = 0; u < 2; ++u) {
      tp0[u] = __builtin_amdgcn_raw_buffer_load_b128(r, tb + u * 20480 + lx, 0, SC1);
      tp1[u] = __builtin_amdgcn_raw_buffer_load_b128(r, tb + u * 20480 + lx + 1024, 0, SC1);
      tq0[u] = __builtin_amdgcn_raw_buffer_load_b64(r, tb + u * 20480 + lm, 0, SC1);
      tq1[u] = __builtin_amdgcn_raw_buffer_load_b64(r, tb + u * 20480 + lm + 256, 0, SC1);
    }
  };
  auto tok_use = [&](int seg, int hf, u32x4 x0, u32x4 x1, u32x4 m) {
    u32 spins = 0;
    while (!allok(mx(mx(mx(mx4(tp0[0]), mx4(tp1[0])), mx(mx2(tq0[0]), mx2(tq1[0]))), mx(mx(mx4(tp0[1]), mx4(tp1[1])), mx(mx2(tq0[1]), mx2(tq1[1])))))) {
      if (spin_fail(spins, tmo)) break;
      tok_load(seg, hf);
    }
#pragma unroll
    for (int u = 0; u < 2; ++u) {
      f32x4 d = {0.f, 0.f, 0.f, 0.f};
      const u32x4 pmv = {tq0[u].x, tq0[u].y, tq1[u].x, tq1[u].y};
      d = mfma(x0, tp0[u], d); d = mfma(x1, tp1[u], d); d = mfma(m, pmv, d);
      if (diag) *(f32x4*)(smem + L_QX + (seg & 1) * 16384 + (hf * 2 + u) * 4096 + redo) = d;
    }
  };
  auto tok_out = [&](int seg) {
    const int t2 = tid & 127;
    const char* rp = smem + L_QX + (seg & 1) * 16384 + (t2 >> 5) * 4096 + (t2 & 31) * 8;
    f32x2 acc = *(const f32x2*)rp;
#pragma unroll
    for (int p = 1; p < 16; ++p) acc += *(const f32x2*)(rp + p * 256);
    *(u32*)(ws + AO_OFF + ((size_t)(seg * 32 + (t2 >> 2)) * DM + j * 8 + (t2 & 3) * 2) * 2) = pk(acc.x, acc.y);
  };
  u32x4 ux0 = {0, 0, 0, 0}, ux1 = ux0, um = ux0, ox0 = ux0, ox1 = ux0, om = ux0;
  for (int s = 0; s < NSEG; ++s) {
    const u32 flip = __builtin_amdgcn_readlane(flips, s), base = HO_OFF + s * 65536;
    STAMP(s, 0);
    gather_m(r, base, flip, s == 0, smem, wave, lane, tmo);
    bar();
    STAMP(s, 1);
    if (s > 0) tok_load(s - 1, 0);
    if (wave >= 6 && s > 1) tok_out(s - 2);
    {
      f32x4 c0, c1;
      proj2<false, 512>(W[0], W[1], smem + L_M + g * 128 + (lane & 7) * 16, c0, c1);
      if (s > 0) tok_use(s - 1, 0, ux0, ux1, um);
      ox0 = ux0; ox1 = ux1; om = um;
      um = u32x4{pk(c0[0], c0[1]), pk(c0[2], c0[3]), pk(c1[0], c1[1]), pk(c1[2], c1[3])};
      if (g >= 2) um = u32x4{0, 0, 0, 0};
      if (s == 0) ux_load(0);
      ux0 = nx0; ux1 = nx1;
    }
    if (s + 1 < NSEG) {
      STAMP(s, 2);
      const u32 o0 = base + homap(lx, flip), o1 = o0 + 1024, o2 = base + homap(lm, flip), o3 = o2 + 256;
      u32x4 p0, p1; u32x2 q0, q1;
      u32 spins = 0;
      for (;;) {
        p0 = __builtin_amdgcn_raw_buffer_load_b128(r, o0, 0, SC1);
        p1 = __builtin_amdgcn_raw_buffer_load_b128(r, o1, 0, SC1);
        q0 = __builtin_amdgcn_raw_buffer_load_b64(r, o2, 0, SC1);
        q1 = __builtin_amdgcn_raw_buffer_load_b64(r, o3, 0, SC1);
        if (allok(mx(mx(mx4(p0), mx4(p1)), mx(mx2(q0), mx2(q1))))) break;
        if (spin_fail(spins, tmo)) break;
      }
      STAMP(s, 3);
      f32x4 d = {0.f, 0.f, 0.f, 0.f}, e = d;
      const u32x4 pmv = {q0.x, q0.y, q1.x, q1.y};
      d = mfma(ux0, p0, d); e = mfma(ux1, p1, e); d = mfma(um, pmv, d);
      d += e;
      if (diag) *(f32x4*)(smem + L_QM + redo) = d;
    }
    bar();
    if (wave == 0 && s + 1 < NSEG) {
      const char* rp = smem + L_QM + (lane >> 5) * 2048 + (lane & 31) * 8;
      f32x2 acc = *(const f32x2*)rp;
#pragma unroll
      for (int p = 1; p < 8; ++p) acc += *(const f32x2*)(rp + p * 256);
      auto t0 = __builtin_amdgcn_permlane32_swap(__float_as_uint(acc.x), __float_as_uint(acc.x), false, false);
      auto t1 = __builtin_amdgcn_permlane32_swap(__float_as_uint(acc.y), __float_as_uint(acc.y), false, false);
      const u32 o = pks(__uint_as_float(t0[0]) + __uint_as_float(t0[1]), __uint_as_float(t1[0]) + __uint_as_float(t1[1]));
      const u32 nflip = __builtin_amdgcn_readlane(flips, s + 1);
      if (lane < 32) __builtin_amdgcn_raw_buffer_store_b32(o, r, base + 65536 + homap(HO_M + j * 128 + lane * 4, nflip), 0, SC1);
    }
    STAMP(s, 4);
    if (s > 0) tok_load(s - 1, 1);
    if (s + 1 < NSEG) ux_load(s + 1);
    STAMP(s, 5);
    if (s > 0) tok_use(s - 1, 1, ox0, ox1, om);
  }
  tok_load(NSEG - 1, 0);
  tok_use(NSEG - 1, 0, ux0, ux1, um);
  tok_load(NSEG - 1, 1);
  tok_use(NSEG - 1, 1, ux0, ux1, um);
  bar();
  if (wave >= 6) tok_out(NSEG - 1);
  else if (wave >= 4) tok_out(NSEG - 2);
  STAMP(65, 0);
}

__device__ __forceinline__ void role_v(const Args& a, int h, char* smem) {
  const int tid = threadIdx.x, lane = tid & 63, wave = __builtin_amdgcn_readfirstlane(tid >> 6);
  const int c16 = lane & 15, g = lane >> 4;
  unsigned char* ws = a.ws;
  __amdgpu_buffer_rsrc_t r = __builtin_amdgcn_make_buffer_rsrc(ws, 0, WS_END, 0x00020000);
  STAMP(64, 0);
  u32x4 WV[KS], WO[5][4];
  {
    const unsigned char* wsrc = ws + WQK_OFF + (size_t)h * WQK_H + (16 + wave) * KS * 1024 + lane * 16;
#pragma unroll
    for (int t = 0; t < KS; ++t) WV[t] = *(const u32x4*)(wsrc + t * 1024);
#pragma unroll
    for (int b = 0; b < 2; ++b) {
      u32x4 st[9];
#pragma unroll
      for (int i = 0; i < 9; ++i) st[i] = *(const u32x4*)(a.wo + (size_t)h * DH * DM + ((b * 9 + i) * 512 + tid) * 8);
#pragma unroll
      for (int i = 0; i < 9; ++i) { const int pc = (b * 9 + i) * 512 + tid; *(u32x4*)(smem + (pc / 72) * 1168 + (pc % 72) * 16) = st[i]; }
    }
    __syncthreads();
    const u32 sel = (c16 & 1) ? 0x07060302u : 0x05040100u;
#pragma unroll
    for (int i = 0; i < 5; ++i) {
      const int ct = wave + 8 * i < 36 ? wave + 8 * i : 0;
      const char* wl = smem + g * 32 * 1168 + ct * 32 + (c16 >> 1) * 4;
#pragma unroll
      for (int es = 0; es < 4; ++es) {
#pragma unroll
        for (int p = 0; p < 4; ++p) {
          const int e0 = 8 * ((2 * p) & 3) + 2 * es + (p >> 1);
          WO[i][es][p] = __builtin_amdgcn_perm(*(const u32*)(wl + (e0 + 8) * 1168), *(const u32*)(wl + e0 * 1168), sel);
        }
        asm volatile("" : "+v"(WO[i][es]) :: "memory");
      }
    }
    __syncthreads();
  }
  STAMP(64, 1);
  stage_x(ws, 0, wave, lane);
  for (int sg = 0; sg < NSEG; ++sg) {
    wait_vm();
    bar();
    if (sg > 0 && tid == 0) __builtin_amdgcn_raw_buffer_store_b32(1u, r, FLG_OFF + (sg - 1) * 128 + h * 4, 0, SC1);
    if (sg + 1 < NSEG) stage_x(ws, sg + 1, wave, lane);
    {
      const char* xb = smem + L_X + (sg & 1) * XF_SEG + lane * 16;
      f32x4 c0 = {0.f, 0.f, 0.f, 0.f}, c1 = c0;
#pragma unroll
      for (int t = 0; t < KS; ++t) {
        c0 = mfma(WV[t], *(const u32x4*)(xb + t * 1024), c0);
        c1 = mfma(WV[t], *(const u32x4*)(xb + (KS + t) * 1024), c1);
      }
      __builtin_amdgcn_sched_group_barrier(0x100, 8, 0);
#pragma unroll
      for (int t = 0; t < KS - 4; ++t) { __builtin_amdgcn_sched_group_barrier(0x008, 2, 0); __builtin_amdgcn_sched_group_barrier(0x100, 2, 0); }
      __builtin_amdgcn_sched_group_barrier(0x008, 8, 0);
      char* vt = smem + L_QM + (wave >> 1) * 1024 + lane * 16 + (wave & 1) * 8;
      *(u32x2*)vt = u32x2{pk(c0[0], c0[1]), pk(c0[2], c0[3])};
      *(u32x2*)(vt + 4096) = u32x2{pk(c1[0], c1[1]), pk(c1[2], c1[3])};
    }
    bar();
    u32x4 vf[2][4];
#pragma unroll
    for (int rt = 0; rt < 2; ++rt)
#pragma unroll
      for (int es = 0; es < 4; ++es) vf[rt][es] = *(const u32x4*)(smem + L_QM + rt * 4096 + es * 1024 + lane * 16);
#pragma unroll
    for (int i = 0; i < 5; ++i) {
      const int ct = wave + 8 * i;
      if (ct < 36) {
        f32x4 d0 = {0.f, 0.f, 0.f, 0.f}, d1 = d0;
#pragma unroll
        for (int es = 0; es < 4; ++es) { d0 = mfma(vf[0][es], WO[i][es], d0); d1 = mfma(vf[1][es], WO[i][es], d1); }
        const u32x4 o = {pk(d0[0], d0[1]), pk(d0[2], d0[3]), pk(d1[0], d1[1]), pk(d1[2], d1[3])};
        __builtin_amdgcn_raw_buffer_store_b128(o, r, UX_OFF + ((sg * NB + ct * 2 + (c16 >> 3)) * 16 + (h >> 1)) * 1024 + (g * 16 + (h & 1) * 8 + (lane & 7)) * 16, 0, SC1);
      }
    }
    if (sg == 0) STAMP(64, 2);
  }
  wait_vm();
  bar();
  if (tid == 0) __builtin_amdgcn_raw_buffer_store_b32(1u, r, FLG_OFF + (NSEG - 1) * 128 + h * 4, 0, SC1);
  STAMP(64, 3);
}

__device__ __forceinline__ void role_p(const Args& a, int half) {
  u32* tmo = (u32*)(a.ws + CTL_OFF) + 64;
  __amdgpu_buffer_rsrc_t r = __builtin_amdgcn_make_buffer_rsrc(a.ws, 0, WS_END, 0x00020000);
  for (int s = 0; s < NSEG - 1; ++s) {
    const u32 o = PT_OFF + s * PT_STEP + half * 40960 + threadIdx.x * 16;
    u32 spins = 0;
    for (;;) {
      u32 m = 0;
#pragma unroll
      for (int i = 0; i < 5; ++i) m = mx(m, mx4(__builtin_amdgcn_raw_buffer_load_b128(r, o + i * 8192, 0, SC1)));
      if (allok(m)) break;
      if (spin_fail(spins, tmo)) return;
      __builtin_amdgcn_s_sleep(2);
    }
  }
}

__device__ __forceinline__ float gelu_tanh(float x) {
  const float u = 0.7978845608028654f * (x + 0.044715f * x * x * x);
  const float e = __expf(2.f * u);
  return 0.5f * x * (2.f - 2.f / (e + 1.f));
}

__device__ __forceinline__ void role_f(const Args& a, int f, char* smem) {
  const int tid = threadIdx.x, lane = tid & 63, wave = __builtin_amdgcn_readfirstlane(tid >> 6);
  const int c16 = lane & 15, g = lane >> 4;
  unsigned char* ws = a.ws;
  u32* tmo = (u32*)(ws + CTL_OFF) + 64;
  u32* cnt = (u32*)(ws + CTL_OFF) + 128;
  __amdgpu_buffer_rsrc_t r = __builtin_amdgcn_make_buffer_rsrc(ws, 0, WS_END, 0x00020000);
  STAMP(64, 0);
  for (int it = f * 27 + wave; it < f * 27 + 27; it += 8) {
    const int ks = it / 18, bb = it % 18;
    u32 d[8];
#pragma unroll
    for (int jj = 0; jj < 8; ++jj)
      d[jj] = *(const u32*)(a.wd + (size_t)(ks * 32 + (jj >> 2) * 16 + g * 4 + (jj & 3)) * DM + bb * 32 + c16 * 2);
    const u32x4 lo = {plo(d[0], d[1]), plo(d[2], d[3]), plo(d[4], d[5]), plo(d[6], d[7])};
    const u32x4 hi = {phi(d[0], d[1]), phi(d[2], d[3]), phi(d[4], d[5]), phi(d[6], d[7])};
    __builtin_amdgcn_raw_buffer_store_b128(lo, r, WDF_OFF + (bb * 144 + ks) * 2048 + lane * 16, 0, SC1);
    __builtin_amdgcn_raw_buffer_store_b128(hi, r, WDF_OFF + (bb * 144 + ks) * 2048 + 1024 + lane * 16, 0, SC1);
  }
#pragma unroll 1
  for (int job = 0; job < 2; ++job) {
    int tid = threadIdx.x;
    asm volatile("" : "+v"(tid));
    const int hv = wave & 1, tq = wave >> 1;
    const int sl = (f >> 1) * 3 + (f & 1) + job;
    const int q0 = ((f & 1) && job == 0) ? 16 : 0, q1 = (!(f & 1) && job == 1) ? 16 : 32;
    u32x4 WG[KS], WU[KS];
    __syncthreads();
    {
      u32x4 st[9];
#pragma unroll
      for (int i = 0; i < 9; ++i) {
        const int pc = i * 512 + tid, mat = pc >= 2304, rw = (pc - mat * 2304) >> 2;
        st[i] = *(const u32x4*)((mat ? a.wu : a.wg) + (size_t)rw * FF + sl * 32 + (pc & 3) * 8);
      }
#pragma unroll
      for (int i = 0; i < 9; ++i) { const int pc = i * 512 + tid; *(u32x4*)(smem + (pc >> 2) * 80 + (pc & 3) * 16) = st[i]; }
      __syncthreads();
      const char* wl = smem + g * 8 * 80 + hv * 32 + (c16 >> 1) * 4;
      const u32 sel = (c16 & 1) ? 0x07060302u : 0x05040100u;
#pragma unroll
      for (int t = 0; t < KS; ++t) {
#pragma unroll
        for (int p = 0; p < 4; ++p) {
          const int o = (t * 32 + 2 * p) * 80;
          WG[t][p] = __builtin_amdgcn_perm(*(const u32*)(wl + o + 80), *(const u32*)(wl + o), sel);
          WU[t][p] = __builtin_amdgcn_perm(*(const u32*)(wl + 576 * 80 + o + 80), *(const u32*)(wl + 576 * 80 + o), sel);
        }
        asm volatile("" : "+v"(WG[t]), "+v"(WU[t]) :: "memory");
      }
      __syncthreads();
    }
    if (job == 0) STAMP(64, 1);
    auto stage_q = [&](int quad, int i0) {
#pragma unroll
      for (int i = 0; i < 3; ++i) {
        const int p = wave * 9 + i0 + i;
        glds16(ws + XF_OFF + (size_t)quad * 73728 + p * 1024 + lane * 16, (quad & 1) * 73728 + p * 1024);
      }
    };
    stage_q(q0, 0); stage_q(q0, 3); stage_q(q0, 6);
    for (int quad = q0; quad < q1; ++quad) {
      wait_vm();
      bar();
      if (quad + 1 < q1) { stage_q(quad + 1, 0); stage_q(quad + 1, 3); stage_q(quad + 1, 6); }
      f32x4 cg, cu;
      proj2<true, 1024>(WG, WU, smem + (quad & 1) * 73728 + tq * KS * 1024 + lane * 16, cg, cu);
      float hh[4];
#pragma unroll
      for (int i = 0; i < 4; ++i) hh[i] = bfr(gelu_tanh(bfr(cg[i]))) * bfr(cu[i]);
      const u32x2 o = {pk(hh[0], hh[1]), pk(hh[2], hh[3])};
      __builtin_amdgcn_raw_buffer_store_b64(o, r, H_OFF + ((quad * 4 + tq) * 144 + sl) * 1024 + lane * 16 + hv * 8, 0, SC1);
    }
  }
  wait_vm();
  __syncthreads();
  STAMP(64, 2);
  if (tid == 0) {
    __hip_atomic_fetch_add(cnt, 1u, __ATOMIC_RELAXED, __HIP_MEMORY_SCOPE_AGENT);
    u32 spins = 0;
    while (__hip_atomic_load(cnt, __ATOMIC_RELAXED, __HIP_MEMORY_SCOPE_AGENT) < (u32)NF) {
      __builtin_amdgcn_s_sleep(8);
      if (spin_fail(spins, tmo)) break;
    }
    __builtin_amdgcn_fence(__ATOMIC_ACQUIRE, "agent");
    wait_vm();
  }
  __syncthreads();
  STAMP(64, 3);
  {
    const int tm = f / 6, b3 = (f % 6) * 3, tg = wave & 3, kh = wave >> 2;
    const unsigned char* hb = ws + H_OFF + ((size_t)(tm * 8 + tg * 2) * 144 + kh * 72) * 1024 + lane * 16;
    const unsigned char* wb = ws + WDF_OFF + (size_t)(b3 * 144 + kh * 72) * 2048 + lane * 16;
    f32x4 acc[2][6];
#pragma unroll
    for (int u = 0; u < 2; ++u)
#pragma unroll
      for (int i = 0; i < 6; ++i) acc[u][i] = f32x4{0.f, 0.f, 0.f, 0.f};
    u32x4 hf[2][4][2], wf[2][4][6];
    auto ld = [&](int st, int t0) {
#pragma unroll
      for (int t = 0; t < 4; ++t) {
        hf[st][t][0] = *(const u32x4*)(hb + (t0 + t) * 1024); hf[st][t][1] = *(const u32x4*)(hb + (144 + t0 + t) * 1024);
#pragma unroll
        for (int i = 0; i < 6; ++i) wf[st][t][i] = *(const u32x4*)(wb + ((i >> 1) * 144 + t0 + t) * 2048 + (i & 1) * 1024);
      }
    };
    auto mm = [&](int st) {
#pragma unroll
      for (int t = 0; t < 4; ++t)
#pragma unroll
        for (int i = 0; i < 6; ++i) { acc[0][i] = mfma(wf[st][t][i], hf[st][t][0], acc[0][i]); acc[1][i] = mfma(wf[st][t][i], hf[st][t][1], acc[1][i]); }
    };
    (void)hf; (void)wf;
#pragma unroll 1
    for (int t0 = 0; t0 < 72; t0 += 4) { ld(0, t0); mm(0); }
    STAMP(64, 4);
    if (kh == 1) {
#pragma unroll
      for (int u = 0; u < 2; ++u)
#pragma unroll
        for (int i = 0; i < 6; ++i) *(f32x4*)(smem + ((tg * 12 + u * 6 + i) * 64 + lane) * 16) = acc[u][i];
    }
    __syncthreads();
    if (kh == 0) {
#pragma unroll
      for (int u = 0; u < 2; ++u)
#pragma unroll
        for (int b = 0; b < 3; ++b) {
          const f32x4 e0 = acc[u][2 * b] + *(const f32x4*)(smem + ((tg * 12 + u * 6 + 2 * b) * 64 + lane) * 16);
          const f32x4 e1 = acc[u][2 * b + 1] + *(const f32x4*)(smem + ((tg * 12 + u * 6 + 2 * b + 1) * 64 + lane) * 16);
          const u32x4 o = {pk(e0[0], e1[0]), pk(e0[1], e1[1]), pk(e0[2], e1[2]), pk(e0[3], e1[3])};
          *(u32x4*)(ws + FO_OFF + ((size_t)(tm * 128 + tg * 32 + u * 16 + c16) * DM + (b3 + b) * 32 + g * 8) * 2) = o;
        }
    }
  }
}

__global__ void __launch_bounds__(512, 2) k2_main(Args a) {
  extern __shared__ __attribute__((aligned(16))) char smem[];
  const int b = blockIdx.x, rr = b & 7, q = b >> 3;
  if (rr == 0) role_a(a, q, smem);
  else if (rr < 4 && q < 24) role_b(a, (rr - 1) * 24 + q, smem);
  else if (rr == 4) role_v(a, q, smem);
  else if (rr > 4) role_f(a, (rr - 5) * 32 + q, smem);
  else if (q >= 30) role_p(a, q & 1);
  DUMP();
}

__global__ void __launch_bounds__(256) k3_final(Args a) {
  const int i = blockIdx.x * 256 + threadIdx.x;
  const u32x4 x = ((const u32x4*)a.x)[i], p = ((const u32x4*)(a.ws + AO_OFF))[i], f = ((const u32x4*)(a.ws + FO_OFF))[i];
  u32x4 o;
#pragma unroll
  for (int k = 0; k < 4; ++k)
    o[k] = pk(bfr(bflo(x[k]) + bflo(p[k])) + bflo(f[k]), bfr(bfhi(x[k]) + bfhi(p[k])) + bfhi(f[k]));
  ((u32x4*)a.out)[i] = o;
}

extern "C" void kernel_launch(void* const* d_in, const int* in_sizes, int n_in, void* d_out, int out_size, void* d_ws,
                              size_t ws_size, hipStream_t stream) {
  Args a;
  a.x = (const u16*)d_in[0]; a.mem0 = (const u16*)d_in[1]; a.wq = (const u16*)d_in[2]; a.wk = (const u16*)d_in[3];
  a.wv = (const u16*)d_in[4]; a.wo = (const u16*)d_in[5]; a.wg = (const u16*)d_in[8]; a.wu = (const u16*)d_in[9];
  a.wd = (const u16*)d_in[10];
  a.ws = (unsigned char*)(((uintptr_t)d_ws + 65535) & ~(uintptr_t)65535);
  a.out = (u16*)d_out;
  (void)hipFuncSetAttribute((const void*)k2_main, hipFuncAttributeMaxDynamicSharedMemorySize, LDS_BYTES);
  k1_prep<<<K1_GRID, 576, 0, stream>>>(a);
  k2_main<<<256, 512, LDS_BYTES, stream>>>(a);
  k3_final<<<S_ * DM / 8 / 256, 256, 0, stream>>>(a);
}
```

```cpp
#include <hip/hip_runtime.h>
#include <hip/hip_bf16.h>

typedef unsigned u32;
typedef unsigned long long u64;
typedef unsigned short u16;
typedef __attribute__((ext_vector_type(8))) __bf16 bf16x8;
typedef __attribute__((ext_vector_type(2))) __bf16 bf16x2;
typedef __attribute__((ext_vector_type(4))) float f32x4;
typedef __attribute__((ext_vector_type(2))) float f32x2;
typedef __attribute__((ext_vector_type(4))) unsigned u32x4;
typedef __attribute__((ext_vector_type(2))) unsigned u32x2;

constexpr int S_ = 2048, DM = 576, NH = 32, DH = 128, FF = 4608, NSEG = 64, KS = 18;
constexpr int NB = 72, NF = 96;
constexpr u32 HO_OFF = 0;
constexpr u32 PT_OFF = HO_OFF + NSEG * 65536;
constexpr u32 PT_STEP = 4 * 20480;
constexpr u32 FLG_OFF = PT_OFF + NSEG * PT_STEP;
constexpr u32 FLM_OFF = FLG_OFF + NSEG * 128;
constexpr u32 FLP_OFF = FLM_OFF + NSEG * 3072;
constexpr u32 POISON_BYTES = FLP_OFF + 256;
constexpr u32 CTL_OFF = (POISON_BYTES + 4095) & ~4095u;
constexpr u32 M0_OFF = CTL_OFF + 4096;
constexpr u32 XF_OFF = M0_OFF + 12288;
constexpr u32 XF_SEG = 2 * KS * 1024;
constexpr u32 WVO_OFF = XF_OFF + NSEG * XF_SEG;
constexpr u32 WVO_J = 16 * KS * 1024;
constexpr u32 UX_OFF = WVO_OFF + NB * WVO_J;
constexpr u32 UM_OFF = UX_OFF + NSEG * NB * 16384;
constexpr u32 H_OFF = UM_OFF + NSEG * NB * 4096;
constexpr u32 WDF_OFF = H_OFF + 128 * 144 * 1024;
constexpr u32 FO_OFF = WDF_OFF + 9 * 144 * 4096;
constexpr u32 AO_OFF = FO_OFF + S_ * DM * 2;
constexpr u32 WS_END = AO_OFF + S_ * DM * 2;
constexpr u32 POISON = 0xFFFFFFFFu;
constexpr int SC1 = 16;
constexpr u32 SPIN_CAP = 400000u;
constexpr int LDS_BYTES = 160 * 1024;

__device__ __forceinline__ u32 pk(float lo, float hi) {
  f32x2 v = {lo, hi};
  return __builtin_bit_cast(u32, __builtin_convertvector(v, bf16x2));
}
__device__ __forceinline__ u32 pks(float lo, float hi) { u32 r = pk(lo, hi); return r == POISON ? 0x7FC07FC0u : r; }
__device__ __forceinline__ float bfr(float x) { return (float)(__bf16)x; }
__device__ __forceinline__ float bflo(u32 w) { return __uint_as_float(w << 16); }
__device__ __forceinline__ float bfhi(u32 w) { return __uint_as_float(w & 0xFFFF0000u); }
__device__ __forceinline__ f32x4 mfma(u32x4 a, u32x4 b, f32x4 c) {
  return __builtin_amdgcn_mfma_f32_16x16x32_bf16(__builtin_bit_cast(bf16x8, a), __builtin_bit_cast(bf16x8, b), c, 0, 0, 0);
}
__device__ __forceinline__ u32 plo(u32 a, u32 b) { return __builtin_amdgcn_perm(b, a, 0x05040100u); }
__device__ __forceinline__ u32 phi(u32 a, u32 b) { return __builtin_amdgcn_perm(b, a, 0x07060302u); }
__device__ __forceinline__ float vmax(float a, float b) { float r; asm("v_max_f32 %0, %1, %2" : "=v"(r) : "v"(a), "v"(b)); return r; }
__device__ __forceinline__ float vmax3(float a, float b, float c) { float r; asm("v_max3_f32 %0, %1, %2, %3" : "=v"(r) : "v"(a), "v"(b), "v"(c)); return r; }
__device__ __forceinline__ float xmax(float x) {
  auto s = __builtin_amdgcn_permlane16_swap(__float_as_uint(x), __float_as_uint(x), false, false);
  x = vmax(__uint_as_float(s[0]), __uint_as_float(s[1]));
  auto t = __builtin_amdgcn_permlane32_swap(__float_as_uint(x), __float_as_uint(x), false, false);
  return vmax(__uint_as_float(t[0]), __uint_as_float(t[1]));
}
__device__ __forceinline__ float xsum(float x) {
  auto s = __builtin_amdgcn_permlane16_swap(__float_as_uint(x), __float_as_uint(x), false, false);
  x = __uint_as_float(s[0]) + __uint_as_float(s[1]);
  auto t = __builtin_amdgcn_permlane32_swap(__float_as_uint(x), __float_as_uint(x), false, false);
  return __uint_as_float(t[0]) + __uint_as_float(t[1]);
}
__device__ __forceinline__ float ror8(float x) {
  return __builtin_bit_cast(float, __builtin_amdgcn_mov_dpp(__builtin_bit_cast(int, x), 0x128, 0xf, 0xf, true));
}
__device__ __forceinline__ u32 mx(u32 a, u32 b) { return a > b ? a : b; }
__device__ __forceinline__ u32 mx4(u32x4 v) { return mx(mx(v.x, v.y), mx(v.z, v.w)); }
__device__ __forceinline__ u32 mx2(u32x2 v) { return mx(v.x, v.y); }
__device__ __forceinline__ bool allok(u32 m) { return __all(m != POISON); }
__device__ __forceinline__ void glds16(const void* g, u32 lds) {
  u32 keep;
  asm volatile("s_mov_b32 %0, m0\n\ts_mov_b32 m0, %2\n\ts_nop 0\n\tglobal_load_lds_dwordx4 %1, off\n\ts_mov_b32 m0, %0"
               : "=&s"(keep) : "v"(g), "s"(lds) : "memory");
}
__device__ __forceinline__ void wait_vm() { asm volatile("s_waitcnt vmcnt(0)" ::: "memory"); }
__device__ __forceinline__ void bar() {
  asm volatile("s_waitcnt lgkmcnt(0)" ::: "memory");
  __builtin_amdgcn_s_barrier();
  asm volatile("" ::: "memory");
}
__device__ __forceinline__ bool spin_fail(u32& spins, u32* tmo) {
  ++spins;
  if (spins > SPIN_CAP) { __hip_atomic_store(tmo, 1u, __ATOMIC_RELAXED, __HIP_MEMORY_SCOPE_AGENT); return true; }
  if ((spins & 1023u) == 0u && __hip_atomic_load(tmo, __ATOMIC_RELAXED, __HIP_MEMORY_SCOPE_AGENT) != 0u) return true;
  return false;
}
__device__ __forceinline__ u32 homap(u32 lo, u32 flip) { return ((lo & ~8191u) << 1) + (lo & 8191u) + flip; }
constexpr u32 HO_PM = 16384, HO_M = 20480;

#define STAMP(step, k) do {} while (0)
#define STAMPC(step, k) do {} while (0)
#define DUMP() do {} while (0)
struct Args {
  const u16* x; const u16* mem0; const u16* wq; const u16* wk; const u16* wv; const u16* wo;
  const u16* wg; const u16* wu; const u16* wd; unsigned char* ws; u16* out;
};

constexpr int K1_WVO = 2592, K1_LN = 512, K1_JOBS = K1_WVO + K1_LN, K1_GRID = K1_JOBS / 4 + 8;
__global__ void __launch_bounds__(256) k1_prep(Args a) {
  const int lane = threadIdx.x & 63, wave = __builtin_amdgcn_readfirstlane(threadIdx.x >> 6);
  const int b = blockIdx.x;
  unsigned char* ws = a.ws;
  const int c16 = lane & 15, g = lane >> 4;
  if (b == K1_GRID - 1) {
    if (threadIdx.x == 0) { ((u32*)(ws + CTL_OFF))[64] = 0u; ((u32*)(ws + CTL_OFF))[128] = 0u; }
    for (int i = threadIdx.x; i < 576; i += 256) {
      u32x4 v = *(const u32x4*)(a.mem0 + (i & 7) * DM + (i >> 3) * 8);
      if (v.x == POISON) v.x = 0x7FC07FC0u; if (v.y == POISON) v.y = 0x7FC07FC0u;
      if (v.z == POISON) v.z = 0x7FC07FC0u; if (v.w == POISON) v.w = 0x7FC07FC0u;
      *(u32x4*)(ws + M0_OFF + i * 16) = v;
    }
    return;
  }
  if (b >= K1_JOBS / 4) return;
  const int job = b * 4 + wave;
  {
    const u32x4 pv = {POISON, POISON, POISON, POISON};
    for (u32 o = (job * 64 + lane) * 16; o < POISON_BYTES; o += K1_JOBS * 64 * 16) *(u32x4*)(ws + o) = pv;
  }
  if (job < K1_WVO) {
    const int jx = (b >> 3) * 4 + wave, h = (b & 7) * 4 + jx / 81, kp = (jx / 9) % 9, bb = jx % 9;
    u32x4 bf[4][4];
#pragma unroll
    for (int es = 0; es < 4; ++es) {
      u32x2 r[8];
#pragma unroll
      for (int jj = 0; jj < 8; ++jj)
        r[jj] = *(const u32x2*)(a.wo + (size_t)(h * DH + es * 32 + g * 8 + jj) * DM + bb * 64 + c16 * 4);
#pragma unroll
      for (int p = 0; p < 4; ++p) {
        bf[es][0][p] = plo(r[2 * p].x, r[2 * p + 1].x); bf[es][1][p] = phi(r[2 * p].x, r[2 * p + 1].x);
        bf[es][2][p] = plo(r[2 * p].y, r[2 * p + 1].y); bf[es][3][p] = phi(r[2 * p].y, r[2 * p + 1].y);
      }
    }
#pragma unroll
    for (int tt = 0; tt < 2; ++tt) {
      const int t = kp * 2 + tt;
      f32x4 acc[2][4];
#pragma unroll
      for (int rt = 0; rt < 2; ++rt) {
        const int k = t * 32 + (c16 >> 2) * 8 + (c16 & 3) + rt * 4;
#pragma unroll
        for (int i = 0; i < 4; ++i) acc[rt][i] = f32x4{0.f, 0.f, 0.f, 0.f};
#pragma unroll
        for (int es = 0; es < 4; ++es) {
          const u32x4 af = *(const u32x4*)(a.wv + (size_t)k * (NH * DH) + h * DH + es * 32 + g * 8);
#pragma unroll
          for (int i = 0; i < 4; ++i) acc[rt][i] = mfma(af, bf[es][i], acc[rt][i]);
        }
      }
      const int j = bb * 8 + (c16 >> 1);
      unsigned char* dst = ws + WVO_OFF + (size_t)j * WVO_J + ((h >> 1) * KS + t) * 1024 + (g * 16 + (h & 1) * 8 + (c16 & 1) * 4) * 16;
#pragma unroll
      for (int i = 0; i < 4; ++i) {
        u32x4 v = {pk(acc[0][i][0], acc[0][i][1]), pk(acc[0][i][2], acc[0][i][3]), pk(acc[1][i][0], acc[1][i][1]), pk(acc[1][i][2], acc[1][i][3])};
        *(u32x4*)(dst + i * 16) = v;
      }
    }
  } else {
    const int seg = (job - K1_WVO) >> 3;
#pragma unroll
    for (int rr = 0; rr < 4; ++rr) {
      const int row = ((job - K1_WVO) & 7) * 4 + rr;
      const bool act = lane < 36;
      u32x4 v[2] = {{0, 0, 0, 0}, {0, 0, 0, 0}};
      if (act) { const u32x4* p = (const u32x4*)(a.x + (size_t)(seg * 32 + row) * DM + lane * 16); v[0] = p[0]; v[1] = p[1]; }
      float f[16];
#pragma unroll
      for (int i = 0; i < 8; ++i) { f[2 * i] = bflo(v[i >> 2][i & 3]); f[2 * i + 1] = bfhi(v[i >> 2][i & 3]); }
      float s = 0.f;
#pragma unroll
      for (int i = 0; i < 16; ++i) s += f[i];
#pragma unroll
      for (int o = 1; o < 64; o <<= 1) s += __shfl_xor(s, o);
      const float mean = s * (1.f / DM);
      float q = 0.f;
#pragma unroll
      for (int i = 0; i < 16; ++i) { const float d = f[i] - mean; q += act ? d * d : 0.f; }
#pragma unroll
      for (int o = 1; o < 64; o <<= 1) q += __shfl_xor(q, o);
      const float mb = bfr(mean), den = bfr(sqrtf(bfr(bfr(q * (1.f / DM)) + 1e-5f)));
      if (act) {
#pragma unroll
        for (int c = 0; c < 2; ++c) {
          u32x4 o;
#pragma unroll
          for (int i = 0; i < 4; ++i) o[i] = pk(bfr(f[c * 8 + 2 * i] - mb) / den, bfr(f[c * 8 + 2 * i + 1] - mb) / den);
          const int ch = lane * 2 + c;
          *(u32x4*)(ws + XF_OFF + seg * XF_SEG + ((row >> 4) * KS + (ch >> 2)) * 1024 + ((ch & 3) * 16 + (row & 15)) * 16) = o;
        }
      }
    }
  }
}

constexpr u32 L_X = 0;
constexpr u32 L_M = 2 * XF_SEG;
constexpr u32 L_QM = L_M + 9216;
constexpr u32 L_QX = L_QM + 8192;

template <bool WA, int ST>
__device__ __forceinline__ void proj2(const u32x4 (&W0)[KS], const u32x4 (&W1)[KS], const char* xb, f32x4& c0, f32x4& c1) {
  u32x4 xf[KS];
#pragma unroll
  for (int t = 0; t < KS; ++t) xf[t] = *(const u32x4*)(xb + t * ST);
  c0 = f32x4{0.f, 0.f, 0.f, 0.f}; c1 = c0;
#pragma unroll
  for (int t = 0; t < KS; ++t) {
    c0 = WA ? mfma(W0[t], xf[t], c0) : mfma(xf[t], W0[t], c0);
    c1 = WA ? mfma(W1[t], xf[t], c1) : mfma(xf[t], W1[t], c1);
  }
  __builtin_amdgcn_sched_group_barrier(0x100, 6, 0);
#pragma unroll
  for (int t = 0; t < KS - 6; ++t) { __builtin_amdgcn_sched_group_barrier(0x008, 2, 0); __builtin_amdgcn_sched_group_barrier(0x100, 1, 0); }
  __builtin_amdgcn_sched_group_barrier(0x008, 12, 0);
}
__device__ __forceinline__ void stage_x(const unsigned char* ws, int seg, int wave, int lane) {
  const unsigned char* src = ws + XF_OFF + seg * XF_SEG + lane * 16;
  const u32 dst = L_X + (seg & 1) * XF_SEG;
#pragma unroll
  for (int i = 0; i < 5; ++i) {
    const int p = i * 8 + wave;
    if (p < 36) glds16(src + p * 1024, dst + p * 1024);
  }
}
__device__ __forceinline__ void stage_x5(const unsigned char* ws, int seg, int w5, int lane, int part) {
  const unsigned char* src = ws + XF_OFF + seg * XF_SEG + lane * 16;
  const u32 dst = L_X + (seg & 1) * XF_SEG;
#pragma unroll
  for (int i = 0; i < 4; ++i) {
    const int p = (part * 4 + i) * 5 + w5;
    if (p < 36) glds16(src + p * 1024, dst + p * 1024);
  }
}
__device__ __forceinline__ u32 get_flips(__amdgpu_buffer_rsrc_t r, int lane, u32* tmo) {
  u32 f, spins = 0;
  for (;;) {
    f = __builtin_amdgcn_raw_buffer_load_b32(r, FLP_OFF + lane * 4, 0, SC1);
    if (allok(f)) break;
    if (spin_fail(spins, tmo)) break;
    __builtin_amdgcn_s_sleep(8);
  }
  return f & 8192u;
}
__device__ __forceinline__ void gather_m(__amdgpu_buffer_rsrc_t r, u32 base, u32 flip, bool first, char* smem, int wave, int lane, u32* tmo) {
  const u32 l0 = (wave * 64 + lane) * 16, l1 = (512 + wave * 8 + (lane & 7)) * 16;
  const u32 o0 = first ? M0_OFF + l0 : base + homap(HO_M + l0, flip), o1 = first ? M0_OFF + l1 : base + homap(HO_M + l1, flip);
  u32 spins = 0;
  u32x4 v, w;
  for (;;) {
    v = __builtin_amdgcn_raw_buffer_load_b128(r, o0, 0, SC1);
    w = __builtin_amdgcn_raw_buffer_load_b128(r, o1, 0, SC1);
    if (allok(mx(mx4(v), mx4(w)))) break;
    if (spin_fail(spins, tmo)) break;
  }
  *(u32x4*)(smem + L_M + l0) = v;
  if (lane < 8) *(u32x4*)(smem + L_M + l1) = w;
  wait_vm();
}

__device__ __forceinline__ void role_a(const Args& a, int h, char* smem) {
  const int tid = threadIdx.x, lane = tid & 63, wave = __builtin_amdgcn_readfirstlane(tid >> 6);
  const int c16 = lane & 15, g = lane >> 4;
  unsigned char* ws = a.ws;
  u32* tmo = (u32*)(ws + CTL_OFF) + 64;
  __amdgpu_buffer_rsrc_t r = __builtin_amdgcn_make_buffer_rsrc(ws, 0, WS_END, 0x00020000);
  STAMP(64, 0);
  u32x4 W[2][KS];
  {
    u32x4 st[18];
    auto ldw = [&](int hf) {
#pragma unroll
      for (int i = 0; i < 18; ++i) {
        const int pc = i * 512 + tid;
        st[i] = *(const u32x4*)(((pc & 16) ? a.wk : a.wq) + (size_t)(hf * 288 + (pc >> 5)) * (NH * DH) + h * DH + (pc & 15) * 8);
      }
    };
    ldw(0);
#pragma unroll
    for (int hf = 0; hf < 2; ++hf) {
#pragma unroll
      for (int i = 0; i < 18; ++i) { const int pc = i * 512 + tid; *(u32x4*)(smem + (pc >> 5) * 528 + (pc & 31) * 16) = st[i]; }
      if (hf == 0) ldw(1);
      bar();
      const char* wl = smem + g * 8 * 528 + wave * 64 + c16 * 4;
#pragma unroll
      for (int t = 0; t < 9; ++t) {
#pragma unroll
        for (int p = 0; p < 4; ++p) {
          const u32 d0 = *(const u32*)(wl + (t * 32 + 2 * p) * 528), d1 = *(const u32*)(wl + (t * 32 + 2 * p + 1) * 528);
          W[0][hf * 9 + t][p] = plo(d0, d1); W[1][hf * 9 + t][p] = phi(d0, d1);
        }
        asm volatile("" : "+v"(W[0][hf * 9 + t]), "+v"(W[1][hf * 9 + t]) :: "memory");
      }
      bar();
    }
  }
  stage_x(ws, 0, wave, lane);
  stage_x(ws, 1, wave, lane);
  const u32 flips = get_flips(r, lane, tmo);
  wait_vm();
  bar();
  const u32 qsel = (wave >> 2) * 8192 + (wave & 3) * 1024 + lane * 16;
  auto proj_x = [&](int seg, int rt) {
    f32x4 c0, c1;
    proj2<true, 1024>(W[0], W[1], smem + L_X + (seg & 1) * XF_SEG + rt * KS * 1024 + lane * 16, c0, c1);
    u32x4 o = {pk(c0[0], c1[0]), pk(c0[1], c1[1]), pk(c0[2], c1[2]), pk(c0[3], c1[3])};
    *(u32x4*)(smem + L_QX + (seg & 1) * 16384 + qsel + rt * 4096) = o;
  };
  const u32 lx = (((h >> 1) * 4 + g) * 16 + (h & 1) * 8 + (lane & 7)) * 16, lm = HO_PM + h * 128 + g * 64 + (lane & 7) * 8;
  STAMP(64, 1); STAMPC(65, 0); STAMP(65, 1);
  proj_x(0, 0);
  proj_x(0, 1);
  for (int s = 0; s < NSEG; ++s) {
    const u32 flip = __builtin_amdgcn_readlane(flips, s), base = HO_OFF + s * 65536;
    STAMP(s, 0);
    gather_m(r, base, flip, s == 0, smem, wave, lane, tmo);
    STAMP(s, 1);
    bar();
    STAMP(s, 2);
    {
      f32x4 c0, c1;
      proj2<true, 512>(W[0], W[1], smem + L_M + g * 128 + (lane & 7) * 16, c0, c1);
      u32x4 o = {pk(c0[0], c1[0]), pk(c0[1], c1[1]), pk(c0[2], c1[2]), pk(c0[3], c1[3])};
      *(u32x4*)(smem + L_QM + wave * 1024 + lane * 16) = o;
    }
    u32x4 kf[8];
    if (wave == 0) {
      __builtin_amdgcn_s_setprio(3);
#pragma unroll
      for (int w = 0; w < 8; ++w) kf[w] = *(const u32x4*)(smem + L_QX + (s & 1) * 16384 + 8192 + lane * 16 + w * 1024);
    }
    bar();
    STAMP(s, 3);
    const float C2 = 0.08838834764831845f * 1.4426950408889634f;
    if (wave != 0) __builtin_amdgcn_s_sleep(3);
    if (wave == 0) {
      if (s < NSEG - 1) {
        const char* qb = smem + L_QM + lane * 16;
        f32x4 s0 = {0.f, 0.f, 0.f, 0.f}, s1 = s0, s2 = s0;
#pragma unroll
        for (int w = 0; w < 4; ++w) {
          const u32x4 qf = *(const u32x4*)(qb + w * 1024);
          s2 = mfma(*(const u32x4*)(qb + 4096 + w * 1024), qf, s2);
          s0 = mfma(kf[w], qf, s0);
          s1 = mfma(kf[4 + w], qf, s1);
        }
        const bool hi = c16 >= 8;
        float v[6];
#pragma unroll
        for (int i = 0; i < 4; ++i) v[i] = hi ? s1[i] : s0[i];
        v[4] = g < 2 ? (hi ? s2[2] : s2[0]) : -INFINITY;
        v[5] = g < 2 ? (hi ? s2[3] : s2[1]) : -INFINITY;
        float m = vmax(vmax3(v[0], v[1], v[2]), vmax3(v[3], v[4], v[5]));
        m = xmax(m);
        m = vmax(m, ror8(m)) * C2;
#pragma unroll
        for (int i = 0; i < 6; ++i) v[i] = __builtin_amdgcn_exp2f(__builtin_fmaf(v[i], C2, -m));
        float sum = ((v[0] + v[1]) + (v[2] + v[3])) + (v[4] + v[5]);
        sum += ror8(sum);
        const float inv = __builtin_amdgcn_rcpf(xsum(sum));
        const u32x2 px = {pk(v[0] * inv, v[1] * inv) & 0x7FFF7FFFu, pk(v[2] * inv, v[3] * inv) & 0x7FFF7FFFu};
        const u32 pm = pk(v[4] * inv, v[5] * inv) & 0x7FFF7FFFu;
        __builtin_amdgcn_raw_buffer_store_b64(px, r, base + homap(lx + (c16 >> 3) * 8, flip), 0, SC1);
        if (g < 2) __builtin_amdgcn_raw_buffer_store_b32(pm, r, base + homap(lm + (c16 >> 3) * 4, flip), 0, SC1);
      }
      __builtin_amdgcn_s_setprio(0);
    } else if (wave < 3) {
      const char* qx = smem + L_QX + (s & 1) * 16384 + lane * 16;
      f32x4 s0 = {0.f, 0.f, 0.f, 0.f}, s1 = s0, s2 = s0;
#pragma unroll
      for (int w = 0; w < 4; ++w) {
        const u32x4 qf = *(const u32x4*)(qx + (wave - 1) * 4096 + w * 1024);
        s2 = mfma(*(const u32x4*)(smem + L_QM + 4096 + lane * 16 + w * 1024), qf, s2);
        s0 = mfma(*(const u32x4*)(qx + 8192 + w * 1024), qf, s0);
        s1 = mfma(*(const u32x4*)(qx + 12288 + w * 1024), qf, s1);
      }
      const int qi = (wave - 1) * 16 + c16;
      const float NEG = -1.0e9f * 1.4426950408889634f;
      float v[12];
#pragma unroll
      for (int i = 0; i < 4; ++i) {
        v[i] = (4 * g + i <= qi) ? s0[i] * C2 : NEG;
        v[4 + i] = (16 + 4 * g + i <= qi) ? s1[i] * C2 : NEG;
        v[8 + i] = g < 2 ? s2[i] * C2 : -INFINITY;
      }
      float m = v[0];
#pragma unroll
      for (int i = 1; i < 12; ++i) m = fmaxf(m, v[i]);
      m = xmax(m);
      float sum = 0.f;
#pragma unroll
      for (int i = 0; i < 12; ++i) { v[i] = __builtin_amdgcn_exp2f(v[i] - m); sum += v[i]; }
      const float inv = __builtin_amdgcn_rcpf(xsum(sum));
      u32x4 px; u32x2 pm;
#pragma unroll
      for (int i = 0; i < 4; ++i) px[i] = pk(v[2 * i] * inv, v[2 * i + 1] * inv) & 0x7FFF7FFFu;
#pragma unroll
      for (int i = 0; i < 2; ++i) pm[i] = pk(v[8 + 2 * i] * inv, v[9 + 2 * i] * inv) & 0x7FFF7FFFu;
      const u32 tb = PT_OFF + s * PT_STEP + ((wave - 1) * 2 + (c16 >> 3)) * 20480;
      __builtin_amdgcn_raw_buffer_store_b128(px, r, tb + lx, 0, SC1);
      if (g < 2) __builtin_amdgcn_raw_buffer_store_b64(pm, r, tb + lm, 0, SC1);
    }
    STAMP(s, 4);
    if (s + 1 < NSEG) {
      if (wave == 4) __builtin_amdgcn_s_sleep(5);
      proj_x(s + 1, 0);
      if (wave >= 3 && s + 2 < NSEG) { stage_x5(ws, s + 2, wave - 3, lane, 0); stage_x5(ws, s + 2, wave - 3, lane, 1); }
      proj_x(s + 1, 1);
    }
  }
  STAMPC(65, 2); STAMP(65, 3);
}

__device__ __forceinline__ void role_b(const Args& a, int j, char* smem) {
  const int tid = threadIdx.x, lane = tid & 63, wave = __builtin_amdgcn_readfirstlane(tid >> 6);
  const int c16 = lane & 15, g = lane >> 4;
  unsigned char* ws = a.ws;
  u32* tmo = (u32*)(ws + CTL_OFF) + 64;
  __amdgpu_buffer_rsrc_t r = __builtin_amdgcn_make_buffer_rsrc(ws, 0, WS_END, 0x00020000);
  STAMP(64, 0);
  u32x4 W[2][KS];
  {
    const unsigned char* wsrc = ws + WVO_OFF + (size_t)j * WVO_J + wave * 2 * KS * 1024 + lane * 16;
#pragma unroll
    for (int tt = 0; tt < 2; ++tt)
#pragma unroll
      for (int t = 0; t < KS; ++t) W[tt][t] = *(const u32x4*)(wsrc + (tt * KS + t) * 1024);
  }
  wait_vm();
  bar();
  const u32 flips = get_flips(r, lane, tmo);
  STAMP(64, 1);
  u32x4 nx0, nx1;
  u32 fl = POISON;
  auto ux_load = [&](int seg) {
    const u32 fo = FLG_OFF + seg * 128 + wave * 16 + (lane & 3) * 4;
    u32 spins = 0;
    while (!allok(fl)) {
      fl = __builtin_amdgcn_raw_buffer_load_b32(r, fo, 0, SC1);
      if (spin_fail(spins, tmo)) break;
    }
    asm volatile("" ::: "memory");
    const u32 uo = UX_OFF + ((seg * NB + j) * 16 + wave * 2) * 1024 + lane * 16;
    nx0 = __builtin_amdgcn_raw_buffer_load_b128(r, uo, 0, SC1);
    nx1 = __builtin_amdgcn_raw_buffer_load_b128(r, uo + 1024, 0, SC1);
    fl = seg + 1 < NSEG ? __builtin_amdgcn_raw_buffer_load_b32(r, fo + 128, 0, SC1) : 0u;
  };
  const u32 lx = wave * 2048 + lane * 16;
  const u32 lm = HO_PM + (wave * 4 + (c16 >> 3)) * 128 + (g & 1) * 64 + (lane & 7) * 8;
  const bool diag = (c16 >> 3) == (g >> 1);
  const u32 redo = ((wave * 2 + (g >> 1)) * 64 + (lane & 7) * 8 + (g & 1) * 4) * 4;
  for (int s = 0; s < NSEG; ++s) {
    const u32 flip = __builtin_amdgcn_readlane(flips, s), base = HO_OFF + s * 65536;
    STAMP(s, 0);
    gather_m(r, base, flip, s == 0, smem, wave, lane, tmo);
    bar();
    STAMP(s, 1);
    u32x4 um;
    {
      f32x4 c0, c1;
      proj2<false, 512>(W[0], W[1], smem + L_M + g * 128 + (lane & 7) * 16, c0, c1);
      um = u32x4{pk(c0[0], c0[1]), pk(c0[2], c0[3]), pk(c1[0], c1[1]), pk(c1[2], c1[3])};
      if (g < 2) __builtin_amdgcn_raw_buffer_store_b128(um, r, UM_OFF + ((s * NB + j) * 8 + wave) * 512 + lane * 16, 0, SC1);
      else um = u32x4{0, 0, 0, 0};
    }
    if (s + 1 < NSEG) {
      if (s == 0) ux_load(0);
      STAMP(s, 2);
      const u32 o0 = base + homap(lx, flip), o1 = o0 + 1024, o2 = base + homap(lm, flip), o3 = o2 + 256;
      u32x4 p0, p1; u32x2 q0, q1;
      u32 spins = 0;
      for (;;) {
        p0 = __builtin_amdgcn_raw_buffer_load_b128(r, o0, 0, SC1);
        p1 = __builtin_amdgcn_raw_buffer_load_b128(r, o1, 0, SC1);
        q0 = __builtin_amdgcn_raw_buffer_load_b64(r, o2, 0, SC1);
        q1 = __builtin_amdgcn_raw_buffer_load_b64(r, o3, 0, SC1);
        if (allok(mx(mx(mx4(p0), mx4(p1)), mx(mx2(q0), mx2(q1))))) break;
        if (spin_fail(spins, tmo)) break;
      }
      STAMP(s, 3);
      f32x4 d = {0.f, 0.f, 0.f, 0.f}, e = d;
      const u32x4 pmv = {q0.x, q0.y, q1.x, q1.y};
      d = mfma(nx0, p0, d); e = mfma(nx1, p1, e); d = mfma(um, pmv, d);
      d += e;
      if (diag) *(f32x4*)(smem + L_QM + redo) = d;
    }
    wait_vm();
    bar();
    if (wave == 0 && s + 1 < NSEG) {
      const char* rp = smem + L_QM + (lane >> 5) * 2048 + (lane & 31) * 8;
      f32x2 acc = *(const f32x2*)rp;
#pragma unroll
      for (int p = 1; p < 8; ++p) acc += *(const f32x2*)(rp + p * 256);
      auto t0 = __builtin_amdgcn_permlane32_swap(__float_as_uint(acc.x), __float_as_uint(acc.x), false, false);
      auto t1 = __builtin_amdgcn_permlane32_swap(__float_as_uint(acc.y), __float_as_uint(acc.y), false, false);
      const u32 o = pks(__uint_as_float(t0[0]) + __uint_as_float(t0[1]), __uint_as_float(t1[0]) + __uint_as_float(t1[1]));
      const u32 nflip = __builtin_amdgcn_readlane(flips, s + 1);
      if (lane < 32) __builtin_amdgcn_raw_buffer_store_b32(o, r, base + 65536 + homap(HO_M + j * 128 + lane * 4, nflip), 0, SC1);
    }
    STAMP(s, 4);
    if (tid == 64) __builtin_amdgcn_raw_buffer_store_b32(1u, r, FLM_OFF + s * 3072 + (j / 6) * 128 + (j % 6) * 4, 0, SC1);
    if (s + 2 < NSEG) ux_load(s + 1);
    STAMP(s, 5);
  }
  STAMP(65, 0);
}

__device__ __forceinline__ void role_o(const Args& a, int o24, char* smem) {
  const int tid = threadIdx.x, lane = tid & 63, wave = __builtin_amdgcn_readfirstlane(tid >> 6);
  const int c16 = lane & 15, g = lane >> 4, o = o24 >> 1;
  unsigned char* ws = a.ws;
  u32* tmo = (u32*)(ws + CTL_OFF) + 64;
  __amdgpu_buffer_rsrc_t r = __builtin_amdgcn_make_buffer_rsrc(ws, 0, WS_END, 0x00020000);
  STAMP(64, 0);
  if (wave < 3 && o24 * 3 + wave < NSEG) {
    const int blk = o24 * 3 + wave;
    u32 best[2] = {~0u, ~0u};
    for (int rep = 0; rep < 4; ++rep)
      for (int f = 0; f < 2; ++f) {
        u32* p = (u32*)(ws + HO_OFF + blk * 65536 + homap(32768 - 128 * (rep + 1), f * 8192));
        __hip_atomic_store(p, POISON, __ATOMIC_RELAXED, __HIP_MEMORY_SCOPE_AGENT);
        wait_vm();
        u64 t0, t1;
        asm volatile("s_memtime %0\n\ts_waitcnt lgkmcnt(0)" : "=s"(t0) :: "memory");
        u32 v = __hip_atomic_load(p, __ATOMIC_RELAXED, __HIP_MEMORY_SCOPE_AGENT);
        asm volatile("s_waitcnt vmcnt(0)" : "+v"(v) :: "memory");
        asm volatile("s_memtime %0\n\ts_waitcnt lgkmcnt(0)" : "=s"(t1) :: "memory");
        const u32 d = (u32)(t1 - t0);
        best[f] = d < best[f] ? d : best[f];
      }
    if (lane == 0) __builtin_amdgcn_raw_buffer_store_b32(best[1] < best[0] ? 8192u : 0u, r, FLP_OFF + blk * 4, 0, SC1);
  }
  STAMP(64, 1);
  const u32 lx = wave * 2048 + lane * 16;
  const u32 lm = HO_PM + (wave * 4 + (c16 >> 3)) * 128 + (g & 1) * 64 + (lane & 7) * 8;
  const bool diag = (c16 >> 3) == (g >> 1);
  const u32 redo = ((wave * 2 + (g >> 1)) * 64 + (lane & 7) * 8 + (g & 1) * 4) * 4;
  for (int s = o24 & 1; s < NSEG; s += 2) {
    u32 spins = 0;
    while (!allok(__builtin_amdgcn_raw_buffer_load_b32(r, FLG_OFF + s * 128 + wave * 16 + (lane & 3) * 4, 0, SC1))) {
      if (spin_fail(spins, tmo)) break;
      __builtin_amdgcn_s_sleep(16);
    }
    asm volatile("" ::: "memory");
    u32x4 x0[6], x1[6], um[6];
#pragma unroll
    for (int c = 0; c < 6; ++c) {
      const u32 uo = UX_OFF + ((s * NB + o * 6 + c) * 16 + wave * 2) * 1024 + lane * 16;
      x0[c] = __builtin_amdgcn_raw_buffer_load_b128(r, uo, 0, SC1);
      x1[c] = __builtin_amdgcn_raw_buffer_load_b128(r, uo + 1024, 0, SC1);
    }
    if (wave == 0) {
      while (!allok(__builtin_amdgcn_raw_buffer_load_b32(r, FLM_OFF + s * 3072 + o * 128 + (lane % 6) * 4, 0, SC1))) {
        if (spin_fail(spins, tmo)) break;
        __builtin_amdgcn_s_sleep(4);
      }
    }
    bar();
#pragma unroll
    for (int c = 0; c < 6; ++c)
      um[c] = __builtin_amdgcn_raw_buffer_load_b128(r, UM_OFF + ((s * NB + o * 6 + c) * 8 + wave) * 512 + (lane & 31) * 16, 0, SC1);
    u32x4 p0[4], p1[4]; u32x2 q0[4], q1[4];
    for (;;) {
      u32 m = 0;
#pragma unroll
      for (int u = 0; u < 4; ++u) {
        const u32 tb = PT_OFF + s * PT_STEP + u * 20480;
        p0[u] = __builtin_amdgcn_raw_buffer_load_b128(r, tb + lx, 0, SC1);
        p1[u] = __builtin_amdgcn_raw_buffer_load_b128(r, tb + lx + 1024, 0, SC1);
        q0[u] = __builtin_amdgcn_raw_buffer_load_b64(r, tb + lm, 0, SC1);
        q1[u] = __builtin_amdgcn_raw_buffer_load_b64(r, tb + lm + 256, 0, SC1);
      }
#pragma unroll
      for (int u = 0; u < 4; ++u) m = mx(m, mx(mx(mx4(p0[u]), mx4(p1[u])), mx(mx2(q0[u]), mx2(q1[u]))));
      if (allok(m)) break;
      if (spin_fail(spins, tmo)) break;
    }
#pragma unroll
    for (int c = 0; c < 6; ++c) {
      if (g >= 2) um[c] = u32x4{0, 0, 0, 0};
#pragma unroll
      for (int u = 0; u < 4; ++u) {
        f32x4 d = {0.f, 0.f, 0.f, 0.f};
        const u32x4 pmv = {q0[u].x, q0[u].y, q1[u].x, q1[u].y};
        d = mfma(x0[c], p0[u], d); d = mfma(x1[c], p1[u], d); d = mfma(um[c], pmv, d);
        if (diag) *(f32x4*)(smem + (c * 4 + u) * 4096 + redo) = d;
      }
    }
    bar();
    if (tid < 384) {
      const char* rp = smem + (tid >> 4) * 4096 + (tid & 15) * 16;
      f32x4 acc = *(const f32x4*)rp;
#pragma unroll
      for (int p = 1; p < 16; ++p) acc += *(const f32x4*)(rp + p * 256);
      *(u32x2*)(ws + AO_OFF + ((size_t)(s * 32 + ((tid & 63) >> 1)) * DM + (o * 6 + (tid >> 6)) * 8 + (tid & 1) * 4) * 2) = u32x2{pk(acc[0], acc[1]), pk(acc[2], acc[3])};
    }
    STAMP(s, 0);
  }
}

__device__ __forceinline__ void role_v(const Args& a, int h, char* smem) {
  const int tid = threadIdx.x, lane = tid & 63, wave = __builtin_amdgcn_readfirstlane(tid >> 6);
  const int c16 = lane & 15, g = lane >> 4;
  unsigned char* ws = a.ws;
  __amdgpu_buffer_rsrc_t r = __builtin_amdgcn_make_buffer_rsrc(ws, 0, WS_END, 0x00020000);
  STAMP(64, 0);
  u32x4 WV[KS], WO[5][4];
  {
    const u32 sel = (c16 & 1) ? 0x07060302u : 0x05040100u;
    u32x4 st[18];
#pragma unroll
    for (int i = 0; i < 18; ++i) { const int pc = i * 512 + tid; st[i] = *(const u32x4*)(a.wv + (size_t)(pc >> 4) * (NH * DH) + h * DH + (pc & 15) * 8); }
#pragma unroll
    for (int i = 0; i < 18; ++i) { const int pc = i * 512 + tid; *(u32x4*)(smem + (pc >> 4) * 272 + (pc & 15) * 16) = st[i]; }
#pragma unroll
    for (int i = 0; i < 18; ++i) st[i] = *(const u32x4*)(a.wo + (size_t)h * DH * DM + (i * 512 + tid) * 8);
    bar();
    {
      const char* wl = smem + g * 8 * 272 + wave * 32 + (c16 >> 1) * 4;
#pragma unroll
      for (int t = 0; t < KS; ++t) {
#pragma unroll
        for (int p = 0; p < 4; ++p)
          WV[t][p] = __builtin_amdgcn_perm(*(const u32*)(wl + (t * 32 + 2 * p + 1) * 272), *(const u32*)(wl + (t * 32 + 2 * p) * 272), sel);
        asm volatile("" : "+v"(WV[t]) :: "memory");
      }
    }
    bar();
#pragma unroll
    for (int i = 0; i < 18; ++i) { const int pc = i * 512 + tid; *(u32x4*)(smem + (pc / 72) * 1168 + (pc % 72) * 16) = st[i]; }
    bar();
#pragma unroll
    for (int i = 0; i < 5; ++i) {
      const int ct = wave + 8 * i < 36 ? wave + 8 * i : 0;
      const char* wl = smem + g * 4 * 1168 + ct * 32 + (c16 >> 1) * 4;
#pragma unroll
      for (int es = 0; es < 4; ++es) {
#pragma unroll
        for (int p = 0; p < 4; ++p) {
          const int e0 = 32 * es + 16 * (p >> 1) + 2 * (p & 1);
          WO[i][es][p] = __builtin_amdgcn_perm(*(const u32*)(wl + (e0 + 1) * 1168), *(const u32*)(wl + e0 * 1168), sel);
        }
        asm volatile("" : "+v"(WO[i][es]) :: "memory");
      }
    }
    bar();
  }
  STAMP(64, 1);
  stage_x(ws, 0, wave, lane);
  for (int sg = 0; sg < NSEG; ++sg) {
    wait_vm();
    bar();
    if (sg > 0 && tid == 0) __builtin_amdgcn_raw_buffer_store_b32(1u, r, FLG_OFF + (sg - 1) * 128 + h * 4, 0, SC1);
    if (sg + 1 < NSEG) stage_x(ws, sg + 1, wave, lane);
    {
      const char* xb = smem + L_X + (sg & 1) * XF_SEG + lane * 16;
      f32x4 c0 = {0.f, 0.f, 0.f, 0.f}, c1 = c0;
#pragma unroll
      for (int t = 0; t < KS; ++t) {
        c0 = mfma(WV[t], *(const u32x4*)(xb + t * 1024), c0);
        c1 = mfma(WV[t], *(const u32x4*)(xb + (KS + t) * 1024), c1);
      }
      __builtin_amdgcn_sched_group_barrier(0x100, 8, 0);
#pragma unroll
      for (int t = 0; t < KS - 4; ++t) { __builtin_amdgcn_sched_group_barrier(0x008, 2, 0); __builtin_amdgcn_sched_group_barrier(0x100, 2, 0); }
      __builtin_amdgcn_sched_group_barrier(0x008, 8, 0);
      char* vt = smem + L_QM + (wave >> 1) * 1024 + lane * 16 + (wave & 1) * 8;
      *(u32x2*)vt = u32x2{pk(c0[0], c0[1]), pk(c0[2], c0[3])};
      *(u32x2*)(vt + 4096) = u32x2{pk(c1[0], c1[1]), pk(c1[2], c1[3])};
    }
    bar();
    u32x4 vf[2][4];
#pragma unroll
    for (int rt = 0; rt < 2; ++rt)
#pragma unroll
      for (int es = 0; es < 4; ++es) vf[rt][es] = *(const u32x4*)(smem + L_QM + rt * 4096 + es * 1024 + lane * 16);
#pragma unroll
    for (int i = 0; i < 5; ++i) {
      const int ct = wave + 8 * i;
      if (ct < 36) {
        f32x4 d0 = {0.f, 0.f, 0.f, 0.f}, d1 = d0;
#pragma unroll
        for (int es = 0; es < 4; ++es) { d0 = mfma(vf[0][es], WO[i][es], d0); d1 = mfma(vf[1][es], WO[i][es], d1); }
        const u32x4 o = {pk(d0[0], d0[1]), pk(d0[2], d0[3]), pk(d1[0], d1[1]), pk(d1[2], d1[3])};
        __builtin_amdgcn_raw_buffer_store_b128(o, r, UX_OFF + ((sg * NB + ct * 2 + (c16 >> 3)) * 16 + (h >> 1)) * 1024 + (g * 16 + (h & 1) * 8 + (lane & 7)) * 16, 0, SC1);
      }
    }
    if (sg == 0) STAMP(64, 2);
  }
  wait_vm();
  bar();
  if (tid == 0) __builtin_amdgcn_raw_buffer_store_b32(1u, r, FLG_OFF + (NSEG - 1) * 128 + h * 4, 0, SC1);
  STAMP(64, 3);
}

__device__ __forceinline__ float gelu_tanh(float x) {
  const float u = 0.7978845608028654f * (x + 0.044715f * x * x * x);
  const float e = __expf(2.f * u);
  return 0.5f * x * (2.f - 2.f / (e + 1.f));
}

__device__ __forceinline__ void role_f(const Args& a, int f, char* smem) {
  const int tid = threadIdx.x, lane = tid & 63, wave = __builtin_amdgcn_readfirstlane(tid >> 6);
  const int c16 = lane & 15, g = lane >> 4;
  unsigned char* ws = a.ws;
  u32* tmo = (u32*)(ws + CTL_OFF) + 64;
  u32* cnt = (u32*)(ws + CTL_OFF) + 128;
  __amdgpu_buffer_rsrc_t r = __builtin_amdgcn_make_buffer_rsrc(ws, 0, WS_END, 0x00020000);
  STAMP(64, 0);
  for (int it = f * 27 + wave; it < f * 27 + 27; it += 8) {
    const int ks = it / 18, bb = it % 18;
    u32 d[8];
#pragma unroll
    for (int jj = 0; jj < 8; ++jj)
      d[jj] = *(const u32*)(a.wd + (size_t)(ks * 32 + (jj >> 2) * 16 + g * 4 + (jj & 3)) * DM + bb * 32 + c16 * 2);
    const u32x4 lo = {plo(d[0], d[1]), plo(d[2], d[3]), plo(d[4], d[5]), plo(d[6], d[7])};
    const u32x4 hi = {phi(d[0], d[1]), phi(d[2], d[3]), phi(d[4], d[5]), phi(d[6], d[7])};
    __builtin_amdgcn_raw_buffer_store_b128(lo, r, WDF_OFF + (bb * 144 + ks) * 2048 + lane * 16, 0, SC1);
    __builtin_amdgcn_raw_buffer_store_b128(hi, r, WDF_OFF + (bb * 144 + ks) * 2048 + 1024 + lane * 16, 0, SC1);
  }
#pragma unroll 1
  for (int job = 0; job < 2; ++job) {
    int tid = threadIdx.x;
    asm volatile("" : "+v"(tid));
    const int hv = wave & 3, rt = wave >> 2;
    const int u0 = f * 48, sl = (u0 >> 6) + job;
    const int q0 = job == 0 ? (u0 & 63) : 0, q1 = min(u0 + 48 - sl * 64, 64);
    if (q1 <= q0) break;
    u32x4 WW[2][KS];
#pragma unroll
    for (int mat = 0; mat < 2; ++mat) {
      __syncthreads();
      u32x4 st[9];
#pragma unroll
      for (int i = 0; i < 9; ++i) {
        const int pc = i * 512 + tid;
        st[i] = *(const u32x4*)((mat ? a.wu : a.wg) + (size_t)(pc >> 3) * FF + sl * 64 + (pc & 7) * 8);
      }
#pragma unroll
      for (int i = 0; i < 9; ++i) { const int pc = i * 512 + tid; *(u32x4*)(smem + (pc >> 3) * 144 + (pc & 7) * 16) = st[i]; }
      __syncthreads();
      const char* wl = smem + g * 8 * 144 + hv * 32 + (c16 >> 1) * 4;
      const u32 sel = (c16 & 1) ? 0x07060302u : 0x05040100u;
#pragma unroll
      for (int t = 0; t < KS; ++t) {
#pragma unroll
        for (int p = 0; p < 4; ++p) {
          const int o = (t * 32 + 2 * p) * 144;
          WW[mat][t][p] = __builtin_amdgcn_perm(*(const u32*)(wl + o + 144), *(const u32*)(wl + o), sel);
        }
        asm volatile("" : "+v"(WW[mat][t]) :: "memory");
      }
    }
    __syncthreads();
    if (job == 0) STAMP(64, 1);
    auto stage = [&](int sg) {
#pragma unroll
      for (int i = 0; i < 5; ++i) {
        const int p = min(i * 8 + wave, 35);
        glds16(ws + XF_OFF + sg * XF_SEG + p * 1024 + lane * 16, (sg & 3) * XF_SEG + p * 1024);
      }
    };
    stage(q0); stage(min(q0 + 1, q1 - 1));
    for (int sg = q0; sg < q1; ++sg) {
      asm volatile("s_waitcnt vmcnt(5)" ::: "memory");
      bar();
      stage(min(sg + 2, q1 - 1));
      f32x4 cg, cu;
      proj2<true, 1024>(WW[0], WW[1], smem + (sg & 3) * XF_SEG + rt * KS * 1024 + lane * 16, cg, cu);
      float hh[4];
#pragma unroll
      for (int i = 0; i < 4; ++i) hh[i] = bfr(gelu_tanh(bfr(cg[i]))) * bfr(cu[i]);
      const u32x2 o = {pk(hh[0], hh[1]), pk(hh[2], hh[3])};
      __builtin_amdgcn_raw_buffer_store_b64(o, r, H_OFF + ((sg * 2 + rt) * 144 + sl * 2 + (hv >> 1)) * 1024 + lane * 16 + (hv & 1) * 8, 0, SC1);
    }
    wait_vm();
  }
  wait_vm();
  __syncthreads();
  STAMP(64, 2);
  if (tid == 0) {
    __hip_atomic_fetch_add(cnt, 1u, __ATOMIC_RELAXED, __HIP_MEMORY_SCOPE_AGENT);
    u32 spins = 0;
    while (__hip_atomic_load(cnt, __ATOMIC_RELAXED, __HIP_MEMORY_SCOPE_AGENT) < (u32)NF) {
      __builtin_amdgcn_s_sleep(8);
      if (spin_fail(spins, tmo)) break;
    }
    __builtin_amdgcn_fence(__ATOMIC_ACQUIRE, "agent");
    wait_vm();
  }
  __syncthreads();
  STAMP(64, 3);
  {
    const int tm = f / 6, b3 = (f % 6) * 3, tg = wave & 3, kh = wave >> 2;
    const unsigned char* hb = ws + H_OFF + ((size_t)(tm * 8 + tg * 2) * 144 + kh * 72) * 1024 + lane * 16;
    const unsigned char* wb = ws + WDF_OFF + (size_t)(b3 * 144 + kh * 72) * 2048 + lane * 16;
    f32x4 acc[2][6];
#pragma unroll
    for (int u = 0; u < 2; ++u)
#pragma unroll
      for (int i = 0; i < 6; ++i) acc[u][i] = f32x4{0.f, 0.f, 0.f, 0.f};
    u32x4 hf[2][2][2], wf[2][2][6];
    auto ld = [&](int st, int t0) {
#pragma unroll
      for (int t = 0; t < 2; ++t) {
        hf[st][t][0] = *(const u32x4*)(hb + (t0 + t) * 1024); hf[st][t][1] = *(const u32x4*)(hb + (144 + t0 + t) * 1024);
#pragma unroll
        for (int i = 0; i < 6; ++i) wf[st][t][i] = *(const u32x4*)(wb + ((i >> 1) * 144 + t0 + t) * 2048 + (i & 1) * 1024);
      }
    };
    auto mm = [&](int st) {
#pragma unroll
      for (int t = 0; t < 2; ++t)
#pragma unroll
        for (int i = 0; i < 6; ++i) { acc[0][i] = mfma(wf[st][t][i], hf[st][t][0], acc[0][i]); acc[1][i] = mfma(wf[st][t][i], hf[st][t][1], acc[1][i]); }
    };
    ld(0, 0);
#pragma unroll 1
    for (int t0 = 0; t0 < 72; t0 += 4) {
      ld(1, t0 + 2);
      __builtin_amdgcn_sched_barrier(0);
      mm(0);
      __builtin_amdgcn_sched_barrier(0);
      ld(0, min(t0 + 4, 70));
      __builtin_amdgcn_sched_barrier(0);
      mm(1);
      __builtin_amdgcn_sched_barrier(0);
    }
    STAMP(64, 4);
    if (kh == 1) {
#pragma unroll
      for (int u = 0; u < 2; ++u)
#pragma unroll
        for (int i = 0; i < 6; ++i) *(f32x4*)(smem + ((tg * 12 + u * 6 + i) * 64 + lane) * 16) = acc[u][i];
    }
    __syncthreads();
    if (kh == 0) {
#pragma unroll
      for (int u = 0; u < 2; ++u)
#pragma unroll
        for (int b = 0; b < 3; ++b) {
          const f32x4 e0 = acc[u][2 * b] + *(const f32x4*)(smem + ((tg * 12 + u * 6 + 2 * b) * 64 + lane) * 16);
          const f32x4 e1 = acc[u][2 * b + 1] + *(const f32x4*)(smem + ((tg * 12 + u * 6 + 2 * b + 1) * 64 + lane) * 16);
          const u32x4 o = {pk(e0[0], e1[0]), pk(e0[1], e1[1]), pk(e0[2], e1[2]), pk(e0[3], e1[3])};
          *(u32x4*)(ws + FO_OFF + ((size_t)(tm * 128 + tg * 32 + u * 16 + c16) * DM + (b3 + b) * 32 + g * 8) * 2) = o;
        }
    }
  }
}

__global__ void __launch_bounds__(512, 2) k2_main(Args a) {
  extern __shared__ __attribute__((aligned(16))) char smem[];
  const int b = blockIdx.x, rr = b & 7, q = b >> 3;
  if (rr == 0) role_a(a, q, smem);
  else if (rr < 4 && q < 24) role_b(a, (rr - 1) * 24 + q, smem);
  else if (rr == 4) role_v(a, q, smem);
  else if (rr > 4) role_f(a, (rr - 5) * 32 + q, smem);
  else role_o(a, (rr - 1) * 8 + (q - 24), smem);
  DUMP();
}

__global__ void __launch_bounds__(256) k3_final(Args a) {
  const int i = blockIdx.x * 256 + threadIdx.x;
  const u32x4 x = ((const u32x4*)a.x)[i], p = ((const u32x4*)(a.ws + AO_OFF))[i], f = ((const u32x4*)(a.ws + FO_OFF))[i];
  u32x4 o;
#pragma unroll
  for (int k = 0; k < 4; ++k)
    o[k] = pk(bfr(bflo(x[k]) + bflo(p[k])) + bflo(f[k]), bfr(bfhi(x[k]) + bfhi(p[k])) + bfhi(f[k]));
  ((u32x4*)a.out)[i] = o;
}

extern "C" void kernel_launch(void* const* d_in, const int* in_sizes, int n_in, void* d_out, int out_size, void* d_ws,
                              size_t ws_size, hipStream_t stream) {
  Args a;
  a.x = (const u16*)d_in[0]; a.mem0 = (const u16*)d_in[1]; a.wq = (const u16*)d_in[2]; a.wk = (const u16*)d_in[3];
  a.wv = (const u16*)d_in[4]; a.wo = (const u16*)d_in[5]; a.wg = (const u16*)d_in[8]; a.wu = (const u16*)d_in[9];
  a.wd = (const u16*)d_in[10];
  a.ws = (unsigned char*)(((uintptr_t)d_ws + 65535) & ~(uintptr_t)65535);
  a.out = (u16*)d_out;
  (void)hipFuncSetAttribute((const void*)k2_main, hipFuncAttributeMaxDynamicSharedMemorySize, LDS_BYTES);
  k1_prep<<<K1_GRID, 256, 0, stream>>>(a);
  k2_main<<<256, 512, LDS_BYTES, stream>>>(a);
  k3_final<<<S_ * DM / 8 / 256, 256, 0, stream>>>(a);
}
```

```cpp
#include <hip/hip_runtime.h>
#include <hip/hip_bf16.h>

typedef unsigned u32;
typedef unsigned long long u64;
typedef unsigned short u16;
typedef __attribute__((ext_vector_type(8))) __bf16 bf16x8;
typedef __attribute__((ext_vector_type(2))) __bf16 bf16x2;
typedef __attribute__((ext_vector_type(4))) float f32x4;
typedef __attribute__((ext_vector_type(2))) float f32x2;
typedef __attribute__((ext_vector_type(4))) unsigned u32x4;
typedef __attribute__((ext_vector_type(2))) unsigned u32x2;

constexpr int S_ = 2048, DM = 576, NH = 32, DH = 128, FF = 4608, NSEG = 64, KS = 18;
constexpr int NB = 72, NF = 96;
constexpr u32 HO_OFF = 0;
constexpr u32 PT_OFF = HO_OFF + NSEG * 65536;
constexpr u32 PT_STEP = 4 * 20480;
constexpr u32 FLG_OFF = PT_OFF + NSEG * PT_STEP;
constexpr u32 FLM_OFF = FLG_OFF + NSEG * 128;
constexpr u32 FLQ_OFF = FLM_OFF + NSEG * 3072;
constexpr u32 FLP_OFF = FLQ_OFF + NSEG * 128;
constexpr u32 POISON_BYTES = FLP_OFF + 256;
constexpr u32 CTL_OFF = (POISON_BYTES + 4095) & ~4095u;
constexpr u32 M0_OFF = CTL_OFF + 4096;
constexpr u32 XF_OFF = M0_OFF + 12288;
constexpr u32 XF_SEG = 2 * KS * 1024;
constexpr u32 WVO_OFF = XF_OFF + NSEG * XF_SEG;
constexpr u32 WVO_J = 16 * KS * 1024;
constexpr u32 UX_OFF = WVO_OFF + NB * WVO_J;
constexpr u32 UM_OFF = UX_OFF + NSEG * NB * 16384;
constexpr u32 QX_OFF = UM_OFF + NSEG * NB * 4096;
constexpr u32 H_OFF = QX_OFF + NSEG * NH * 8192;
constexpr u32 WDF_OFF = H_OFF + 128 * 144 * 1024;
constexpr u32 FO_OFF = WDF_OFF + 9 * 144 * 4096;
constexpr u32 AO_OFF = FO_OFF + S_ * DM * 2;
constexpr u32 WS_END = AO_OFF + S_ * DM * 2;
constexpr u32 POISON = 0xFFFFFFFFu;
constexpr int SC1 = 16;
constexpr u32 SPIN_CAP = 400000u;
constexpr int LDS_BYTES = 160 * 1024;

__device__ __forceinline__ u32 pk(float lo, float hi) {
  f32x2 v = {lo, hi};
  return __builtin_bit_cast(u32, __builtin_convertvector(v, bf16x2));
}
__device__ __forceinline__ u32 pks(float lo, float hi) { u32 r = pk(lo, hi); return r == POISON ? 0x7FC07FC0u : r; }
__device__ __forceinline__ float bfr(float x) { return (float)(__bf16)x; }
__device__ __forceinline__ float bflo(u32 w) { return __uint_as_float(w << 16); }
__device__ __forceinline__ float bfhi(u32 w) { return __uint_as_float(w & 0xFFFF0000u); }
__device__ __forceinline__ f32x4 mfma(u32x4 a, u32x4 b, f32x4 c) {
  return __builtin_amdgcn_mfma_f32_16x16x32_bf16(__builtin_bit_cast(bf16x8, a), __builtin_bit_cast(bf16x8, b), c, 0, 0, 0);
}
__device__ __forceinline__ u32 plo(u32 a, u32 b) { return __builtin_amdgcn_perm(b, a, 0x05040100u); }
__device__ __forceinline__ u32 phi(u32 a, u32 b) { return __builtin_amdgcn_perm(b, a, 0x07060302u); }
__device__ __forceinline__ float vmax(float a, float b) { float r; asm("v_max_f32 %0, %1, %2" : "=v"(r) : "v"(a), "v"(b)); return r; }
__device__ __forceinline__ float vmax3(float a, float b, float c) { float r; asm("v_max3_f32 %0, %1, %2, %3" : "=v"(r) : "v"(a), "v"(b), "v"(c)); return r; }
__device__ __forceinline__ float xmax(float x) {
  auto s = __builtin_amdgcn_permlane16_swap(__float_as_uint(x), __float_as_uint(x), false, false);
  x = vmax(__uint_as_float(s[0]), __uint_as_float(s[1]));
  auto t = __builtin_amdgcn_permlane32_swap(__float_as_uint(x), __float_as_uint(x), false, false);
  return vmax(__uint_as_float(t[0]), __uint_as_float(t[1]));
}
__device__ __forceinline__ float xsum(float x) {
  auto s = __builtin_amdgcn_permlane16_swap(__float_as_uint(x), __float_as_uint(x), false, false);
  x = __uint_as_float(s[0]) + __uint_as_float(s[1]);
  auto t = __builtin_amdgcn_permlane32_swap(__float_as_uint(x), __float_as_uint(x), false, false);
  return __uint_as_float(t[0]) + __uint_as_float(t[1]);
}
__device__ __forceinline__ float ror8(float x) {
  return __builtin_bit_cast(float, __builtin_amdgcn_mov_dpp(__builtin_bit_cast(int, x), 0x128, 0xf, 0xf, true));
}
__device__ __forceinline__ u32 mx(u32 a, u32 b) { return a > b ? a : b; }
__device__ __forceinline__ u32 mx4(u32x4 v) { return mx(mx(v.x, v.y), mx(v.z, v.w)); }
__device__ __forceinline__ u32 mx2(u32x2 v) { return mx(v.x, v.y); }
__device__ __forceinline__ bool allok(u32 m) { return __all(m != POISON); }
__device__ __forceinline__ void glds16(const void* g, u32 lds) {
  u32 keep;
  asm volatile("s_mov_b32 %0, m0\n\ts_mov_b32 m0, %2\n\ts_nop 0\n\tglobal_load_lds_dwordx4 %1, off\n\ts_mov_b32 m0, %0"
               : "=&s"(keep) : "v"(g), "s"(lds) : "memory");
}
__device__ __forceinline__ void wait_vm() { asm volatile("s_waitcnt vmcnt(0)" ::: "memory"); }
__device__ __forceinline__ void bar() {
  asm volatile("s_waitcnt lgkmcnt(0)" ::: "memory");
  __builtin_amdgcn_s_barrier();
  asm volatile("" ::: "memory");
}
__device__ __forceinline__ bool spin_fail(u32& spins, u32* tmo) {
  ++spins;
  if (spins > SPIN_CAP) { __hip_atomic_store(tmo, 1u, __ATOMIC_RELAXED, __HIP_MEMORY_SCOPE_AGENT); return true; }
  if ((spins & 1023u) == 0u && __hip_atomic_load(tmo, __ATOMIC_RELAXED, __HIP_MEMORY_SCOPE_AGENT) != 0u) return true;
  return false;
}
__device__ __forceinline__ u32 homap(u32 lo, u32 flip) { return ((lo & ~8191u) << 1) + (lo & 8191u) + flip; }
constexpr u32 HO_PM = 16384, HO_M = 20480;

#define STAMP(step, k) do {} while (0)
#define STAMPC(step, k) do {} while (0)
#define DUMP() do {} while (0)
struct Args {
  const u16* x; const u16* mem0; const u16* wq; const u16* wk; const u16* wv; const u16* wo;
  const u16* wg; const u16* wu; const u16* wd; unsigned char* ws; u16* out;
};

constexpr int K1_WVO = 2592, K1_LN = 512, K1_LNB = K1_LN / 4, K1_JOBS = K1_WVO + K1_LN, K1_GRID = K1_JOBS / 4 + 8;
__global__ void __launch_bounds__(256) k1_prep(Args a) {
  const int lane = threadIdx.x & 63, wave = __builtin_amdgcn_readfirstlane(threadIdx.x >> 6);
  const int b = blockIdx.x;
  unsigned char* ws = a.ws;
  const int c16 = lane & 15, g = lane >> 4;
  if (b == K1_GRID - 1) {
    if (threadIdx.x == 0) { ((u32*)(ws + CTL_OFF))[64] = 0u; ((u32*)(ws + CTL_OFF))[128] = 0u; }
    for (int i = threadIdx.x; i < 576; i += 256) {
      u32x4 v = *(const u32x4*)(a.mem0 + (i & 7) * DM + (i >> 3) * 8);
      if (v.x == POISON) v.x = 0x7FC07FC0u; if (v.y == POISON) v.y = 0x7FC07FC0u;
      if (v.z == POISON) v.z = 0x7FC07FC0u; if (v.w == POISON) v.w = 0x7FC07FC0u;
      *(u32x4*)(ws + M0_OFF + i * 16) = v;
    }
    return;
  }
  if (b >= K1_JOBS / 4) return;
  const int job = b * 4 + wave;
  auto poison = [&]() {
    const u32x4 pv = {POISON, POISON, POISON, POISON};
    for (u32 o = (job * 64 + lane) * 16; o < POISON_BYTES; o += K1_JOBS * 64 * 16) *(u32x4*)(ws + o) = pv;
  };
  if (b >= K1_LNB) {
    const int jx = ((b - K1_LNB) >> 3) * 4 + wave, h = (b & 7) * 4 + jx / 81, kp = (jx / 9) % 9, bb = jx % 9;
    u32x2 rw[4][8];
    u32x4 af[2][2][4];
#pragma unroll
    for (int es = 0; es < 4; ++es)
#pragma unroll
      for (int jj = 0; jj < 8; ++jj)
        rw[es][jj] = *(const u32x2*)(a.wo + (size_t)(h * DH + es * 32 + g * 8 + jj) * DM + bb * 64 + c16 * 4);
#pragma unroll
    for (int i = 0; i < 16; ++i) {
      const int k = (kp * 2 + (i >> 3)) * 32 + (c16 >> 2) * 8 + (c16 & 3) + ((i >> 2) & 1) * 4;
      af[i >> 3][(i >> 2) & 1][i & 3] = *(const u32x4*)(a.wv + (size_t)k * (NH * DH) + h * DH + (i & 3) * 32 + g * 8);
    }
    poison();
    __builtin_amdgcn_sched_barrier(0);
    u32x4 bf[4][4];
#pragma unroll
    for (int es = 0; es < 4; ++es)
#pragma unroll
      for (int p = 0; p < 4; ++p) {
        bf[es][0][p] = plo(rw[es][2 * p].x, rw[es][2 * p + 1].x); bf[es][1][p] = phi(rw[es][2 * p].x, rw[es][2 * p + 1].x);
        bf[es][2][p] = plo(rw[es][2 * p].y, rw[es][2 * p + 1].y); bf[es][3][p] = phi(rw[es][2 * p].y, rw[es][2 * p + 1].y);
      }
#pragma unroll
    for (int tt = 0; tt < 2; ++tt) {
      const int t = kp * 2 + tt;
      f32x4 acc[2][4];
#pragma unroll
      for (int rt = 0; rt < 2; ++rt)
#pragma unroll
        for (int i = 0; i < 4; ++i) {
          acc[rt][i] = f32x4{0.f, 0.f, 0.f, 0.f};
#pragma unroll
          for (int es = 0; es < 4; ++es) acc[rt][i] = mfma(af[tt][rt][es], bf[es][i], acc[rt][i]);
        }
      const int j = bb * 8 + (c16 >> 1);
      unsigned char* dst = ws + WVO_OFF + (size_t)j * WVO_J + ((h >> 1) * KS + t) * 1024 + (g * 16 + (h & 1) * 8 + (c16 & 1) * 4) * 16;
#pragma unroll
      for (int i = 0; i < 4; ++i) {
        u32x4 v = {pk(acc[0][i][0], acc[0][i][1]), pk(acc[0][i][2], acc[0][i][3]), pk(acc[1][i][0], acc[1][i][1]), pk(acc[1][i][2], acc[1][i][3])};
        *(u32x4*)(dst + i * 16) = v;
      }
    }
  } else {
    const int row = job * 4 + g;
    u32x4 v[5];
#pragma unroll
    for (int i = 0; i < 5; ++i) {
      v[i] = u32x4{0, 0, 0, 0};
      if (i < 4 || c16 < 8) v[i] = *(const u32x4*)(a.x + (size_t)row * DM + (i * 16 + c16) * 8);
    }
    poison();
    float sm = 0.f;
#pragma unroll
    for (int i = 0; i < 20; ++i) sm += bflo(v[i >> 2][i & 3]) + bfhi(v[i >> 2][i & 3]);
#pragma unroll
    for (int o = 1; o < 16; o <<= 1) sm += __shfl_xor(sm, o);
    const float mean = sm * (1.f / DM);
    float q = 0.f;
#pragma unroll
    for (int i = 0; i < 20; ++i) {
      const float d0 = bflo(v[i >> 2][i & 3]) - mean, d1 = bfhi(v[i >> 2][i & 3]) - mean;
      q += (i < 16 || c16 < 8) ? d0 * d0 + d1 * d1 : 0.f;
    }
#pragma unroll
    for (int o = 1; o < 16; o <<= 1) q += __shfl_xor(q, o);
    const float mb = bfr(mean), den = bfr(sqrtf(bfr(bfr(q * (1.f / DM)) + 1e-5f)));
#pragma unroll
    for (int i = 0; i < 5; ++i) {
      u32x4 o;
#pragma unroll
      for (int k = 0; k < 4; ++k) o[k] = pk(bfr(bflo(v[i][k]) - mb) / den, bfr(bfhi(v[i][k]) - mb) / den);
      const int ch = i * 16 + c16;
      if (i < 4 || c16 < 8) *(u32x4*)(ws + XF_OFF + ((row >> 4) * KS + (ch >> 2)) * 1024 + ((ch & 3) * 16 + (row & 15)) * 16) = o;
    }
  }
}

constexpr u32 L_X = 0;
constexpr u32 L_M = 2 * XF_SEG;
constexpr u32 L_QM = L_M + 9216;
constexpr u32 L_QX = L_QM + 8192;

template <bool WA, int ST>
__device__ __forceinline__ void proj2(const u32x4 (&W0)[KS], const u32x4 (&W1)[KS], const char* xb, f32x4& c0, f32x4& c1) {
  u32x4 xf[KS];
#pragma unroll
  for (int t = 0; t < KS; ++t) xf[t] = *(const u32x4*)(xb + t * ST);
  c0 = f32x4{0.f, 0.f, 0.f, 0.f}; c1 = c0;
#pragma unroll
  for (int t = 0; t < KS; ++t) {
    c0 = WA ? mfma(W0[t], xf[t], c0) : mfma(xf[t], W0[t], c0);
    c1 = WA ? mfma(W1[t], xf[t], c1) : mfma(xf[t], W1[t], c1);
  }
  __builtin_amdgcn_sched_group_barrier(0x100, 6, 0);
#pragma unroll
  for (int t = 0; t < KS - 6; ++t) { __builtin_amdgcn_sched_group_barrier(0x008, 2, 0); __builtin_amdgcn_sched_group_barrier(0x100, 1, 0); }
  __builtin_amdgcn_sched_group_barrier(0x008, 12, 0);
}
__device__ __forceinline__ void stage_x(const unsigned char* ws, int seg, int wave, int lane) {
  const unsigned char* src = ws + XF_OFF + seg * XF_SEG + lane * 16;
  const u32 dst = L_X + (seg & 1) * XF_SEG;
#pragma unroll
  for (int i = 0; i < 5; ++i) {
    const int p = i * 8 + wave;
    if (p < 36) glds16(src + p * 1024, dst + p * 1024);
  }
}
__device__ __forceinline__ u32 get_flips(__amdgpu_buffer_rsrc_t r, int lane, u32* tmo) {
  u32 f, spins = 0;
  for (;;) {
    f = __builtin_amdgcn_raw_buffer_load_b32(r, FLP_OFF + lane * 4, 0, SC1);
    if (allok(f)) break;
    if (spin_fail(spins, tmo)) break;
    __builtin_amdgcn_s_sleep(8);
  }
  return f & 8192u;
}
__device__ __forceinline__ void gather_m(__amdgpu_buffer_rsrc_t r, u32 base, u32 flip, bool first, char* smem, int wave, int lane, u32* tmo) {
  const u32 l0 = (wave * 64 + lane) * 16, l1 = (512 + wave * 8 + (lane & 7)) * 16;
  const u32 o0 = first ? M0_OFF + l0 : base + homap(HO_M + l0, flip), o1 = first ? M0_OFF + l1 : base + homap(HO_M + l1, flip);
  u32 spins = 0;
  u32x4 v, w;
  for (;;) {
    v = __builtin_amdgcn_raw_buffer_load_b128(r, o0, 0, SC1);
    w = __builtin_amdgcn_raw_buffer_load_b128(r, o1, 0, SC1);
    if (allok(mx(mx4(v), mx4(w)))) break;
    if (spin_fail(spins, tmo)) break;
  }
  *(u32x4*)(smem + L_M + l0) = v;
  if (lane < 8) *(u32x4*)(smem + L_M + l1) = w;
  wait_vm();
}

__device__ __forceinline__ void role_a(const Args& a, int h, char* smem) {
  const int tid = threadIdx.x, lane = tid & 63, wave = __builtin_amdgcn_readfirstlane(tid >> 6);
  const int c16 = lane & 15, g = lane >> 4;
  unsigned char* ws = a.ws;
  u32* tmo = (u32*)(ws + CTL_OFF) + 64;
  __amdgpu_buffer_rsrc_t r = __builtin_amdgcn_make_buffer_rsrc(ws, 0, WS_END, 0x00020000);
  STAMP(64, 0);
  u32x4 W[2][KS];
  {
    u32x4 st[18];
    auto ldw = [&](int hf) {
#pragma unroll
      for (int i = 0; i < 18; ++i) {
        const int pc = i * 512 + tid;
        st[i] = *(const u32x4*)(((pc & 16) ? a.wk : a.wq) + (size_t)(hf * 288 + (pc >> 5)) * (NH * DH) + h * DH + (pc & 15) * 8);
      }
    };
    ldw(0);
#pragma unroll
    for (int hf = 0; hf < 2; ++hf) {
#pragma unroll
      for (int i = 0; i < 18; ++i) { const int pc = i * 512 + tid; *(u32x4*)(smem + (pc >> 5) * 528 + (pc & 31) * 16) = st[i]; }
      if (hf == 0) ldw(1);
      bar();
      const char* wl = smem + g * 8 * 528 + wave * 64 + c16 * 4;
#pragma unroll
      for (int t = 0; t < 9; ++t) {
#pragma unroll
        for (int p = 0; p < 4; ++p) {
          const u32 d0 = *(const u32*)(wl + (t * 32 + 2 * p) * 528), d1 = *(const u32*)(wl + (t * 32 + 2 * p + 1) * 528);
          W[0][hf * 9 + t][p] = plo(d0, d1); W[1][hf * 9 + t][p] = phi(d0, d1);
        }
        asm volatile("" : "+v"(W[0][hf * 9 + t]), "+v"(W[1][hf * 9 + t]) :: "memory");
      }
      bar();
    }
  }
  stage_x(ws, 0, wave, lane);
  stage_x(ws, 1, wave, lane);
  const u32 flips = get_flips(r, lane, tmo);
  wait_vm();
  bar();
  const u32 qsel = (wave >> 2) * 8192 + (wave & 3) * 1024 + lane * 16;
  auto proj_x = [&](int seg, int rt) {
    f32x4 c0, c1;
    proj2<true, 1024>(W[0], W[1], smem + L_X + (seg & 1) * XF_SEG + rt * KS * 1024 + lane * 16, c0, c1);
    u32x4 o = {pk(c0[0], c1[0]), pk(c0[1], c1[1]), pk(c0[2], c1[2]), pk(c0[3], c1[3])};
    *(u32x4*)(smem + L_QX + (seg & 1) * 16384 + qsel + rt * 4096) = o;
  };
  const u32 lx = (((h >> 1) * 4 + g) * 16 + (h & 1) * 8 + (lane & 7)) * 16, lm = HO_PM + h * 128 + g * 64 + (lane & 7) * 8;
  STAMP(64, 1); STAMPC(65, 0); STAMP(65, 1);
  if (wave >= 4) { proj_x(0, 0); proj_x(0, 1); }
  u32x4 kf[8];
  u32 fl = POISON;
  auto q_load = [&](int seg) {
    const u32 fo = FLQ_OFF + seg * 128 + h * 4;
    u32 spins = 0;
    while (!allok(fl)) {
      fl = __builtin_amdgcn_raw_buffer_load_b32(r, fo, 0, SC1);
      if (spin_fail(spins, tmo)) break;
    }
    asm volatile("" ::: "memory");
#pragma unroll
    for (int w = 0; w < 4; ++w) kf[w] = __builtin_amdgcn_raw_buffer_load_b128(r, QX_OFF + ((seg * NH + h) * 8 + (wave - 1) * 4 + w) * 1024 + lane * 16, 0, SC1);
    fl = seg + 1 < NSEG ? __builtin_amdgcn_raw_buffer_load_b32(r, fo + 128, 0, SC1) : 0u;
  };
  for (int s = 0; s < NSEG; ++s) {
    const u32 flip = __builtin_amdgcn_readlane(flips, s), base = HO_OFF + s * 65536;
    STAMP(s, 0);
    gather_m(r, base, flip, s == 0, smem, wave, lane, tmo);
    STAMP(s, 1);
    bar();
    STAMP(s, 2);
    {
      f32x4 c0, c1;
      proj2<true, 512>(W[0], W[1], smem + L_M + g * 128 + (lane & 7) * 16, c0, c1);
      u32x4 o = {pk(c0[0], c1[0]), pk(c0[1], c1[1]), pk(c0[2], c1[2]), pk(c0[3], c1[3])};
      *(u32x4*)(smem + L_QM + wave * 1024 + lane * 16) = o;
    }
    if (wave == 0) {
      __builtin_amdgcn_s_setprio(3);
#pragma unroll
      for (int w = 0; w < 8; ++w) kf[w] = *(const u32x4*)(smem + L_QX + (s & 1) * 16384 + 8192 + lane * 16 + w * 1024);
    }
    bar();
    STAMP(s, 3);
    const float C2 = 0.08838834764831845f * 1.4426950408889634f;
    if (wave != 0) __builtin_amdgcn_s_sleep(11);
    if (wave == 0) {
      if (s < NSEG - 1) {
        const char* qb = smem + L_QM + lane * 16;
        f32x4 s0 = {0.f, 0.f, 0.f, 0.f}, s1 = s0, s2 = s0;
#pragma unroll
        for (int w = 0; w < 4; ++w) {
          const u32x4 qf = *(const u32x4*)(qb + w * 1024);
          s2 = mfma(*(const u32x4*)(qb + 4096 + w * 1024), qf, s2);
          s0 = mfma(kf[w], qf, s0);
          s1 = mfma(kf[4 + w], qf, s1);
        }
        const bool hi = c16 >= 8;
        float v[6];
#pragma unroll
        for (int i = 0; i < 4; ++i) v[i] = hi ? s1[i] : s0[i];
        v[4] = g < 2 ? (hi ? s2[2] : s2[0]) : -INFINITY;
        v[5] = g < 2 ? (hi ? s2[3] : s2[1]) : -INFINITY;
        float m = vmax(vmax3(v[0], v[1], v[2]), vmax3(v[3], v[4], v[5]));
        m = xmax(m);
        m = vmax(m, ror8(m)) * C2;
#pragma unroll
        for (int i = 0; i < 6; ++i) v[i] = __builtin_amdgcn_exp2f(__builtin_fmaf(v[i], C2, -m));
        float sum = ((v[0] + v[1]) + (v[2] + v[3])) + (v[4] + v[5]);
        sum += ror8(sum);
        const float inv = __builtin_amdgcn_rcpf(xsum(sum));
        const u32x2 px = {pk(v[0] * inv, v[1] * inv) & 0x7FFF7FFFu, pk(v[2] * inv, v[3] * inv) & 0x7FFF7FFFu};
        const u32 pm = pk(v[4] * inv, v[5] * inv) & 0x7FFF7FFFu;
        __builtin_amdgcn_raw_buffer_store_b64(px, r, base + homap(lx + (c16 >> 3) * 8, flip), 0, SC1);
        if (g < 2) __builtin_amdgcn_raw_buffer_store_b32(pm, r, base + homap(lm + (c16 >> 3) * 4, flip), 0, SC1);
      }
      __builtin_amdgcn_s_setprio(0);
    } else if (wave < 3) {
      const char* qx = smem + L_QX + (s & 1) * 16384 + lane * 16;
      if (s == 0) q_load(0);
      f32x4 s0 = {0.f, 0.f, 0.f, 0.f}, s1 = s0, s2 = s0;
#pragma unroll
      for (int w = 0; w < 4; ++w) {
        s2 = mfma(*(const u32x4*)(smem + L_QM + 4096 + lane * 16 + w * 1024), kf[w], s2);
        s0 = mfma(*(const u32x4*)(qx + 8192 + w * 1024), kf[w], s0);
        s1 = mfma(*(const u32x4*)(qx + 12288 + w * 1024), kf[w], s1);
      }
      const int qi = (wave - 1) * 16 + c16;
      const float NEG = -1.0e9f * 1.4426950408889634f;
      float v[12];
#pragma unroll
      for (int i = 0; i < 4; ++i) {
        v[i] = (4 * g + i <= qi) ? s0[i] * C2 : NEG;
        v[4 + i] = (16 + 4 * g + i <= qi) ? s1[i] * C2 : NEG;
        v[8 + i] = g < 2 ? s2[i] * C2 : -INFINITY;
      }
      float m = v[0];
#pragma unroll
      for (int i = 1; i < 12; ++i) m = fmaxf(m, v[i]);
      m = xmax(m);
      float sum = 0.f;
#pragma unroll
      for (int i = 0; i < 12; ++i) { v[i] = __builtin_amdgcn_exp2f(v[i] - m); sum += v[i]; }
      const float inv = __builtin_amdgcn_rcpf(xsum(sum));
      u32x4 px; u32x2 pm;
#pragma unroll
      for (int i = 0; i < 4; ++i) px[i] = pk(v[2 * i] * inv, v[2 * i + 1] * inv) & 0x7FFF7FFFu;
#pragma unroll
      for (int i = 0; i < 2; ++i) pm[i] = pk(v[8 + 2 * i] * inv, v[9 + 2 * i] * inv) & 0x7FFF7FFFu;
      const u32 tb = PT_OFF + s * PT_STEP + ((wave - 1) * 2 + (c16 >> 3)) * 20480;
      __builtin_amdgcn_raw_buffer_store_b128(px, r, tb + lx, 0, SC1);
      if (g < 2) __builtin_amdgcn_raw_buffer_store_b64(pm, r, tb + lm, 0, SC1);
      if (s + 1 < NSEG) q_load(s + 1);
    }
    STAMP(s, 4);
    if (wave >= 4) {
      if (s + 1 < NSEG) { proj_x(s + 1, 0); proj_x(s + 1, 1); }
    } else if (s + 2 < NSEG) {
      const unsigned char* src = ws + XF_OFF + (s + 2) * XF_SEG + lane * 16;
      const int p0 = wave == 3 ? 18 : wave * 6, pn = wave == 3 ? 18 : 6;
      for (int p = p0; p < p0 + pn; ++p) glds16(src + p * 1024, L_X + (s & 1) * XF_SEG + p * 1024);
    }
  }
  STAMPC(65, 2); STAMP(65, 3);
}

__device__ __forceinline__ void role_b(const Args& a, int j, char* smem) {
  const int tid = threadIdx.x, lane = tid & 63, wave = __builtin_amdgcn_readfirstlane(tid >> 6);
  const int c16 = lane & 15, g = lane >> 4;
  unsigned char* ws = a.ws;
  u32* tmo = (u32*)(ws + CTL_OFF) + 64;
  __amdgpu_buffer_rsrc_t r = __builtin_amdgcn_make_buffer_rsrc(ws, 0, WS_END, 0x00020000);
  STAMP(64, 0);
  u32x4 W[2][KS];
  {
    const unsigned char* wsrc = ws + WVO_OFF + (size_t)j * WVO_J + wave * 2 * KS * 1024 + lane * 16;
#pragma unroll
    for (int tt = 0; tt < 2; ++tt)
#pragma unroll
      for (int t = 0; t < KS; ++t) W[tt][t] = *(const u32x4*)(wsrc + (tt * KS + t) * 1024);
  }
  wait_vm();
  bar();
  const u32 flips = get_flips(r, lane, tmo);
  STAMP(64, 1);
  u32x4 nx0, nx1;
  u32 fl = POISON;
  auto ux_load = [&](int seg) {
    const u32 fo = FLG_OFF + seg * 128 + wave * 16 + (lane & 3) * 4;
    u32 spins = 0;
    while (!allok(fl)) {
      fl = __builtin_amdgcn_raw_buffer_load_b32(r, fo, 0, SC1);
      if (spin_fail(spins, tmo)) break;
    }
    asm volatile("" ::: "memory");
    const u32 uo = UX_OFF + ((seg * NB + j) * 16 + wave * 2) * 1024 + lane * 16;
    nx0 = __builtin_amdgcn_raw_buffer_load_b128(r, uo, 0, SC1);
    nx1 = __builtin_amdgcn_raw_buffer_load_b128(r, uo + 1024, 0, SC1);
    fl = seg + 1 < NSEG ? __builtin_amdgcn_raw_buffer_load_b32(r, fo + 128, 0, SC1) : 0u;
  };
  const u32 lx = wave * 2048 + lane * 16;
  const u32 lm = HO_PM + (wave * 4 + (c16 >> 3)) * 128 + (g & 1) * 64 + (lane & 7) * 8;
  const bool diag = (c16 >> 3) == (g >> 1);
  const u32 redo = ((wave * 2 + (g >> 1)) * 64 + (lane & 7) * 8 + (g & 1) * 4) * 4;
  for (int s = 0; s < NSEG; ++s) {
    const u32 flip = __builtin_amdgcn_readlane(flips, s), base = HO_OFF + s * 65536;
    STAMP(s, 0);
    gather_m(r, base, flip, s == 0, smem, wave, lane, tmo);
    bar();
    STAMP(s, 1);
    u32x4 um;
    {
      f32x4 c0, c1;
      proj2<false, 512>(W[0], W[1], smem + L_M + g * 128 + (lane & 7) * 16, c0, c1);
      um = u32x4{pk(c0[0], c0[1]), pk(c0[2], c0[3]), pk(c1[0], c1[1]), pk(c1[2], c1[3])};
      if (g < 2) __builtin_amdgcn_raw_buffer_store_b128(um, r, UM_OFF + ((s * NB + j) * 8 + wave) * 512 + lane * 16, 0, SC1);
      else um = u32x4{0, 0, 0, 0};
    }
    if (s + 1 < NSEG) {
      if (s == 0) ux_load(0);
      STAMP(s, 2);
      const u32 o0 = base + homap(lx, flip), o1 = o0 + 1024, o2 = base + homap(lm, flip), o3 = o2 + 256;
      u32x4 p0, p1; u32x2 q0, q1;
      u32 spins = 0;
      for (;;) {
        p0 = __builtin_amdgcn_raw_buffer_load_b128(r, o0, 0, SC1);
        p1 = __builtin_amdgcn_raw_buffer_load_b128(r, o1, 0, SC1);
        q0 = __builtin_amdgcn_raw_buffer_load_b64(r, o2, 0, SC1);
        q1 = __builtin_amdgcn_raw_buffer_load_b64(r, o3, 0, SC1);
        if (allok(mx(mx(mx4(p0), mx4(p1)), mx(mx2(q0), mx2(q1))))) break;
        if (spin_fail(spins, tmo)) break;
      }
      STAMP(s, 3);
      f32x4 d = {0.f, 0.f, 0.f, 0.f}, e = d;
      const u32x4 pmv = {q0.x, q0.y, q1.x, q1.y};
      d = mfma(nx0, p0, d); e = mfma(nx1, p1, e); d = mfma(um, pmv, d);
      d += e;
      if (diag) *(f32x4*)(smem + L_QM + redo) = d;
    }
    wait_vm();
    bar();
    if (wave == 0 && s + 1 < NSEG) {
      const char* rp = smem + L_QM + (lane >> 5) * 2048 + (lane & 31) * 8;
      f32x2 acc = *(const f32x2*)rp;
#pragma unroll
      for (int p = 1; p < 8; ++p) acc += *(const f32x2*)(rp + p * 256);
      auto t0 = __builtin_amdgcn_permlane32_swap(__float_as_uint(acc.x), __float_as_uint(acc.x), false, false);
      auto t1 = __builtin_amdgcn_permlane32_swap(__float_as_uint(acc.y), __float_as_uint(acc.y), false, false);
      const u32 o = pks(__uint_as_float(t0[0]) + __uint_as_float(t0[1]), __uint_as_float(t1[0]) + __uint_as_float(t1[1]));
      const u32 nflip = __builtin_amdgcn_readlane(flips, s + 1);
      if (lane < 32) __builtin_amdgcn_raw_buffer_store_b32(o, r, base + 65536 + homap(HO_M + j * 128 + lane * 4, nflip), 0, SC1);
    }
    STAMP(s, 4);
    if (tid == 64) __builtin_amdgcn_raw_buffer_store_b32(1u, r, FLM_OFF + s * 3072 + (j / 6) * 128 + (j % 6) * 4, 0, SC1);
    if (s + 2 < NSEG) ux_load(s + 1);
    STAMP(s, 5);
  }
  STAMP(65, 0);
}

__device__ __forceinline__ void role_o(const Args& a, int o24, char* smem) {
  const int tid = threadIdx.x, lane = tid & 63, wave = __builtin_amdgcn_readfirstlane(tid >> 6);
  const int c16 = lane & 15, g = lane >> 4, o = o24 >> 1;
  unsigned char* ws = a.ws;
  u32* tmo = (u32*)(ws + CTL_OFF) + 64;
  __amdgpu_buffer_rsrc_t r = __builtin_amdgcn_make_buffer_rsrc(ws, 0, WS_END, 0x00020000);
  STAMP(64, 0);
  if (wave < 3 && o24 * 3 + wave < NSEG) {
    const int blk = o24 * 3 + wave;
    u32 best[2] = {~0u, ~0u};
    for (int rep = 0; rep < 4; ++rep)
      for (int f = 0; f < 2; ++f) {
        u32* p = (u32*)(ws + HO_OFF + blk * 65536 + homap(32768 - 128 * (rep + 1), f * 8192));
        __hip_atomic_store(p, POISON, __ATOMIC_RELAXED, __HIP_MEMORY_SCOPE_AGENT);
        wait_vm();
        u64 t0, t1;
        asm volatile("s_memtime %0\n\ts_waitcnt lgkmcnt(0)" : "=s"(t0) :: "memory");
        u32 v = __hip_atomic_load(p, __ATOMIC_RELAXED, __HIP_MEMORY_SCOPE_AGENT);
        asm volatile("s_waitcnt vmcnt(0)" : "+v"(v) :: "memory");
        asm volatile("s_memtime %0\n\ts_waitcnt lgkmcnt(0)" : "=s"(t1) :: "memory");
        const u32 d = (u32)(t1 - t0);
        best[f] = d < best[f] ? d : best[f];
      }
    if (lane == 0) __builtin_amdgcn_raw_buffer_store_b32(best[1] < best[0] ? 8192u : 0u, r, FLP_OFF + blk * 4, 0, SC1);
  }
  STAMP(64, 1);
  const u32 lx = wave * 2048 + lane * 16;
  const u32 lm = HO_PM + (wave * 4 + (c16 >> 3)) * 128 + (g & 1) * 64 + (lane & 7) * 8;
  const bool diag = (c16 >> 3) == (g >> 1);
  const u32 redo = ((wave * 2 + (g >> 1)) * 64 + (lane & 7) * 8 + (g & 1) * 4) * 4;
  for (int s = o24 & 1; s < NSEG; s += 2) {
    u32 spins = 0;
    while (!allok(__builtin_amdgcn_raw_buffer_load_b32(r, FLG_OFF + s * 128 + wave * 16 + (lane & 3) * 4, 0, SC1))) {
      if (spin_fail(spins, tmo)) break;
      __builtin_amdgcn_s_sleep(16);
    }
    asm volatile("" ::: "memory");
    u32x4 x0[6], x1[6], um[6];
#pragma unroll
    for (int c = 0; c < 6; ++c) {
      const u32 uo = UX_OFF + ((s * NB + o * 6 + c) * 16 + wave * 2) * 1024 + lane * 16;
      x0[c] = __builtin_amdgcn_raw_buffer_load_b128(r, uo, 0, SC1);
      x1[c] = __builtin_amdgcn_raw_buffer_load_b128(r, uo + 1024, 0, SC1);
    }
    if (wave == 0) {
      while (!allok(__builtin_amdgcn_raw_buffer_load_b32(r, FLM_OFF + s * 3072 + o * 128 + (lane % 6) * 4, 0, SC1))) {
        if (spin_fail(spins, tmo)) break;
        __builtin_amdgcn_s_sleep(4);
      }
    }
    bar();
#pragma unroll
    for (int c = 0; c < 6; ++c)
      um[c] = __builtin_amdgcn_raw_buffer_load_b128(r, UM_OFF + ((s * NB + o * 6 + c) * 8 + wave) * 512 + (lane & 31) * 16, 0, SC1);
    u32x4 p0[4], p1[4]; u32x2 q0[4], q1[4];
    for (;;) {
      u32 m = 0;
#pragma unroll
      for (int u = 0; u < 4; ++u) {
        const u32 tb = PT_OFF + s * PT_STEP + u * 20480;
        p0[u] = __builtin_amdgcn_raw_buffer_load_b128(r, tb + lx, 0, SC1);
        p1[u] = __builtin_amdgcn_raw_buffer_load_b128(r, tb + lx + 1024, 0, SC1);
        q0[u] = __builtin_amdgcn_raw_buffer_load_b64(r, tb + lm, 0, SC1);
        q1[u] = __builtin_amdgcn_raw_buffer_load_b64(r, tb + lm + 256, 0, SC1);
      }
#pragma unroll
      for (int u = 0; u < 4; ++u) m = mx(m, mx(mx(mx4(p0[u]), mx4(p1[u])), mx(mx2(q0[u]), mx2(q1[u]))));
      if (allok(m)) break;
      if (spin_fail(spins, tmo)) break;
    }
#pragma unroll
    for (int c = 0; c < 6; ++c) {
      if (g >= 2) um[c] = u32x4{0, 0, 0, 0};
#pragma unroll
      for (int u = 0; u < 4; ++u) {
        f32x4 d = {0.f, 0.f, 0.f, 0.f};
        const u32x4 pmv = {q0[u].x, q0[u].y, q1[u].x, q1[u].y};
        d = mfma(x0[c], p0[u], d); d = mfma(x1[c], p1[u], d); d = mfma(um[c], pmv, d);
        if (diag) *(f32x4*)(smem + (c * 4 + u) * 4096 + redo) = d;
      }
    }
    bar();
    if (tid < 384) {
      const char* rp = smem + (tid >> 4) * 4096 + (tid & 15) * 16;
      f32x4 acc = *(const f32x4*)rp;
#pragma unroll
      for (int p = 1; p < 16; ++p) acc += *(const f32x4*)(rp + p * 256);
      *(u32x2*)(ws + AO_OFF + ((size_t)(s * 32 + ((tid & 63) >> 1)) * DM + (o * 6 + (tid >> 6)) * 8 + (tid & 1) * 4) * 2) = u32x2{pk(acc[0], acc[1]), pk(acc[2], acc[3])};
    }
    STAMP(s, 0);
  }
}

__device__ __forceinline__ void role_v(const Args& a, int h, char* smem) {
  const int tid = threadIdx.x, lane = tid & 63, wave = __builtin_amdgcn_readfirstlane(tid >> 6);
  const int c16 = lane & 15, g = lane >> 4;
  unsigned char* ws = a.ws;
  __amdgpu_buffer_rsrc_t r = __builtin_amdgcn_make_buffer_rsrc(ws, 0, WS_END, 0x00020000);
  STAMP(64, 0);
  u32x4 WV[KS], WO[5][4];
  {
    const u32 sel = (c16 & 1) ? 0x07060302u : 0x05040100u;
    u32x4 st[18];
#pragma unroll
    for (int i = 0; i < 18; ++i) { const int pc = i * 512 + tid; st[i] = *(const u32x4*)(a.wv + (size_t)(pc >> 4) * (NH * DH) + h * DH + (pc & 15) * 8); }
#pragma unroll
    for (int i = 0; i < 18; ++i) { const int pc = i * 512 + tid; *(u32x4*)(smem + (pc >> 4) * 272 + (pc & 15) * 16) = st[i]; }
#pragma unroll
    for (int i = 0; i < 18; ++i) st[i] = *(const u32x4*)(a.wo + (size_t)h * DH * DM + (i * 512 + tid) * 8);
    bar();
    {
      const char* wl = smem + g * 8 * 272 + wave * 32 + (c16 >> 1) * 4;
#pragma unroll
      for (int t = 0; t < KS; ++t) {
#pragma unroll
        for (int p = 0; p < 4; ++p)
          WV[t][p] = __builtin_amdgcn_perm(*(const u32*)(wl + (t * 32 + 2 * p + 1) * 272), *(const u32*)(wl + (t * 32 + 2 * p) * 272), sel);
        asm volatile("" : "+v"(WV[t]) :: "memory");
      }
    }
    bar();
#pragma unroll
    for (int i = 0; i < 18; ++i) { const int pc = i * 512 + tid; *(u32x4*)(smem + (pc / 72) * 1168 + (pc % 72) * 16) = st[i]; }
    bar();
#pragma unroll
    for (int i = 0; i < 5; ++i) {
      const int ct = wave + 8 * i < 36 ? wave + 8 * i : 0;
      const char* wl = smem + g * 4 * 1168 + ct * 32 + (c16 >> 1) * 4;
#pragma unroll
      for (int es = 0; es < 4; ++es) {
#pragma unroll
        for (int p = 0; p < 4; ++p) {
          const int e0 = 32 * es + 16 * (p >> 1) + 2 * (p & 1);
          WO[i][es][p] = __builtin_amdgcn_perm(*(const u32*)(wl + (e0 + 1) * 1168), *(const u32*)(wl + e0 * 1168), sel);
        }
        asm volatile("" : "+v"(WO[i][es]) :: "memory");
      }
    }
    bar();
  }
  STAMP(64, 1);
  stage_x(ws, 0, wave, lane);
  for (int sg = 0; sg < NSEG; ++sg) {
    wait_vm();
    bar();
    if (sg > 0 && tid == 0) __builtin_amdgcn_raw_buffer_store_b32(1u, r, FLG_OFF + (sg - 1) * 128 + h * 4, 0, SC1);
    if (sg + 1 < NSEG) stage_x(ws, sg + 1, wave, lane);
    {
      const char* xb = smem + L_X + (sg & 1) * XF_SEG + lane * 16;
      f32x4 c0 = {0.f, 0.f, 0.f, 0.f}, c1 = c0;
#pragma unroll
      for (int t = 0; t < KS; ++t) {
        c0 = mfma(WV[t], *(const u32x4*)(xb + t * 1024), c0);
        c1 = mfma(WV[t], *(const u32x4*)(xb + (KS + t) * 1024), c1);
      }
      __builtin_amdgcn_sched_group_barrier(0x100, 8, 0);
#pragma unroll
      for (int t = 0; t < KS - 4; ++t) { __builtin_amdgcn_sched_group_barrier(0x008, 2, 0); __builtin_amdgcn_sched_group_barrier(0x100, 2, 0); }
      __builtin_amdgcn_sched_group_barrier(0x008, 8, 0);
      char* vt = smem + L_QM + (wave >> 1) * 1024 + lane * 16 + (wave & 1) * 8;
      *(u32x2*)vt = u32x2{pk(c0[0], c0[1]), pk(c0[2], c0[3])};
      *(u32x2*)(vt + 4096) = u32x2{pk(c1[0], c1[1]), pk(c1[2], c1[3])};
    }
    bar();
    u32x4 vf[2][4];
#pragma unroll
    for (int rt = 0; rt < 2; ++rt)
#pragma unroll
      for (int es = 0; es < 4; ++es) vf[rt][es] = *(const u32x4*)(smem + L_QM + rt * 4096 + es * 1024 + lane * 16);
#pragma unroll
    for (int i = 0; i < 5; ++i) {
      const int ct = wave + 8 * i;
      if (ct < 36) {
        f32x4 d0 = {0.f, 0.f, 0.f, 0.f}, d1 = d0;
#pragma unroll
        for (int es = 0; es < 4; ++es) { d0 = mfma(vf[0][es], WO[i][es], d0); d1 = mfma(vf[1][es], WO[i][es], d1); }
        const u32x4 o = {pk(d0[0], d0[1]), pk(d0[2], d0[3]), pk(d1[0], d1[1]), pk(d1[2], d1[3])};
        __builtin_amdgcn_raw_buffer_store_b128(o, r, UX_OFF + ((sg * NB + ct * 2 + (c16 >> 3)) * 16 + (h >> 1)) * 1024 + (g * 16 + (h & 1) * 8 + (lane & 7)) * 16, 0, SC1);
      }
    }
    if (sg == 0) STAMP(64, 2);
  }
  wait_vm();
  bar();
  if (tid == 0) __builtin_amdgcn_raw_buffer_store_b32(1u, r, FLG_OFF + (NSEG - 1) * 128 + h * 4, 0, SC1);
  STAMP(64, 3);
}

__device__ __forceinline__ float gelu_tanh(float x) {
  const float u = 0.7978845608028654f * (x + 0.044715f * x * x * x);
  const float e = __expf(2.f * u);
  return 0.5f * x * (2.f - 2.f / (e + 1.f));
}

__device__ __forceinline__ void role_f(const Args& a, int f, char* smem) {
  const int tid = threadIdx.x, lane = tid & 63, wave = __builtin_amdgcn_readfirstlane(tid >> 6);
  const int c16 = lane & 15, g = lane >> 4;
  unsigned char* ws = a.ws;
  u32* tmo = (u32*)(ws + CTL_OFF) + 64;
  u32* cnt = (u32*)(ws + CTL_OFF) + 128;
  __amdgpu_buffer_rsrc_t r = __builtin_amdgcn_make_buffer_rsrc(ws, 0, WS_END, 0x00020000);
  STAMP(64, 0);
  for (int it = f * 27 + wave; it < f * 27 + 27; it += 8) {
    const int ks = it / 18, bb = it % 18;
    u32 d[8];
#pragma unroll
    for (int jj = 0; jj < 8; ++jj)
      d[jj] = *(const u32*)(a.wd + (size_t)(ks * 32 + (jj >> 2) * 16 + g * 4 + (jj & 3)) * DM + bb * 32 + c16 * 2);
    const u32x4 lo = {plo(d[0], d[1]), plo(d[2], d[3]), plo(d[4], d[5]), plo(d[6], d[7])};
    const u32x4 hi = {phi(d[0], d[1]), phi(d[2], d[3]), phi(d[4], d[5]), phi(d[6], d[7])};
    __builtin_amdgcn_raw_buffer_store_b128(lo, r, WDF_OFF + (bb * 144 + ks) * 2048 + lane * 16, 0, SC1);
    __builtin_amdgcn_raw_buffer_store_b128(hi, r, WDF_OFF + (bb * 144 + ks) * 2048 + 1024 + lane * 16, 0, SC1);
  }
  auto stage = [&](int sg) {
#pragma unroll
    for (int i = 0; i < 5; ++i) {
      const int p = min(i * 8 + wave, 35);
      glds16(ws + XF_OFF + sg * XF_SEG + p * 1024 + lane * 16, (sg & 3) * XF_SEG + p * 1024);
    }
  };
  if (f >= 64) {
    const int h = f - 64;
    u32x4 WW[2][KS];
    {
      u32x4 st[18];
#pragma unroll
      for (int i = 0; i < 18; ++i) { const int pc = i * 512 + tid; st[i] = *(const u32x4*)(a.wq + (size_t)(pc >> 4) * (NH * DH) + h * DH + (pc & 15) * 8); }
#pragma unroll
      for (int i = 0; i < 18; ++i) { const int pc = i * 512 + tid; *(u32x4*)(smem + (pc >> 4) * 272 + (pc & 15) * 16) = st[i]; }
      bar();
      const char* wl = smem + g * 8 * 272 + (wave & 3) * 64 + c16 * 4;
#pragma unroll
      for (int t = 0; t < KS; ++t) {
#pragma unroll
        for (int p = 0; p < 4; ++p) {
          const u32 d0 = *(const u32*)(wl + (t * 32 + 2 * p) * 272), d1 = *(const u32*)(wl + (t * 32 + 2 * p + 1) * 272);
          WW[0][t][p] = plo(d0, d1); WW[1][t][p] = phi(d0, d1);
        }
        asm volatile("" : "+v"(WW[0][t]), "+v"(WW[1][t]) :: "memory");
      }
      wait_vm();
      bar();
    }
    stage(0); stage(1);
    for (int sg = 0; sg < NSEG; ++sg) {
      asm volatile("s_waitcnt vmcnt(5)" ::: "memory");
      bar();
      if (sg >= 2 && tid == 0) __builtin_amdgcn_raw_buffer_store_b32(1u, r, FLQ_OFF + (sg - 2) * 128 + h * 4, 0, SC1);
      stage(min(sg + 2, NSEG - 1));
      f32x4 c0, c1;
      proj2<true, 1024>(WW[0], WW[1], smem + (sg & 3) * XF_SEG + (wave >> 2) * KS * 1024 + lane * 16, c0, c1);
      const u32x4 o = {pk(c0[0], c1[0]), pk(c0[1], c1[1]), pk(c0[2], c1[2]), pk(c0[3], c1[3])};
      __builtin_amdgcn_raw_buffer_store_b128(o, r, QX_OFF + ((sg * NH + h) * 8 + wave) * 1024 + lane * 16, 0, SC1);
    }
    wait_vm();
    bar();
    if (tid < 2) __builtin_amdgcn_raw_buffer_store_b32(1u, r, FLQ_OFF + (NSEG - 2 + tid) * 128 + h * 4, 0, SC1);
  }
#pragma unroll 1
  for (int job = 0; job < 2; ++job) {
    int tid = threadIdx.x;
    asm volatile("" : "+v"(tid));
    const int hv = wave & 3, rt = wave >> 2;
    const int u0 = f < 64 ? f * 60 : 3840 + (f - 64) * 24, sl = (u0 >> 6) + job;
    const int q0 = job == 0 ? (u0 & 63) : 0, q1 = min(u0 + (f < 64 ? 60 : 24) - sl * 64, 64);
    if (q1 <= q0) break;
    u32x4 WW[2][KS];
#pragma unroll
    for (int mat = 0; mat < 2; ++mat) {
      __syncthreads();
      u32x4 st[9];
#pragma unroll
      for (int i = 0; i < 9; ++i) {
        const int pc = i * 512 + tid;
        st[i] = *(const u32x4*)((mat ? a.wu : a.wg) + (size_t)(pc >> 3) * FF + sl * 64 + (pc & 7) * 8);
      }
#pragma unroll
      for (int i = 0; i < 9; ++i) { const int pc = i * 512 + tid; *(u32x4*)(smem + (pc >> 3) * 144 + (pc & 7) * 16) = st[i]; }
      __syncthreads();
      const char* wl = smem + g * 8 * 144 + hv * 32 + (c16 >> 1) * 4;
      const u32 sel = (c16 & 1) ? 0x07060302u : 0x05040100u;
#pragma unroll
      for (int t = 0; t < KS; ++t) {
#pragma unroll
        for (int p = 0; p < 4; ++p) {
          const int o = (t * 32 + 2 * p) * 144;
          WW[mat][t][p] = __builtin_amdgcn_perm(*(const u32*)(wl + o + 144), *(const u32*)(wl + o), sel);
        }
        asm volatile("" : "+v"(WW[mat][t]) :: "memory");
      }
    }
    __syncthreads();
    if (job == 0) STAMP(64, 1);
    stage(q0); stage(min(q0 + 1, q1 - 1));
    for (int sg = q0; sg < q1; ++sg) {
      asm volatile("s_waitcnt vmcnt(5)" ::: "memory");
      bar();
      stage(min(sg + 2, q1 - 1));
      f32x4 cg, cu;
      proj2<true, 1024>(WW[0], WW[1], smem + (sg & 3) * XF_SEG + rt * KS * 1024 + lane * 16, cg, cu);
      float hh[4];
#pragma unroll
      for (int i = 0; i < 4; ++i) hh[i] = bfr(gelu_tanh(bfr(cg[i]))) * bfr(cu[i]);
      const u32x2 o = {pk(hh[0], hh[1]), pk(hh[2], hh[3])};
      __builtin_amdgcn_raw_buffer_store_b64(o, r, H_OFF + ((sg * 2 + rt) * 144 + sl * 2 + (hv >> 1)) * 1024 + lane * 16 + (hv & 1) * 8, 0, SC1);
    }
    wait_vm();
  }
  wait_vm();
  __syncthreads();
  STAMP(64, 2);
  if (tid == 0) {
    __hip_atomic_fetch_add(cnt, 1u, __ATOMIC_RELAXED, __HIP_MEMORY_SCOPE_AGENT);
    u32 spins = 0;
    while (__hip_atomic_load(cnt, __ATOMIC_RELAXED, __HIP_MEMORY_SCOPE_AGENT) < (u32)NF) {
      __builtin_amdgcn_s_sleep(8);
      if (spin_fail(spins, tmo)) break;
    }
    __builtin_amdgcn_fence(__ATOMIC_ACQUIRE, "agent");
    wait_vm();
  }
  __syncthreads();
  STAMP(64, 3);
  {
    const int tm = f / 6, b3 = (f % 6) * 3, tg = wave & 3, kh = wave >> 2;
    const unsigned char* hb = ws + H_OFF + ((size_t)(tm * 8 + tg * 2) * 144 + kh * 72) * 1024 + lane * 16;
    const unsigned char* wb = ws + WDF_OFF + (size_t)(b3 * 144 + kh * 72) * 2048 + lane * 16;
    f32x4 acc[2][6];
#pragma unroll
    for (int u = 0; u < 2; ++u)
#pragma unroll
      for (int i = 0; i < 6; ++i) acc[u][i] = f32x4{0.f, 0.f, 0.f, 0.f};
    u32x4 hf[2][2][2], wf[2][2][6];
    auto ld = [&](int st, int t0) {
#pragma unroll
      for (int t = 0; t < 2; ++t) {
        hf[st][t][0] = *(const u32x4*)(hb + (t0 + t) * 1024); hf[st][t][1] = *(const u32x4*)(hb + (144 + t0 + t) * 1024);
#pragma unroll
        for (int i = 0; i < 6; ++i) wf[st][t][i] = *(const u32x4*)(wb + ((i >> 1) * 144 + t0 + t) * 2048 + (i & 1) * 1024);
      }
    };
    auto mm = [&](int st) {
#pragma unroll
      for (int t = 0; t < 2; ++t)
#pragma unroll
        for (int i = 0; i < 6; ++i) { acc[0][i] = mfma(wf[st][t][i], hf[st][t][0], acc[0][i]); acc[1][i] = mfma(wf[st][t][i], hf[st][t][1], acc[1][i]); }
    };
    ld(0, 0);
#pragma unroll 1
    for (int t0 = 0; t0 < 72; t0 += 4) {
      ld(1, t0 + 2);
      __builtin_amdgcn_sched_barrier(0);
      mm(0);
      __builtin_amdgcn_sched_barrier(0);
      ld(0, min(t0 + 4, 70));
      __builtin_amdgcn_sched_barrier(0);
      mm(1);
      __builtin_amdgcn_sched_barrier(0);
    }
    STAMP(64, 4);
    if (kh == 1) {
#pragma unroll
      for (int u = 0; u < 2; ++u)
#pragma unroll
        for (int i = 0; i < 6; ++i) *(f32x4*)(smem + ((tg * 12 + u * 6 + i) * 64 + lane) * 16) = acc[u][i];
    }
    __syncthreads();
    if (kh == 0) {
#pragma unroll
      for (int u = 0; u < 2; ++u)
#pragma unroll
        for (int b = 0; b < 3; ++b) {
          const f32x4 e0 = acc[u][2 * b] + *(const f32x4*)(smem + ((tg * 12 + u * 6 + 2 * b) * 64 + lane) * 16);
          const f32x4 e1 = acc[u][2 * b + 1] + *(const f32x4*)(smem + ((tg * 12 + u * 6 + 2 * b + 1) * 64 + lane) * 16);
          const u32x4 o = {pk(e0[0], e1[0]), pk(e0[1], e1[1]), pk(e0[2], e1[2]), pk(e0[3], e1[3])};
          *(u32x4*)(ws + FO_OFF + ((size_t)(tm * 128 + tg * 32 + u * 16 + c16) * DM + (b3 + b) * 32 + g * 8) * 2) = o;
        }
    }
  }
}

__global__ void __launch_bounds__(512, 2) k2_main(Args a) {
  extern __shared__ __attribute__((aligned(16))) char smem[];
  const int b = blockIdx.x, rr = b & 7, q = b >> 3;
  if (rr == 0) role_a(a, q, smem);
  else if (rr < 4 && q < 24) role_b(a, (rr - 1) * 24 + q, smem);
  else if (rr == 4) role_v(a, q, smem);
  else if (rr > 4) role_f(a, (rr - 5) * 32 + q, smem);
  else role_o(a, (rr - 1) * 8 + (q - 24), smem);
  DUMP();
}

__global__ void __launch_bounds__(256) k3_final(Args a) {
  const int i = blockIdx.x * 256 + threadIdx.x;
  const u32x4 x = ((const u32x4*)a.x)[i], p = ((const u32x4*)(a.ws + AO_OFF))[i], f = ((const u32x4*)(a.ws + FO_OFF))[i];
  u32x4 o;
#pragma unroll
  for (int k = 0; k < 4; ++k)
    o[k] = pk(bfr(bflo(x[k]) + bflo(p[k])) + bflo(f[k]), bfr(bfhi(x[k]) + bfhi(p[k])) + bfhi(f[k]));
  ((u32x4*)a.out)[i] = o;
}

extern "C" void kernel_launch(void* const* d_in, const int* in_sizes, int n_in, void* d_out, int out_size, void* d_ws,
                              size_t ws_size, hipStream_t stream) {
  Args a;
  a.x = (const u16*)d_in[0]; a.mem0 = (const u16*)d_in[1]; a.wq = (const u16*)d_in[2]; a.wk = (const u16*)d_in[3];
  a.wv = (const u16*)d_in[4]; a.wo = (const u16*)d_in[5]; a.wg = (const u16*)d_in[8]; a.wu = (const u16*)d_in[9];
  a.wd = (const u16*)d_in[10];
  a.ws = (unsigned char*)(((uintptr_t)d_ws + 65535) & ~(uintptr_t)65535);
  a.out = (u16*)d_out;
  (void)hipFuncSetAttribute((const void*)k2_main, hipFuncAttributeMaxDynamicSharedMemorySize, LDS_BYTES);
  k1_prep<<<K1_GRID, 256, 0, stream>>>(a);
  k2_main<<<256, 512, LDS_BYTES, stream>>>(a);
  k3_final<<<S_ * DM / 8 / 256, 256, 0, stream>>>(a);
}
```

```cpp
#include <hip/hip_runtime.h>
#include <hip/hip_bf16.h>

typedef unsigned u32;
typedef unsigned long long u64;
typedef unsigned short u16;
typedef __attribute__((ext_vector_type(8))) __bf16 bf16x8;
typedef __attribute__((ext_vector_type(2))) __bf16 bf16x2;
typedef __attribute__((ext_vector_type(4))) float f32x4;
typedef __attribute__((ext_vector_type(2))) float f32x2;
typedef __attribute__((ext_vector_type(4))) unsigned u32x4;
typedef __attribute__((ext_vector_type(2))) unsigned u32x2;

constexpr int S_ = 2048, DM = 576, NH = 32, DH = 128, FF = 4608, NSEG = 64, KS = 18;
constexpr int NB = 72, NF = 96;
constexpr u32 HO_OFF = 0;
constexpr u32 PT_OFF = HO_OFF + NSEG * 65536;
constexpr u32 PT_STEP = 4 * 20480;
constexpr u32 FLG_OFF = PT_OFF + NSEG * PT_STEP;
constexpr u32 FLM_OFF = FLG_OFF + NSEG * 128;
constexpr u32 FLQ_OFF = FLM_OFF + NSEG * 3072;
constexpr u32 FLP_OFF = FLQ_OFF + NSEG * 128;
constexpr u32 POISON_BYTES = FLP_OFF + 256;
constexpr u32 CTL_OFF = (POISON_BYTES + 4095) & ~4095u;
constexpr u32 M0_OFF = CTL_OFF + 4096;
constexpr u32 XF_OFF = M0_OFF + 12288;
constexpr u32 XF_SEG = 2 * KS * 1024;
constexpr u32 WVO_OFF = XF_OFF + NSEG * XF_SEG;
constexpr u32 WVO_J = 16 * KS * 1024;
constexpr u32 UX_OFF = WVO_OFF + NB * WVO_J;
constexpr u32 UM_OFF = UX_OFF + NSEG * NB * 16384;
constexpr u32 QX_OFF = UM_OFF + NSEG * NB * 4096;
constexpr u32 H_OFF = QX_OFF + NSEG * NH * 16384;
constexpr u32 WDF_OFF = H_OFF + 128 * 144 * 1024;
constexpr u32 FO_OFF = WDF_OFF + 9 * 144 * 4096;
constexpr u32 AO_OFF = FO_OFF + S_ * DM * 2;
constexpr u32 WS_END = AO_OFF + S_ * DM * 2;
constexpr u32 POISON = 0xFFFFFFFFu;
constexpr int SC1 = 16;
constexpr u32 SPIN_CAP = 400000u;
constexpr int LDS_BYTES = 160 * 1024;

__device__ __forceinline__ u32 pk(float lo, float hi) {
  f32x2 v = {lo, hi};
  return __builtin_bit_cast(u32, __builtin_convertvector(v, bf16x2));
}
__device__ __forceinline__ u32 pks(float lo, float hi) { u32 r = pk(lo, hi); return r == POISON ? 0x7FC07FC0u : r; }
__device__ __forceinline__ float bfr(float x) { return (float)(__bf16)x; }
__device__ __forceinline__ float bflo(u32 w) { return __uint_as_float(w << 16); }
__device__ __forceinline__ float bfhi(u32 w) { return __uint_as_float(w & 0xFFFF0000u); }
__device__ __forceinline__ f32x4 mfma(u32x4 a, u32x4 b, f32x4 c) {
  return __builtin_amdgcn_mfma_f32_16x16x32_bf16(__builtin_bit_cast(bf16x8, a), __builtin_bit_cast(bf16x8, b), c, 0, 0, 0);
}
__device__ __forceinline__ u32 plo(u32 a, u32 b) { return __builtin_amdgcn_perm(b, a, 0x05040100u); }
__device__ __forceinline__ u32 phi(u32 a, u32 b) { return __builtin_amdgcn_perm(b, a, 0x07060302u); }
__device__ __forceinline__ float vmax(float a, float b) { float r; asm("v_max_f32 %0, %1, %2" : "=v"(r) : "v"(a), "v"(b)); return r; }
__device__ __forceinline__ float vmax3(float a, float b, float c) { float r; asm("v_max3_f32 %0, %1, %2, %3" : "=v"(r) : "v"(a), "v"(b), "v"(c)); return r; }
__device__ __forceinline__ float xmax(float x) {
  auto s = __builtin_amdgcn_permlane16_swap(__float_as_uint(x), __float_as_uint(x), false, false);
  x = vmax(__uint_as_float(s[0]), __uint_as_float(s[1]));
  auto t = __builtin_amdgcn_permlane32_swap(__float_as_uint(x), __float_as_uint(x), false, false);
  return vmax(__uint_as_float(t[0]), __uint_as_float(t[1]));
}
__device__ __forceinline__ float xsum(float x) {
  auto s = __builtin_amdgcn_permlane16_swap(__float_as_uint(x), __float_as_uint(x), false, false);
  x = __uint_as_float(s[0]) + __uint_as_float(s[1]);
  auto t = __builtin_amdgcn_permlane32_swap(__float_as_uint(x), __float_as_uint(x), false, false);
  return __uint_as_float(t[0]) + __uint_as_float(t[1]);
}
__device__ __forceinline__ float ror8(float x) {
  return __builtin_bit_cast(float, __builtin_amdgcn_mov_dpp(__builtin_bit_cast(int, x), 0x128, 0xf, 0xf, true));
}
__device__ __forceinline__ u32 mx(u32 a, u32 b) { return a > b ? a : b; }
__device__ __forceinline__ u32 mx4(u32x4 v) { return mx(mx(v.x, v.y), mx(v.z, v.w)); }
__device__ __forceinline__ u32 mx2(u32x2 v) { return mx(v.x, v.y); }
__device__ __forceinline__ bool allok(u32 m) { return __all(m != POISON); }
__device__ __forceinline__ void glds16(const void* g, u32 lds) {
  u32 keep;
  asm volatile("s_mov_b32 %0, m0\n\ts_mov_b32 m0, %2\n\ts_nop 0\n\tglobal_load_lds_dwordx4 %1, off\n\ts_mov_b32 m0, %0"
               : "=&s"(keep) : "v"(g), "s"(lds) : "memory");
}
__device__ __forceinline__ void wait_vm() { asm volatile("s_waitcnt vmcnt(0)" ::: "memory"); }
__device__ __forceinline__ void bar() {
  asm volatile("s_waitcnt lgkmcnt(0)" ::: "memory");
  __builtin_amdgcn_s_barrier();
  asm volatile("" ::: "memory");
}
__device__ __forceinline__ bool spin_fail(u32& spins, u32* tmo) {
  ++spins;
  if (spins > SPIN_CAP) { __hip_atomic_store(tmo, 1u, __ATOMIC_RELAXED, __HIP_MEMORY_SCOPE_AGENT); return true; }
  if ((spins & 1023u) == 0u && __hip_atomic_load(tmo, __ATOMIC_RELAXED, __HIP_MEMORY_SCOPE_AGENT) != 0u) return true;
  return false;
}
__device__ __forceinline__ u32 homap(u32 lo, u32 flip) { return ((lo & ~8191u) << 1) + (lo & 8191u) + flip; }
constexpr u32 HO_PM = 16384, HO_M = 20480;

#define STAMP(step, k) do {} while (0)
#define STAMPC(step, k) do {} while (0)
#define DUMP() do {} while (0)
struct Args {
  const u16* x; const u16* mem0; const u16* wq; const u16* wk; const u16* wv; const u16* wo;
  const u16* wg; const u16* wu; const u16* wd; unsigned char* ws; u16* out;
};

constexpr int K1_WVO = 2592, K1_LN = 256, K1_LNB = K1_LN / 4, K1_JOBS = K1_WVO + K1_LN, K1_GRID = K1_JOBS / 4 + 8;
__global__ void __launch_bounds__(256) k1_prep(Args a) {
  const int lane = threadIdx.x & 63, wave = __builtin_amdgcn_readfirstlane(threadIdx.x >> 6);
  const int b = blockIdx.x;
  unsigned char* ws = a.ws;
  const int c16 = lane & 15, g = lane >> 4;
  if (b == K1_GRID - 1) {
    if (threadIdx.x == 0) { ((u32*)(ws + CTL_OFF))[64] = 0u; ((u32*)(ws + CTL_OFF))[128] = 0u; }
    for (int i = threadIdx.x; i < 576; i += 256) {
      u32x4 v = *(const u32x4*)(a.mem0 + (i & 7) * DM + (i >> 3) * 8);
      if (v.x == POISON) v.x = 0x7FC07FC0u; if (v.y == POISON) v.y = 0x7FC07FC0u;
      if (v.z == POISON) v.z = 0x7FC07FC0u; if (v.w == POISON) v.w = 0x7FC07FC0u;
      *(u32x4*)(ws + M0_OFF + i * 16) = v;
    }
    return;
  }
  if (b >= K1_JOBS / 4) return;
  const int job = b * 4 + wave;
  auto poison = [&]() {
    const u32x4 pv = {POISON, POISON, POISON, POISON};
    for (u32 o = (job * 64 + lane) * 16; o < POISON_BYTES; o += K1_JOBS * 64 * 16) *(u32x4*)(ws + o) = pv;
  };
  if (b >= K1_LNB) {
    const int jx = ((b - K1_LNB) >> 3) * 4 + wave, h = (b & 7) * 4 + jx / 81, kp = (jx / 9) % 9, bb = jx % 9;
    u32x2 rw[4][8];
    u32x4 af[2][2][4];
#pragma unroll
    for (int es = 0; es < 4; ++es)
#pragma unroll
      for (int jj = 0; jj < 8; ++jj)
        rw[es][jj] = *(const u32x2*)(a.wo + (size_t)(h * DH + es * 32 + g * 8 + jj) * DM + bb * 64 + c16 * 4);
#pragma unroll
    for (int i = 0; i < 16; ++i) {
      const int k = (kp * 2 + (i >> 3)) * 32 + (c16 >> 2) * 8 + (c16 & 3) + ((i >> 2) & 1) * 4;
      af[i >> 3][(i >> 2) & 1][i & 3] = *(const u32x4*)(a.wv + (size_t)k * (NH * DH) + h * DH + (i & 3) * 32 + g * 8);
    }
    poison();
    __builtin_amdgcn_sched_barrier(0);
    u32x4 bf[4][4];
#pragma unroll
    for (int es = 0; es < 4; ++es)
#pragma unroll
      for (int p = 0; p < 4; ++p) {
        bf[es][0][p] = plo(rw[es][2 * p].x, rw[es][2 * p + 1].x); bf[es][1][p] = phi(rw[es][2 * p].x, rw[es][2 * p + 1].x);
        bf[es][2][p] = plo(rw[es][2 * p].y, rw[es][2 * p + 1].y); bf[es][3][p] = phi(rw[es][2 * p].y, rw[es][2 * p + 1].y);
      }
#pragma unroll
    for (int tt = 0; tt < 2; ++tt) {
      const int t = kp * 2 + tt;
      f32x4 acc[2][4];
#pragma unroll
      for (int rt = 0; rt < 2; ++rt)
#pragma unroll
        for (int i = 0; i < 4; ++i) {
          acc[rt][i] = f32x4{0.f, 0.f, 0.f, 0.f};
#pragma unroll
          for (int es = 0; es < 4; ++es) acc[rt][i] = mfma(af[tt][rt][es], bf[es][i], acc[rt][i]);
        }
      const int j = bb * 8 + (c16 >> 1);
      unsigned char* dst = ws + WVO_OFF + (size_t)j * WVO_J + ((h >> 1) * KS + t) * 1024 + (g * 16 + (h & 1) * 8 + (c16 & 1) * 4) * 16;
#pragma unroll
      for (int i = 0; i < 4; ++i) {
        u32x4 v = {pk(acc[0][i][0], acc[0][i][1]), pk(acc[0][i][2], acc[0][i][3]), pk(acc[1][i][0], acc[1][i][1]), pk(acc[1][i][2], acc[1][i][3])};
        *(u32x4*)(dst + i * 16) = v;
      }
    }
  } else {
    u32x4 vv[2][5];
#pragma unroll
    for (int rr = 0; rr < 2; ++rr)
#pragma unroll
    for (int i = 0; i < 5; ++i) {
      vv[rr][i] = u32x4{0, 0, 0, 0};
      if (i < 4 || c16 < 8) vv[rr][i] = *(const u32x4*)(a.x + (size_t)(job * 8 + rr * 4 + g) * DM + (i * 16 + c16) * 8);
    }
    poison();
#pragma unroll
    for (int rr = 0; rr < 2; ++rr) {
    const int row = job * 8 + rr * 4 + g;
    const u32x4 (&v)[5] = vv[rr];
    float sm = 0.f;
#pragma unroll
    for (int i = 0; i < 20; ++i) sm += bflo(v[i >> 2][i & 3]) + bfhi(v[i >> 2][i & 3]);
#pragma unroll
    for (int o = 1; o < 16; o <<= 1) sm += __shfl_xor(sm, o);
    const float mean = sm * (1.f / DM);
    float q = 0.f;
#pragma unroll
    for (int i = 0; i < 20; ++i) {
      const float d0 = bflo(v[i >> 2][i & 3]) - mean, d1 = bfhi(v[i >> 2][i & 3]) - mean;
      q += (i < 16 || c16 < 8) ? d0 * d0 + d1 * d1 : 0.f;
    }
#pragma unroll
    for (int o = 1; o < 16; o <<= 1) q += __shfl_xor(q, o);
    const float mb = bfr(mean), den = bfr(sqrtf(bfr(bfr(q * (1.f / DM)) + 1e-5f)));
#pragma unroll
    for (int i = 0; i < 5; ++i) {
      u32x4 o;
#pragma unroll
      for (int k = 0; k < 4; ++k) o[k] = pk(bfr(bflo(v[i][k]) - mb) / den, bfr(bfhi(v[i][k]) - mb) / den);
      const int ch = i * 16 + c16;
      if (i < 4 || c16 < 8) *(u32x4*)(ws + XF_OFF + ((row >> 4) * KS + (ch >> 2)) * 1024 + ((ch & 3) * 16 + (row & 15)) * 16) = o;
    }
    }
  }
}

constexpr u32 L_X = 0;
constexpr u32 L_M = 2 * XF_SEG;
constexpr u32 L_QM = L_M + 9216;
constexpr u32 L_QX = L_QM + 8192;

template <bool WA, int ST>
__device__ __forceinline__ void proj2(const u32x4 (&W0)[KS], const u32x4 (&W1)[KS], const char* xb, f32x4& c0, f32x4& c1) {
  u32x4 xf[KS];
#pragma unroll
  for (int t = 0; t < KS; ++t) xf[t] = *(const u32x4*)(xb + t * ST);
  c0 = f32x4{0.f, 0.f, 0.f, 0.f}; c1 = c0;
#pragma unroll
  for (int t = 0; t < KS; ++t) {
    c0 = WA ? mfma(W0[t], xf[t], c0) : mfma(xf[t], W0[t], c0);
    c1 = WA ? mfma(W1[t], xf[t], c1) : mfma(xf[t], W1[t], c1);
  }
  __builtin_amdgcn_sched_group_barrier(0x100, 6, 0);
#pragma unroll
  for (int t = 0; t < KS - 6; ++t) { __builtin_amdgcn_sched_group_barrier(0x008, 2, 0); __builtin_amdgcn_sched_group_barrier(0x100, 1, 0); }
  __builtin_amdgcn_sched_group_barrier(0x008, 12, 0);
}
__device__ __forceinline__ void stage_x(const unsigned char* ws, int seg, int wave, int lane) {
  const unsigned char* src = ws + XF_OFF + seg * XF_SEG + lane * 16;
  const u32 dst = L_X + (seg & 1) * XF_SEG;
#pragma unroll
  for (int i = 0; i < 5; ++i) {
    const int p = i * 8 + wave;
    if (p < 36) glds16(src + p * 1024, dst + p * 1024);
  }
}
__device__ __forceinline__ u32 get_flips(__amdgpu_buffer_rsrc_t r, int lane, u32* tmo) {
  u32 f, spins = 0;
  for (;;) {
    f = __builtin_amdgcn_raw_buffer_load_b32(r, FLP_OFF + lane * 4, 0, SC1);
    if (allok(f)) break;
    if (spin_fail(spins, tmo)) break;
    __builtin_amdgcn_s_sleep(8);
  }
  return f & 8192u;
}
__device__ __forceinline__ void gather_m(__amdgpu_buffer_rsrc_t r, u32 base, u32 flip, bool first, char* smem, int wave, int lane, u32* tmo) {
  const u32 l0 = (wave * 64 + lane) * 16, l1 = (512 + wave * 8 + (lane & 7)) * 16;
  const u32 o0 = first ? M0_OFF + l0 : base + homap(HO_M + l0, flip), o1 = first ? M0_OFF + l1 : base + homap(HO_M + l1, flip);
  u32 spins = 0;
  u32x4 v, w;
  for (;;) {
    v = __builtin_amdgcn_raw_buffer_load_b128(r, o0, 0, SC1);
    w = __builtin_amdgcn_raw_buffer_load_b128(r, o1, 0, SC1);
    if (allok(mx(mx4(v), mx4(w)))) break;
    if (spin_fail(spins, tmo)) break;
  }
  *(u32x4*)(smem + L_M + l0) = v;
  if (lane < 8) *(u32x4*)(smem + L_M + l1) = w;
  wait_vm();
}

__device__ __forceinline__ void load_wqk(const Args& a, int h, u32x4 (&W)[2][KS], char* smem) {
  const int tid = threadIdx.x, lane = tid & 63, wave = __builtin_amdgcn_readfirstlane(tid >> 6);
  const int c16 = lane & 15, g = lane >> 4;
  {
    u32x4 st[18];
    auto ldw = [&](int hf) {
#pragma unroll
      for (int i = 0; i < 18; ++i) {
        const int pc = i * 512 + tid;
        st[i] = *(const u32x4*)(((pc & 16) ? a.wk : a.wq) + (size_t)(hf * 288 + (pc >> 5)) * (NH * DH) + h * DH + (pc & 15) * 8);
      }
    };
    ldw(0);
#pragma unroll
    for (int hf = 0; hf < 2; ++hf) {
#pragma unroll
      for (int i = 0; i < 18; ++i) { const int pc = i * 512 + tid; *(u32x4*)(smem + (pc >> 5) * 528 + (pc & 31) * 16) = st[i]; }
      if (hf == 0) ldw(1);
      bar();
      const char* wl = smem + g * 8 * 528 + wave * 64 + c16 * 4;
#pragma unroll
      for (int t = 0; t < 9; ++t) {
#pragma unroll
        for (int p = 0; p < 4; ++p) {
          const u32 d0 = *(const u32*)(wl + (t * 32 + 2 * p) * 528), d1 = *(const u32*)(wl + (t * 32 + 2 * p + 1) * 528);
          W[0][hf * 9 + t][p] = plo(d0, d1); W[1][hf * 9 + t][p] = phi(d0, d1);
        }
        asm volatile("" : "+v"(W[0][hf * 9 + t]), "+v"(W[1][hf * 9 + t]) :: "memory");
      }
      bar();
    }
  }
}

__device__ __forceinline__ void role_a(const Args& a, int h, char* smem) {
  const int tid = threadIdx.x, lane = tid & 63, wave = __builtin_amdgcn_readfirstlane(tid >> 6);
  const int c16 = lane & 15, g = lane >> 4;
  unsigned char* ws = a.ws;
  u32* tmo = (u32*)(ws + CTL_OFF) + 64;
  __amdgpu_buffer_rsrc_t r = __builtin_amdgcn_make_buffer_rsrc(ws, 0, WS_END, 0x00020000);
  STAMP(64, 0);
  u32x4 W[2][KS];
  load_wqk(a, h, W, smem);
  const u32 flips = get_flips(r, lane, tmo);
  const u32 lx = (((h >> 1) * 4 + g) * 16 + (h & 1) * 8 + (lane & 7)) * 16, lm = HO_PM + h * 128 + g * 64 + (lane & 7) * 8;
  STAMP(64, 1); STAMPC(65, 0); STAMP(65, 1);
  u32x4 kf[8];
  u32 fl = POISON;
  auto q_load = [&](int seg) {
    const u32 fo = FLQ_OFF + seg * 128 + h * 4;
    u32 spins = 0;
    while (!allok(fl)) {
      fl = __builtin_amdgcn_raw_buffer_load_b32(r, fo, 0, SC1);
      if (spin_fail(spins, tmo)) break;
    }
    asm volatile("" ::: "memory");
    const u32 qo = QX_OFF + ((seg * NH + h) * 16 + (wave - 1) * 4) * 1024 + lane * 16;
#pragma unroll
    for (int w = 0; w < 4; ++w) kf[w] = __builtin_amdgcn_raw_buffer_load_b128(r, qo + w * 1024, 0, SC1);
    if (wave == 3) {
#pragma unroll
      for (int w = 4; w < 8; ++w) kf[w] = __builtin_amdgcn_raw_buffer_load_b128(r, qo + w * 1024, 0, SC1);
#pragma unroll
      for (int w = 0; w < 8; ++w) *(u32x4*)(smem + L_QX + (seg & 1) * 16384 + 8192 + w * 1024 + lane * 16) = kf[w];
    }
    fl = seg + 1 < NSEG ? __builtin_amdgcn_raw_buffer_load_b32(r, fo + 128, 0, SC1) : 0u;
  };
  if (wave >= 1 && wave <= 3) q_load(0);
  for (int s = 0; s < NSEG; ++s) {
    const u32 flip = __builtin_amdgcn_readlane(flips, s), base = HO_OFF + s * 65536;
    STAMP(s, 0);
    gather_m(r, base, flip, s == 0, smem, wave, lane, tmo);
    STAMP(s, 1);
    bar();
    STAMP(s, 2);
    {
      f32x4 c0, c1;
      proj2<true, 512>(W[0], W[1], smem + L_M + g * 128 + (lane & 7) * 16, c0, c1);
      u32x4 o = {pk(c0[0], c1[0]), pk(c0[1], c1[1]), pk(c0[2], c1[2]), pk(c0[3], c1[3])};
      *(u32x4*)(smem + L_QM + wave * 1024 + lane * 16) = o;
    }
    if (wave == 0) {
      __builtin_amdgcn_s_setprio(3);
#pragma unroll
      for (int w = 0; w < 8; ++w) kf[w] = *(const u32x4*)(smem + L_QX + (s & 1) * 16384 + 8192 + lane * 16 + w * 1024);
    }
    bar();
    STAMP(s, 3);
    const float C2 = 0.08838834764831845f * 1.4426950408889634f;
    if (wave == 0) {
      if (s < NSEG - 1) {
        const char* qb = smem + L_QM + lane * 16;
        f32x4 s0 = {0.f, 0.f, 0.f, 0.f}, s1 = s0, s2 = s0;
#pragma unroll
        for (int w = 0; w < 4; ++w) {
          const u32x4 qf = *(const u32x4*)(qb + w * 1024);
          s2 = mfma(*(const u32x4*)(qb + 4096 + w * 1024), qf, s2);
          s0 = mfma(kf[w], qf, s0);
          s1 = mfma(kf[4 + w], qf, s1);
        }
        const bool hi = c16 >= 8;
        float v[6];
#pragma unroll
        for (int i = 0; i < 4; ++i) v[i] = hi ? s1[i] : s0[i];
        v[4] = g < 2 ? (hi ? s2[2] : s2[0]) : -INFINITY;
        v[5] = g < 2 ? (hi ? s2[3] : s2[1]) : -INFINITY;
        float m = vmax(vmax3(v[0], v[1], v[2]), vmax3(v[3], v[4], v[5]));
        m = xmax(m);
        m = vmax(m, ror8(m)) * C2;
#pragma unroll
        for (int i = 0; i < 6; ++i) v[i] = __builtin_amdgcn_exp2f(__builtin_fmaf(v[i], C2, -m));
        float sum = ((v[0] + v[1]) + (v[2] + v[3])) + (v[4] + v[5]);
        sum += ror8(sum);
        const float inv = __builtin_amdgcn_rcpf(xsum(sum));
        const u32x2 px = {pk(v[0] * inv, v[1] * inv) & 0x7FFF7FFFu, pk(v[2] * inv, v[3] * inv) & 0x7FFF7FFFu};
        const u32 pm = pk(v[4] * inv, v[5] * inv) & 0x7FFF7FFFu;
        __builtin_amdgcn_raw_buffer_store_b64(px, r, base + homap(lx + (c16 >> 3) * 8, flip), 0, SC1);
        if (g < 2) __builtin_amdgcn_raw_buffer_store_b32(pm, r, base + homap(lm + (c16 >> 3) * 4, flip), 0, SC1);
      }
      __builtin_amdgcn_s_setprio(0);
    } else if (wave < 3) {
      const char* qx = smem + L_QX + (s & 1) * 16384 + lane * 16;
      f32x4 s0 = {0.f, 0.f, 0.f, 0.f}, s1 = s0, s2 = s0;
#pragma unroll
      for (int w = 0; w < 4; ++w) {
        s2 = mfma(*(const u32x4*)(smem + L_QM + 4096 + lane * 16 + w * 1024), kf[w], s2);
        s0 = mfma(*(const u32x4*)(qx + 8192 + w * 1024), kf[w], s0);
        s1 = mfma(*(const u32x4*)(qx + 12288 + w * 1024), kf[w], s1);
      }
      const int qi = (wave - 1) * 16 + c16;
      const float NEG = -1.0e9f * 1.4426950408889634f;
      float v[12];
#pragma unroll
      for (int i = 0; i < 4; ++i) {
        v[i] = (4 * g + i <= qi) ? s0[i] * C2 : NEG;
        v[4 + i] = (16 + 4 * g + i <= qi) ? s1[i] * C2 : NEG;
        v[8 + i] = g < 2 ? s2[i] * C2 : -INFINITY;
      }
      float m = v[0];
#pragma unroll
      for (int i = 1; i < 12; ++i) m = fmaxf(m, v[i]);
      m = xmax(m);
      float sum = 0.f;
#pragma unroll
      for (int i = 0; i < 12; ++i) { v[i] = __builtin_amdgcn_exp2f(v[i] - m); sum += v[i]; }
      const float inv = __builtin_amdgcn_rcpf(xsum(sum));
      u32x4 px; u32x2 pm;
#pragma unroll
      for (int i = 0; i < 4; ++i) px[i] = pk(v[2 * i] * inv, v[2 * i + 1] * inv) & 0x7FFF7FFFu;
#pragma unroll
      for (int i = 0; i < 2; ++i) pm[i] = pk(v[8 + 2 * i] * inv, v[9 + 2 * i] * inv) & 0x7FFF7FFFu;
      const u32 tb = PT_OFF + s * PT_STEP + ((wave - 1) * 2 + (c16 >> 3)) * 20480;
      __builtin_amdgcn_raw_buffer_store_b128(px, r, tb + lx, 0, SC1);
      if (g < 2) __builtin_amdgcn_raw_buffer_store_b64(pm, r, tb + lm, 0, SC1);
    }
    STAMP(s, 4);
    if (wave >= 1 && wave <= 3 && s + 1 < NSEG) q_load(s + 1);
    bar();
  }
  STAMPC(65, 2); STAMP(65, 3);
}

__device__ __forceinline__ void role_b(const Args& a, int j, char* smem) {
  const int tid = threadIdx.x, lane = tid & 63, wave = __builtin_amdgcn_readfirstlane(tid >> 6);
  const int c16 = lane & 15, g = lane >> 4;
  unsigned char* ws = a.ws;
  u32* tmo = (u32*)(ws + CTL_OFF) + 64;
  __amdgpu_buffer_rsrc_t r = __builtin_amdgcn_make_buffer_rsrc(ws, 0, WS_END, 0x00020000);
  STAMP(64, 0);
  u32x4 W[2][KS];
  {
    const unsigned char* wsrc = ws + WVO_OFF + (size_t)j * WVO_J + wave * 2 * KS * 1024 + lane * 16;
#pragma unroll
    for (int tt = 0; tt < 2; ++tt)
#pragma unroll
      for (int t = 0; t < KS; ++t) W[tt][t] = *(const u32x4*)(wsrc + (tt * KS + t) * 1024);
  }
  wait_vm();
  bar();
  const u32 flips = get_flips(r, lane, tmo);
  STAMP(64, 1);
  u32x4 nx0, nx1;
  u32 fl = POISON;
  auto ux_load = [&](int seg) {
    const u32 fo = FLG_OFF + seg * 128 + wave * 16 + (lane & 3) * 4;
    u32 spins = 0;
    while (!allok(fl)) {
      fl = __builtin_amdgcn_raw_buffer_load_b32(r, fo, 0, SC1);
      if (spin_fail(spins, tmo)) break;
    }
    asm volatile("" ::: "memory");
    const u32 uo = UX_OFF + ((seg * NB + j) * 16 + wave * 2) * 1024 + lane * 16;
    nx0 = __builtin_amdgcn_raw_buffer_load_b128(r, uo, 0, SC1);
    nx1 = __builtin_amdgcn_raw_buffer_load_b128(r, uo + 1024, 0, SC1);
    fl = seg + 1 < NSEG ? __builtin_amdgcn_raw_buffer_load_b32(r, fo + 128, 0, SC1) : 0u;
  };
  const u32 lx = wave * 2048 + lane * 16;
  const u32 lm = HO_PM + (wave * 4 + (c16 >> 3)) * 128 + (g & 1) * 64 + (lane & 7) * 8;
  const bool diag = (c16 >> 3) == (g >> 1);
  const u32 redo = ((wave * 2 + (g >> 1)) * 64 + (lane & 7) * 8 + (g & 1) * 4) * 4;
  for (int s = 0; s < NSEG; ++s) {
    const u32 flip = __builtin_amdgcn_readlane(flips, s), base = HO_OFF + s * 65536;
    STAMP(s, 0);
    gather_m(r, base, flip, s == 0, smem, wave, lane, tmo);
    bar();
    STAMP(s, 1);
    u32x4 um;
    {
      f32x4 c0, c1;
      proj2<false, 512>(W[0], W[1], smem + L_M + g * 128 + (lane & 7) * 16, c0, c1);
      um = u32x4{pk(c0[0], c0[1]), pk(c0[2], c0[3]), pk(c1[0], c1[1]), pk(c1[2], c1[3])};
      if (g < 2) __builtin_amdgcn_raw_buffer_store_b128(um, r, UM_OFF + ((s * NB + j) * 8 + wave) * 512 + lane * 16, 0, SC1);
      else um = u32x4{0, 0, 0, 0};
    }
    if (s + 1 < NSEG) {
      if (s == 0) ux_load(0);
      STAMP(s, 2);
      const u32 o0 = base + homap(lx, flip), o1 = o0 + 1024, o2 = base + homap(lm, flip), o3 = o2 + 256;
      u32x4 p0, p1; u32x2 q0, q1;
      u32 spins = 0;
      for (;;) {
        p0 = __builtin_amdgcn_raw_buffer_load_b128(r, o0, 0, SC1);
        p1 = __builtin_amdgcn_raw_buffer_load_b128(r, o1, 0, SC1);
        q0 = __builtin_amdgcn_raw_buffer_load_b64(r, o2, 0, SC1);
        q1 = __builtin_amdgcn_raw_buffer_load_b64(r, o3, 0, SC1);
        if (allok(mx(mx(mx4(p0), mx4(p1)), mx(mx2(q0), mx2(q1))))) break;
        if (spin_fail(spins, tmo)) break;
      }
      STAMP(s, 3);
      f32x4 d = {0.f, 0.f, 0.f, 0.f}, e = d;
      const u32x4 pmv = {q0.x, q0.y, q1.x, q1.y};
      d = mfma(nx0, p0, d); e = mfma(nx1, p1, e); d = mfma(um, pmv, d);
      d += e;
      if (diag) *(f32x4*)(smem + L_QM + redo) = d;
    }
    wait_vm();
    bar();
    if (wave == 0 && s + 1 < NSEG) {
      const char* rp = smem + L_QM + (lane >> 5) * 2048 + (lane & 31) * 8;
      f32x2 acc = *(const f32x2*)rp;
#pragma unroll
      for (int p = 1; p < 8; ++p) acc += *(const f32x2*)(rp + p * 256);
      auto t0 = __builtin_amdgcn_permlane32_swap(__float_as_uint(acc.x), __float_as_uint(acc.x), false, false);
      auto t1 = __builtin_amdgcn_permlane32_swap(__float_as_uint(acc.y), __float_as_uint(acc.y), false, false);
      const u32 o = pks(__uint_as_float(t0[0]) + __uint_as_float(t0[1]), __uint_as_float(t1[0]) + __uint_as_float(t1[1]));
      const u32 nflip = __builtin_amdgcn_readlane(flips, s + 1);
      if (lane < 32) __builtin_amdgcn_raw_buffer_store_b32(o, r, base + 65536 + homap(HO_M + j * 128 + lane * 4, nflip), 0, SC1);
    }
    STAMP(s, 4);
    if (tid == 64) __builtin_amdgcn_raw_buffer_store_b32(1u, r, FLM_OFF + s * 3072 + (j / 6) * 128 + (j % 6) * 4, 0, SC1);
    if (s + 2 < NSEG) ux_load(s + 1);
    STAMP(s, 5);
  }
  STAMP(65, 0);
}

__device__ __forceinline__ void role_o(const Args& a, int o24, char* smem) {
  const int tid = threadIdx.x, lane = tid & 63, wave = __builtin_amdgcn_readfirstlane(tid >> 6);
  const int c16 = lane & 15, g = lane >> 4, o = o24 >> 1;
  unsigned char* ws = a.ws;
  u32* tmo = (u32*)(ws + CTL_OFF) + 64;
  __amdgpu_buffer_rsrc_t r = __builtin_amdgcn_make_buffer_rsrc(ws, 0, WS_END, 0x00020000);
  STAMP(64, 0);
  if (wave < 3 && o24 * 3 + wave < NSEG) {
    const int blk = o24 * 3 + wave;
    u32 best[2] = {~0u, ~0u};
    for (int rep = 0; rep < 4; ++rep)
      for (int f = 0; f < 2; ++f) {
        u32* p = (u32*)(ws + HO_OFF + blk * 65536 + homap(32768 - 128 * (rep + 1), f * 8192));
        __hip_atomic_store(p, POISON, __ATOMIC_RELAXED, __HIP_MEMORY_SCOPE_AGENT);
        wait_vm();
        u64 t0, t1;
        asm volatile("s_memtime %0\n\ts_waitcnt lgkmcnt(0)" : "=s"(t0) :: "memory");
        u32 v = __hip_atomic_load(p, __ATOMIC_RELAXED, __HIP_MEMORY_SCOPE_AGENT);
        asm volatile("s_waitcnt vmcnt(0)" : "+v"(v) :: "memory");
        asm volatile("s_memtime %0\n\ts_waitcnt lgkmcnt(0)" : "=s"(t1) :: "memory");
        const u32 d = (u32)(t1 - t0);
        best[f] = d < best[f] ? d : best[f];
      }
    if (lane == 0) __builtin_amdgcn_raw_buffer_store_b32(best[1] < best[0] ? 8192u : 0u, r, FLP_OFF + blk * 4, 0, SC1);
  }
  STAMP(64, 1);
  const u32 lx = wave * 2048 + lane * 16;
  const u32 lm = HO_PM + (wave * 4 + (c16 >> 3)) * 128 + (g & 1) * 64 + (lane & 7) * 8;
  const bool diag = (c16 >> 3) == (g >> 1);
  const u32 redo = ((wave * 2 + (g >> 1)) * 64 + (lane & 7) * 8 + (g & 1) * 4) * 4;
  for (int s = o24 & 1; s < NSEG; s += 2) {
    u32 spins = 0;
    while (!allok(__builtin_amdgcn_raw_buffer_load_b32(r, FLG_OFF + s * 128 + wave * 16 + (lane & 3) * 4, 0, SC1))) {
      if (spin_fail(spins, tmo)) break;
      __builtin_amdgcn_s_sleep(16);
    }
    asm volatile("" ::: "memory");
    u32x4 x0[6], x1[6], um[6];
#pragma unroll
    for (int c = 0; c < 6; ++c) {
      const u32 uo = UX_OFF + ((s * NB + o * 6 + c) * 16 + wave * 2) * 1024 + lane * 16;
      x0[c] = __builtin_amdgcn_raw_buffer_load_b128(r, uo, 0, SC1);
      x1[c] = __builtin_amdgcn_raw_buffer_load_b128(r, uo + 1024, 0, SC1);
    }
    if (wave == 0) {
      while (!allok(__builtin_amdgcn_raw_buffer_load_b32(r, FLM_OFF + s * 3072 + o * 128 + (lane % 6) * 4, 0, SC1))) {
        if (spin_fail(spins, tmo)) break;
        __builtin_amdgcn_s_sleep(4);
      }
    }
    bar();
#pragma unroll
    for (int c = 0; c < 6; ++c)
      um[c] = __builtin_amdgcn_raw_buffer_load_b128(r, UM_OFF + ((s * NB + o * 6 + c) * 8 + wave) * 512 + (lane & 31) * 16, 0, SC1);
    u32x4 p0[4], p1[4]; u32x2 q0[4], q1[4];
    for (;;) {
      u32 m = 0;
#pragma unroll
      for (int u = 0; u < 4; ++u) {
        const u32 tb = PT_OFF + s * PT_STEP + u * 20480;
        p0[u] = __builtin_amdgcn_raw_buffer_load_b128(r, tb + lx, 0, SC1);
        p1[u] = __builtin_amdgcn_raw_buffer_load_b128(r, tb + lx + 1024, 0, SC1);
        q0[u] = __builtin_amdgcn_raw_buffer_load_b64(r, tb + lm, 0, SC1);
        q1[u] = __builtin_amdgcn_raw_buffer_load_b64(r, tb + lm + 256, 0, SC1);
      }
#pragma unroll
      for (int u = 0; u < 4; ++u) m = mx(m, mx(mx(mx4(p0[u]), mx4(p1[u])), mx(mx2(q0[u]), mx2(q1[u]))));
      if (allok(m)) break;
      if (spin_fail(spins, tmo)) break;
    }
#pragma unroll
    for (int c = 0; c < 6; ++c) {
      if (g >= 2) um[c] = u32x4{0, 0, 0, 0};
#pragma unroll
      for (int u = 0; u < 4; ++u) {
        f32x4 d = {0.f, 0.f, 0.f, 0.f};
        const u32x4 pmv = {q0[u].x, q0[u].y, q1[u].x, q1[u].y};
        d = mfma(x0[c], p0[u], d); d = mfma(x1[c], p1[u], d); d = mfma(um[c], pmv, d);
        if (diag) *(f32x4*)(smem + (c * 4 + u) * 4096 + redo) = d;
      }
    }
    bar();
    if (tid < 384) {
      const char* rp = smem + (tid >> 4) * 4096 + (tid & 15) * 16;
      f32x4 acc = *(const f32x4*)rp;
#pragma unroll
      for (int p = 1; p < 16; ++p) acc += *(const f32x4*)(rp + p * 256);
      *(u32x2*)(ws + AO_OFF + ((size_t)(s * 32 + ((tid & 63) >> 1)) * DM + (o * 6 + (tid >> 6)) * 8 + (tid & 1) * 4) * 2) = u32x2{pk(acc[0], acc[1]), pk(acc[2], acc[3])};
    }
    STAMP(s, 0);
  }
}

__device__ __forceinline__ void role_v(const Args& a, int h, char* smem) {
  const int tid = threadIdx.x, lane = tid & 63, wave = __builtin_amdgcn_readfirstlane(tid >> 6);
  const int c16 = lane & 15, g = lane >> 4;
  unsigned char* ws = a.ws;
  __amdgpu_buffer_rsrc_t r = __builtin_amdgcn_make_buffer_rsrc(ws, 0, WS_END, 0x00020000);
  STAMP(64, 0);
  u32x4 WV[KS], WO[5][4];
  {
    const u32 sel = (c16 & 1) ? 0x07060302u : 0x05040100u;
    u32x4 st[18];
#pragma unroll
    for (int i = 0; i < 18; ++i) { const int pc = i * 512 + tid; st[i] = *(const u32x4*)(a.wv + (size_t)(pc >> 4) * (NH * DH) + h * DH + (pc & 15) * 8); }
#pragma unroll
    for (int i = 0; i < 18; ++i) { const int pc = i * 512 + tid; *(u32x4*)(smem + (pc >> 4) * 272 + (pc & 15) * 16) = st[i]; }
#pragma unroll
    for (int i = 0; i < 18; ++i) st[i] = *(const u32x4*)(a.wo + (size_t)h * DH * DM + (i * 512 + tid) * 8);
    bar();
    {
      const char* wl = smem + g * 8 * 272 + wave * 32 + (c16 >> 1) * 4;
#pragma unroll
      for (int t = 0; t < KS; ++t) {
#pragma unroll
        for (int p = 0; p < 4; ++p)
          WV[t][p] = __builtin_amdgcn_perm(*(const u32*)(wl + (t * 32 + 2 * p + 1) * 272), *(const u32*)(wl + (t * 32 + 2 * p) * 272), sel);
        asm volatile("" : "+v"(WV[t]) :: "memory");
      }
    }
    bar();
#pragma unroll
    for (int i = 0; i < 18; ++i) { const int pc = i * 512 + tid; *(u32x4*)(smem + (pc / 72) * 1168 + (pc % 72) * 16) = st[i]; }
    bar();
#pragma unroll
    for (int i = 0; i < 5; ++i) {
      const int ct = wave + 8 * i < 36 ? wave + 8 * i : 0;
      const char* wl = smem + g * 4 * 1168 + ct * 32 + (c16 >> 1) * 4;
#pragma unroll
      for (int es = 0; es < 4; ++es) {
#pragma unroll
        for (int p = 0; p < 4; ++p) {
          const int e0 = 32 * es + 16 * (p >> 1) + 2 * (p & 1);
          WO[i][es][p] = __builtin_amdgcn_perm(*(const u32*)(wl + (e0 + 1) * 1168), *(const u32*)(wl + e0 * 1168), sel);
        }
        asm volatile("" : "+v"(WO[i][es]) :: "memory");
      }
    }
    bar();
  }
  STAMP(64, 1);
  stage_x(ws, 0, wave, lane);
  for (int sg = 0; sg < NSEG; ++sg) {
    wait_vm();
    bar();
    if (sg > 0 && tid == 0) __builtin_amdgcn_raw_buffer_store_b32(1u, r, FLG_OFF + (sg - 1) * 128 + h * 4, 0, SC1);
    if (sg + 1 < NSEG) stage_x(ws, sg + 1, wave, lane);
    {
      const char* xb = smem + L_X + (sg & 1) * XF_SEG + lane * 16;
      f32x4 c0 = {0.f, 0.f, 0.f, 0.f}, c1 = c0;
#pragma unroll
      for (int t = 0; t < KS; ++t) {
        c0 = mfma(WV[t], *(const u32x4*)(xb + t * 1024), c0);
        c1 = mfma(WV[t], *(const u32x4*)(xb + (KS + t) * 1024), c1);
      }
      __builtin_amdgcn_sched_group_barrier(0x100, 8, 0);
#pragma unroll
      for (int t = 0; t < KS - 4; ++t) { __builtin_amdgcn_sched_group_barrier(0x008, 2, 0); __builtin_amdgcn_sched_group_barrier(0x100, 2, 0); }
      __builtin_amdgcn_sched_group_barrier(0x008, 8, 0);
      char* vt = smem + L_QM + (wave >> 1) * 1024 + lane * 16 + (wave & 1) * 8;
      *(u32x2*)vt = u32x2{pk(c0[0], c0[1]), pk(c0[2], c0[3])};
      *(u32x2*)(vt + 4096) = u32x2{pk(c1[0], c1[1]), pk(c1[2], c1[3])};
    }
    bar();
    u32x4 vf[2][4];
#pragma unroll
    for (int rt = 0; rt < 2; ++rt)
#pragma unroll
      for (int es = 0; es < 4; ++es) vf[rt][es] = *(const u32x4*)(smem + L_QM + rt * 4096 + es * 1024 + lane * 16);
#pragma unroll
    for (int i = 0; i < 5; ++i) {
      const int ct = wave + 8 * i;
      if (ct < 36) {
        f32x4 d0 = {0.f, 0.f, 0.f, 0.f}, d1 = d0;
#pragma unroll
        for (int es = 0; es < 4; ++es) { d0 = mfma(vf[0][es], WO[i][es], d0); d1 = mfma(vf[1][es], WO[i][es], d1); }
        const u32x4 o = {pk(d0[0], d0[1]), pk(d0[2], d0[3]), pk(d1[0], d1[1]), pk(d1[2], d1[3])};
        __builtin_amdgcn_raw_buffer_store_b128(o, r, UX_OFF + ((sg * NB + ct * 2 + (c16 >> 3)) * 16 + (h >> 1)) * 1024 + (g * 16 + (h & 1) * 8 + (lane & 7)) * 16, 0, SC1);
      }
    }
    if (sg == 0) STAMP(64, 2);
  }
  wait_vm();
  bar();
  if (tid == 0) __builtin_amdgcn_raw_buffer_store_b32(1u, r, FLG_OFF + (NSEG - 1) * 128 + h * 4, 0, SC1);
  STAMP(64, 3);
}

__device__ __forceinline__ float gelu_tanh(float x) {
  const float u = 0.7978845608028654f * (x + 0.044715f * x * x * x);
  const float e = __expf(2.f * u);
  return 0.5f * x * (2.f - 2.f / (e + 1.f));
}

__device__ __forceinline__ void role_f(const Args& a, int f, char* smem) {
  const int tid = threadIdx.x, lane = tid & 63, wave = __builtin_amdgcn_readfirstlane(tid >> 6);
  const int c16 = lane & 15, g = lane >> 4;
  unsigned char* ws = a.ws;
  u32* tmo = (u32*)(ws + CTL_OFF) + 64;
  u32* cnt = (u32*)(ws + CTL_OFF) + 128;
  __amdgpu_buffer_rsrc_t r = __builtin_amdgcn_make_buffer_rsrc(ws, 0, WS_END, 0x00020000);
  STAMP(64, 0);
  auto stage = [&](int sg) {
#pragma unroll
    for (int i = 0; i < 5; ++i) {
      const int p = min(i * 8 + wave, 35);
      glds16(ws + XF_OFF + sg * XF_SEG + p * 1024 + lane * 16, (sg & 3) * XF_SEG + p * 1024);
    }
  };
  if (f >= 64) {
    const int h = f - 64;
    u32x4 WW[2][KS];
    load_wqk(a, h, WW, smem);
    wait_vm();
    stage(0); stage(1);
    for (int sg = 0; sg < NSEG; ++sg) {
      if (sg < 4) wait_vm();
      else asm volatile("s_waitcnt vmcnt(5)" ::: "memory");
      bar();
      if (sg >= 1 && tid == 0) __builtin_amdgcn_raw_buffer_store_b32(1u, r, FLQ_OFF + (sg < 4 ? sg - 1 : sg - 2) * 128 + h * 4, 0, SC1);
      stage(min(sg + 2, NSEG - 1));
#pragma unroll
      for (int rt = 0; rt < 2; ++rt) {
        f32x4 c0, c1;
        proj2<true, 1024>(WW[0], WW[1], smem + (sg & 3) * XF_SEG + rt * KS * 1024 + lane * 16, c0, c1);
        const u32x4 o = {pk(c0[0], c1[0]), pk(c0[1], c1[1]), pk(c0[2], c1[2]), pk(c0[3], c1[3])};
        __builtin_amdgcn_raw_buffer_store_b128(o, r, QX_OFF + ((sg * NH + h) * 16 + (wave >> 2) * 8 + rt * 4 + (wave & 3)) * 1024 + lane * 16, 0, SC1);
      }
    }
    wait_vm();
    bar();
    if (tid < 2) __builtin_amdgcn_raw_buffer_store_b32(1u, r, FLQ_OFF + (NSEG - 2 + tid) * 128 + h * 4, 0, SC1);
  }
  for (int it = f * 27 + wave; it < f * 27 + 27; it += 8) {
    const int ks = it / 18, bb = it % 18;
    u32 d[8];
#pragma unroll
    for (int jj = 0; jj < 8; ++jj)
      d[jj] = *(const u32*)(a.wd + (size_t)(ks * 32 + (jj >> 2) * 16 + g * 4 + (jj & 3)) * DM + bb * 32 + c16 * 2);
    const u32x4 lo = {plo(d[0], d[1]), plo(d[2], d[3]), plo(d[4], d[5]), plo(d[6], d[7])};
    const u32x4 hi = {phi(d[0], d[1]), phi(d[2], d[3]), phi(d[4], d[5]), phi(d[6], d[7])};
    __builtin_amdgcn_raw_buffer_store_b128(lo, r, WDF_OFF + (bb * 144 + ks) * 2048 + lane * 16, 0, SC1);
    __builtin_amdgcn_raw_buffer_store_b128(hi, r, WDF_OFF + (bb * 144 + ks) * 2048 + 1024 + lane * 16, 0, SC1);
  }
#pragma unroll 1
  for (int job = 0; job < 3 && f < 64; ++job) {
    int tid = threadIdx.x;
    asm volatile("" : "+v"(tid));
    const int hv = wave & 3, rt = wave >> 2;
    const int u0 = f * 72, sl = (u0 >> 6) + job;
    const int q0 = job == 0 ? (u0 & 63) : 0, q1 = min(u0 + 72 - sl * 64, 64);
    if (q1 <= q0) break;
    u32x4 WW[2][KS];
#pragma unroll
    for (int mat = 0; mat < 2; ++mat) {
      __syncthreads();
      u32x4 st[9];
#pragma unroll
      for (int i = 0; i < 9; ++i) {
        const int pc = i * 512 + tid;
        st[i] = *(const u32x4*)((mat ? a.wu : a.wg) + (size_t)(pc >> 3) * FF + sl * 64 + (pc & 7) * 8);
      }
#pragma unroll
      for (int i = 0; i < 9; ++i) { const int pc = i * 512 + tid; *(u32x4*)(smem + (pc >> 3) * 144 + (pc & 7) * 16) = st[i]; }
      __syncthreads();
      const char* wl = smem + g * 8 * 144 + hv * 32 + (c16 >> 1) * 4;
      const u32 sel = (c16 & 1) ? 0x07060302u : 0x05040100u;
#pragma unroll
      for (int t = 0; t < KS; ++t) {
#pragma unroll
        for (int p = 0; p < 4; ++p) {
          const int o = (t * 32 + 2 * p) * 144;
          WW[mat][t][p] = __builtin_amdgcn_perm(*(const u32*)(wl + o + 144), *(const u32*)(wl + o), sel);
        }
        asm volatile("" : "+v"(WW[mat][t]) :: "memory");
      }
    }
    __syncthreads();
    if (job == 0) STAMP(64, 1);
    stage(q0); stage(min(q0 + 1, q1 - 1));
    for (int sg = q0; sg < q1; ++sg) {
      asm volatile("s_waitcnt vmcnt(5)" ::: "memory");
      bar();
      stage(min(sg + 2, q1 - 1));
      f32x4 cg, cu;
      proj2<true, 1024>(WW[0], WW[1], smem + (sg & 3) * XF_SEG + rt * KS * 1024 + lane * 16, cg, cu);
      float hh[4];
#pragma unroll
      for (int i = 0; i < 4; ++i) hh[i] = bfr(gelu_tanh(bfr(cg[i]))) * bfr(cu[i]);
      const u32x2 o = {pk(hh[0], hh[1]), pk(hh[2], hh[3])};
      __builtin_amdgcn_raw_buffer_store_b64(o, r, H_OFF + ((sg * 2 + rt) * 144 + sl * 2 + (hv >> 1)) * 1024 + lane * 16 + (hv & 1) * 8, 0, SC1);
    }
    wait_vm();
  }
  wait_vm();
  __syncthreads();
  STAMP(64, 2);
  if (tid == 0) {
    __hip_atomic_fetch_add(cnt, 1u, __ATOMIC_RELAXED, __HIP_MEMORY_SCOPE_AGENT);
    u32 spins = 0;
    while (__hip_atomic_load(cnt, __ATOMIC_RELAXED, __HIP_MEMORY_SCOPE_AGENT) < (u32)NF) {
      __builtin_amdgcn_s_sleep(8);
      if (spin_fail(spins, tmo)) break;
    }
    __builtin_amdgcn_fence(__ATOMIC_ACQUIRE, "agent");
    wait_vm();
  }
  __syncthreads();
  STAMP(64, 3);
  {
    const int tm = f / 6, b3 = (f % 6) * 3, tg = wave & 3, kh = wave >> 2;
    const unsigned char* hb = ws + H_OFF + ((size_t)(tm * 8 + tg * 2) * 144 + kh * 72) * 1024 + lane * 16;
    const unsigned char* wb = ws + WDF_OFF + (size_t)(b3 * 144 + kh * 72) * 2048 + lane * 16;
    f32x4 acc[2][6];
#pragma unroll
    for (int u = 0; u < 2; ++u)
#pragma unroll
      for (int i = 0; i < 6; ++i) acc[u][i] = f32x4{0.f, 0.f, 0.f, 0.f};
    u32x4 hf[2][2][2], wf[2][2][6];
    auto ld = [&](int st, int t0) {
#pragma unroll
      for (int t = 0; t < 2; ++t) {
        hf[st][t][0] = *(const u32x4*)(hb + (t0 + t) * 1024); hf[st][t][1] = *(const u32x4*)(hb + (144 + t0 + t) * 1024);
#pragma unroll
        for (int i = 0; i < 6; ++i) wf[st][t][i] = *(const u32x4*)(wb + ((i >> 1) * 144 + t0 + t) * 2048 + (i & 1) * 1024);
      }
    };
    auto mm = [&](int st) {
#pragma unroll
      for (int t = 0; t < 2; ++t)
#pragma unroll
        for (int i = 0; i < 6; ++i) { acc[0][i] = mfma(wf[st][t][i], hf[st][t][0], acc[0][i]); acc[1][i] = mfma(wf[st][t][i], hf[st][t][1], acc[1][i]); }
    };
    ld(0, 0);
#pragma unroll 1
    for (int t0 = 0; t0 < 72; t0 += 4) {
      ld(1, t0 + 2);
      __builtin_amdgcn_sched_barrier(0);
      mm(0);
      __builtin_amdgcn_sched_barrier(0);
      ld(0, min(t0 + 4, 70));
      __builtin_amdgcn_sched_barrier(0);
      mm(1);
      __builtin_amdgcn_sched_barrier(0);
    }
    STAMP(64, 4);
    if (kh == 1) {
#pragma unroll
      for (int u = 0; u < 2; ++u)
#pragma unroll
        for (int i = 0; i < 6; ++i) *(f32x4*)(smem + ((tg * 12 + u * 6 + i) * 64 + lane) * 16) = acc[u][i];
    }
    __syncthreads();
    if (kh == 0) {
#pragma unroll
      for (int u = 0; u < 2; ++u)
#pragma unroll
        for (int b = 0; b < 3; ++b) {
          const f32x4 e0 = acc[u][2 * b] + *(const f32x4*)(smem + ((tg * 12 + u * 6 + 2 * b) * 64 + lane) * 16);
          const f32x4 e1 = acc[u][2 * b + 1] + *(const f32x4*)(smem + ((tg * 12 + u * 6 + 2 * b + 1) * 64 + lane) * 16);
          const u32x4 o = {pk(e0[0], e1[0]), pk(e0[1], e1[1]), pk(e0[2], e1[2]), pk(e0[3], e1[3])};
          *(u32x4*)(ws + FO_OFF + ((size_t)(tm * 128 + tg * 32 + u * 16 + c16) * DM + (b3 + b) * 32 + g * 8) * 2) = o;
        }
    }
  }
}

__global__ void __launch_bounds__(512, 2) k2_main(Args a) {
  extern __shared__ __attribute__((aligned(16))) char smem[];
  const int b = blockIdx.x, rr = b & 7, q = b >> 3;
  if (rr == 0) role_a(a, q, smem);
  else if (rr < 4 && q < 24) role_b(a, (rr - 1) * 24 + q, smem);
  else if (rr == 4) role_v(a, q, smem);
  else if (rr > 4) role_f(a, (rr - 5) * 32 + q, smem);
  else role_o(a, (rr - 1) * 8 + (q - 24), smem);
  DUMP();
}

__global__ void __launch_bounds__(256) k3_final(Args a) {
  const int i = blockIdx.x * 256 + threadIdx.x;
  const u32x4 x = ((const u32x4*)a.x)[i], p = ((const u32x4*)(a.ws + AO_OFF))[i], f = ((const u32x4*)(a.ws + FO_OFF))[i];
  u32x4 o;
#pragma unroll
  for (int k = 0; k < 4; ++k)
    o[k] = pk(bfr(bflo(x[k]) + bflo(p[k])) + bflo(f[k]), bfr(bfhi(x[k]) + bfhi(p[k])) + bfhi(f[k]));
  ((u32x4*)a.out)[i] = o;
}

extern "C" void kernel_launch(void* const* d_in, const int* in_sizes, int n_in, void* d_out, int out_size, void* d_ws,
                              size_t ws_size, hipStream_t stream) {
  Args a;
  a.x = (const u16*)d_in[0]; a.mem0 = (const u16*)d_in[1]; a.wq = (const u16*)d_in[2]; a.wk = (const u16*)d_in[3];
  a.wv = (const u16*)d_in[4]; a.wo = (const u16*)d_in[5]; a.wg = (const u16*)d_in[8]; a.wu = (const u16*)d_in[9];
  a.wd = (const u16*)d_in[10];
  a.ws = (unsigned char*)(((uintptr_t)d_ws + 65535) & ~(uintptr_t)65535);
  a.out = (u16*)d_out;
  (void)hipFuncSetAttribute((const void*)k2_main, hipFuncAttributeMaxDynamicSharedMemorySize, LDS_BYTES);
  k1_prep<<<K1_GRID, 256, 0, stream>>>(a);
  k2_main<<<256, 512, LDS_BYTES, stream>>>(a);
  k3_final<<<S_ * DM / 8 / 256, 256, 0, stream>>>(a);
}
```

```cpp
#include <hip/hip_runtime.h>
#include <hip/hip_bf16.h>

typedef unsigned u32;
typedef unsigned long long u64;
typedef unsigned short u16;
typedef __attribute__((ext_vector_type(8))) __bf16 bf16x8;
typedef __attribute__((ext_vector_type(2))) __bf16 bf16x2;
typedef __attribute__((ext_vector_type(4))) float f32x4;
typedef __attribute__((ext_vector_type(2))) float f32x2;
typedef __attribute__((ext_vector_type(4))) unsigned u32x4;
typedef __attribute__((ext_vector_type(2))) unsigned u32x2;

constexpr int S_ = 2048, DM = 576, NH = 32, DH = 128, FF = 4608, NSEG = 64, KS = 18;
constexpr int NB = 72, NF = 96;
constexpr u32 HO_OFF = 0;
constexpr u32 PT_OFF = HO_OFF + NSEG * 65536;
constexpr u32 PT_STEP = 4 * 20480;
constexpr u32 FLG_OFF = PT_OFF + NSEG * PT_STEP;
constexpr u32 FLM_OFF = FLG_OFF + NSEG * 128;
constexpr u32 FLQ_OFF = FLM_OFF + NSEG * 3072;
constexpr u32 FLP_OFF = FLQ_OFF + NSEG * 128;
constexpr u32 POISON_BYTES = FLP_OFF + 256;
constexpr u32 CTL_OFF = (POISON_BYTES + 4095) & ~4095u;
constexpr u32 M0_OFF = CTL_OFF + 4096;
constexpr u32 XF_OFF = M0_OFF + 12288;
constexpr u32 XF_SEG = 2 * KS * 1024;
constexpr u32 WVO_OFF = XF_OFF + NSEG * XF_SEG;
constexpr u32 WVO_J = 16 * KS * 1024;
constexpr u32 UX_OFF = WVO_OFF + NB * WVO_J;
constexpr u32 UM_OFF = UX_OFF + NSEG * NB * 16384;
constexpr u32 QX_OFF = UM_OFF + NSEG * NB * 4096;
constexpr u32 H_OFF = QX_OFF + NSEG * NH * 16384;
constexpr u32 WDF_OFF = H_OFF + 128 * 144 * 1024;
constexpr u32 FO_OFF = WDF_OFF + 9 * 144 * 4096;
constexpr u32 AO_OFF = FO_OFF + S_ * DM * 2;
constexpr u32 WS_END = AO_OFF + S_ * DM * 2;
constexpr u32 POISON = 0xFFFFFFFFu;
constexpr int SC1 = 16;
constexpr u32 SPIN_CAP = 400000u;
constexpr int LDS_BYTES = 160 * 1024;

__device__ __forceinline__ u32 pk(float lo, float hi) {
  f32x2 v = {lo, hi};
  return __builtin_bit_cast(u32, __builtin_convertvector(v, bf16x2));
}
__device__ __forceinline__ u32 pks(float lo, float hi) { u32 r = pk(lo, hi); return r == POISON ? 0x7FC07FC0u : r; }
__device__ __forceinline__ float bfr(float x) { return (float)(__bf16)x; }
__device__ __forceinline__ float bflo(u32 w) { return __uint_as_float(w << 16); }
__device__ __forceinline__ float bfhi(u32 w) { return __uint_as_float(w & 0xFFFF0000u); }
__device__ __forceinline__ f32x4 mfma(u32x4 a, u32x4 b, f32x4 c) {
  return __builtin_amdgcn_mfma_f32_16x16x32_bf16(__builtin_bit_cast(bf16x8, a), __builtin_bit_cast(bf16x8, b), c, 0, 0, 0);
}
__device__ __forceinline__ u32 plo(u32 a, u32 b) { return __builtin_amdgcn_perm(b, a, 0x05040100u); }
__device__ __forceinline__ u32 phi(u32 a, u32 b) { return __builtin_amdgcn_perm(b, a, 0x07060302u); }
__device__ __forceinline__ float vmax(float a, float b) { float r; asm("v_max_f32 %0, %1, %2" : "=v"(r) : "v"(a), "v"(b)); return r; }
__device__ __forceinline__ float vmax3(float a, float b, float c) { float r; asm("v_max3_f32 %0, %1, %2, %3" : "=v"(r) : "v"(a), "v"(b), "v"(c)); return r; }
__device__ __forceinline__ float xmax(float x) {
  auto s = __builtin_amdgcn_permlane16_swap(__float_as_uint(x), __float_as_uint(x), false, false);
  x = vmax(__uint_as_float(s[0]), __uint_as_float(s[1]));
  auto t = __builtin_amdgcn_permlane32_swap(__float_as_uint(x), __float_as_uint(x), false, false);
  return vmax(__uint_as_float(t[0]), __uint_as_float(t[1]));
}
__device__ __forceinline__ float xsum(float x) {
  auto s = __builtin_amdgcn_permlane16_swap(__float_as_uint(x), __float_as_uint(x), false, false);
  x = __uint_as_float(s[0]) + __uint_as_float(s[1]);
  auto t = __builtin_amdgcn_permlane32_swap(__float_as_uint(x), __float_as_uint(x), false, false);
  return __uint_as_float(t[0]) + __uint_as_float(t[1]);
}
__device__ __forceinline__ float ror8(float x) {
  return __builtin_bit_cast(float, __builtin_amdgcn_mov_dpp(__builtin_bit_cast(int, x), 0x128, 0xf, 0xf, true));
}
__device__ __forceinline__ u32 mx(u32 a, u32 b) { return a > b ? a : b; }
__device__ __forceinline__ u32 mx4(u32x4 v) { return mx(mx(v.x, v.y), mx(v.z, v.w)); }
__device__ __forceinline__ u32 mx2(u32x2 v) { return mx(v.x, v.y); }
__device__ __forceinline__ bool allok(u32 m) { return __all(m != POISON); }
__device__ __forceinline__ void glds16(const void* g, u32 lds) {
  u32 keep;
  asm volatile("s_mov_b32 %0, m0\n\ts_mov_b32 m0, %2\n\ts_nop 0\n\tglobal_load_lds_dwordx4 %1, off\n\ts_mov_b32 m0, %0"
               : "=&s"(keep) : "v"(g), "s"(lds) : "memory");
}
__device__ __forceinline__ void wait_vm() { asm volatile("s_waitcnt vmcnt(0)" ::: "memory"); }
__device__ __forceinline__ void bar() {
  asm volatile("s_waitcnt lgkmcnt(0)" ::: "memory");
  __builtin_amdgcn_s_barrier();
  asm volatile("" ::: "memory");
}
__device__ __forceinline__ bool spin_fail(u32& spins, u32* tmo) {
  ++spins;
  if (spins > SPIN_CAP) { __hip_atomic_store(tmo, 1u, __ATOMIC_RELAXED, __HIP_MEMORY_SCOPE_AGENT); return true; }
  if ((spins & 1023u) == 0u && __hip_atomic_load(tmo, __ATOMIC_RELAXED, __HIP_MEMORY_SCOPE_AGENT) != 0u) return true;
  return false;
}
__device__ __forceinline__ u32 homap(u32 lo, u32 flip) { return ((lo & ~8191u) << 1) + (lo & 8191u) + flip; }
constexpr u32 HO_PM = 16384, HO_M = 20480;

#define STAMP(step, k) do {} while (0)
#define STAMPC(step, k) do {} while (0)
#define DUMP() do {} while (0)
struct Args {
  const u16* x; const u16* mem0; const u16* wq; const u16* wk; const u16* wv; const u16* wo;
  const u16* wg; const u16* wu; const u16* wd; unsigned char* ws; u16* out;
};

constexpr int K1_WVO = 2592, K1_LN = 256, K1_LNB = K1_LN / 4, K1_JOBS = K1_WVO + K1_LN, K1_GRID = K1_JOBS / 4 + 8;
__global__ void __launch_bounds__(256) k1_prep(Args a) {
  const int lane = threadIdx.x & 63, wave = __builtin_amdgcn_readfirstlane(threadIdx.x >> 6);
  const int b = blockIdx.x;
  unsigned char* ws = a.ws;
  const int c16 = lane & 15, g = lane >> 4;
  if (b == K1_GRID - 1) {
    for (int i = threadIdx.x; i < 1024; i += 256) ((u32*)(ws + CTL_OFF))[i] = 0u;
    for (int i = threadIdx.x; i < 576; i += 256) {
      u32x4 v = *(const u32x4*)(a.mem0 + (i & 7) * DM + (i >> 3) * 8);
      if (v.x == POISON) v.x = 0x7FC07FC0u; if (v.y == POISON) v.y = 0x7FC07FC0u;
      if (v.z == POISON) v.z = 0x7FC07FC0u; if (v.w == POISON) v.w = 0x7FC07FC0u;
      *(u32x4*)(ws + M0_OFF + i * 16) = v;
    }
    return;
  }
  if (b >= K1_JOBS / 4) return;
  const int job = b * 4 + wave;
  auto poison = [&]() {
    const u32x4 pv = {POISON, POISON, POISON, POISON};
    for (u32 o = (job * 64 + lane) * 16; o < POISON_BYTES; o += K1_JOBS * 64 * 16) *(u32x4*)(ws + o) = pv;
  };
  if (b >= K1_LNB) {
    const int jx = ((b - K1_LNB) >> 3) * 4 + wave, h = (b & 7) * 4 + jx / 81, kp = (jx / 9) % 9, bb = jx % 9;
    u32x2 rw[4][8];
    u32x4 af[2][2][4];
#pragma unroll
    for (int es = 0; es < 4; ++es)
#pragma unroll
      for (int jj = 0; jj < 8; ++jj)
        rw[es][jj] = *(const u32x2*)(a.wo + (size_t)(h * DH + es * 32 + g * 8 + jj) * DM + bb * 64 + c16 * 4);
#pragma unroll
    for (int i = 0; i < 16; ++i) {
      const int k = (kp * 2 + (i >> 3)) * 32 + (c16 >> 2) * 8 + (c16 & 3) + ((i >> 2) & 1) * 4;
      af[i >> 3][(i >> 2) & 1][i & 3] = *(const u32x4*)(a.wv + (size_t)k * (NH * DH) + h * DH + (i & 3) * 32 + g * 8);
    }
    poison();
    __builtin_amdgcn_sched_barrier(0);
    u32x4 bf[4][4];
#pragma unroll
    for (int es = 0; es < 4; ++es)
#pragma unroll
      for (int p = 0; p < 4; ++p) {
        bf[es][0][p] = plo(rw[es][2 * p].x, rw[es][2 * p + 1].x); bf[es][1][p] = phi(rw[es][2 * p].x, rw[es][2 * p + 1].x);
        bf[es][2][p] = plo(rw[es][2 * p].y, rw[es][2 * p + 1].y); bf[es][3][p] = phi(rw[es][2 * p].y, rw[es][2 * p + 1].y);
      }
#pragma unroll
    for (int tt = 0; tt < 2; ++tt) {
      const int t = kp * 2 + tt;
      f32x4 acc[2][4];
#pragma unroll
      for (int rt = 0; rt < 2; ++rt)
#pragma unroll
        for (int i = 0; i < 4; ++i) {
          acc[rt][i] = f32x4{0.f, 0.f, 0.f, 0.f};
#pragma unroll
          for (int es = 0; es < 4; ++es) acc[rt][i] = mfma(af[tt][rt][es], bf[es][i], acc[rt][i]);
        }
      const int j = bb * 8 + (c16 >> 1);
      unsigned char* dst = ws + WVO_OFF + (size_t)j * WVO_J + ((h >> 1) * KS + t) * 1024 + (g * 16 + (h & 1) * 8 + (c16 & 1) * 4) * 16;
#pragma unroll
      for (int i = 0; i < 4; ++i) {
        u32x4 v = {pk(acc[0][i][0], acc[0][i][1]), pk(acc[0][i][2], acc[0][i][3]), pk(acc[1][i][0], acc[1][i][1]), pk(acc[1][i][2], acc[1][i][3])};
        *(u32x4*)(dst + i * 16) = v;
      }
    }
  } else {
    u32x4 vv[2][5];
#pragma unroll
    for (int rr = 0; rr < 2; ++rr)
#pragma unroll
    for (int i = 0; i < 5; ++i) {
      vv[rr][i] = u32x4{0, 0, 0, 0};
      if (i < 4 || c16 < 8) vv[rr][i] = *(const u32x4*)(a.x + (size_t)(job * 8 + rr * 4 + g) * DM + (i * 16 + c16) * 8);
    }
    poison();
#pragma unroll
    for (int rr = 0; rr < 2; ++rr) {
    const int row = job * 8 + rr * 4 + g;
    const u32x4 (&v)[5] = vv[rr];
    float sm = 0.f;
#pragma unroll
    for (int i = 0; i < 20; ++i) sm += bflo(v[i >> 2][i & 3]) + bfhi(v[i >> 2][i & 3]);
#pragma unroll
    for (int o = 1; o < 16; o <<= 1) sm += __shfl_xor(sm, o);
    const float mean = sm * (1.f / DM);
    float q = 0.f;
#pragma unroll
    for (int i = 0; i < 20; ++i) {
      const float d0 = bflo(v[i >> 2][i & 3]) - mean, d1 = bfhi(v[i >> 2][i & 3]) - mean;
      q += (i < 16 || c16 < 8) ? d0 * d0 + d1 * d1 : 0.f;
    }
#pragma unroll
    for (int o = 1; o < 16; o <<= 1) q += __shfl_xor(q, o);
    const float mb = bfr(mean), den = bfr(sqrtf(bfr(bfr(q * (1.f / DM)) + 1e-5f)));
#pragma unroll
    for (int i = 0; i < 5; ++i) {
      u32x4 o;
#pragma unroll
      for (int k = 0; k < 4; ++k) o[k] = pk(bfr(bflo(v[i][k]) - mb) / den, bfr(bfhi(v[i][k]) - mb) / den);
      const int ch = i * 16 + c16;
      if (i < 4 || c16 < 8) *(u32x4*)(ws + XF_OFF + ((row >> 4) * KS + (ch >> 2)) * 1024 + ((ch & 3) * 16 + (row & 15)) * 16) = o;
    }
    }
  }
}

constexpr u32 L_X = 0;
constexpr u32 L_M = 2 * XF_SEG;
constexpr u32 L_QM = L_M + 9216;
constexpr u32 L_QX = L_QM + 8192;

template <bool WA, int ST>
__device__ __forceinline__ void proj2(const u32x4 (&W0)[KS], const u32x4 (&W1)[KS], const char* xb, f32x4& c0, f32x4& c1) {
  u32x4 xf[KS];
#pragma unroll
  for (int t = 0; t < KS; ++t) xf[t] = *(const u32x4*)(xb + t * ST);
  c0 = f32x4{0.f, 0.f, 0.f, 0.f}; c1 = c0;
#pragma unroll
  for (int t = 0; t < KS; ++t) {
    c0 = WA ? mfma(W0[t], xf[t], c0) : mfma(xf[t], W0[t], c0);
    c1 = WA ? mfma(W1[t], xf[t], c1) : mfma(xf[t], W1[t], c1);
  }
  __builtin_amdgcn_sched_group_barrier(0x100, 6, 0);
#pragma unroll
  for (int t = 0; t < KS - 6; ++t) { __builtin_amdgcn_sched_group_barrier(0x008, 2, 0); __builtin_amdgcn_sched_group_barrier(0x100, 1, 0); }
  __builtin_amdgcn_sched_group_barrier(0x008, 12, 0);
}
__device__ __forceinline__ void stage_x(const unsigned char* ws, int seg, int wave, int lane) {
  const unsigned char* src = ws + XF_OFF + seg * XF_SEG + lane * 16;
  const u32 dst = L_X + (seg & 1) * XF_SEG;
#pragma unroll
  for (int i = 0; i < 5; ++i) {
    const int p = i * 8 + wave;
    if (p < 36) glds16(src + p * 1024, dst + p * 1024);
  }
}
__device__ __forceinline__ u32 get_flips(__amdgpu_buffer_rsrc_t r, int lane, u32* tmo) {
  u32 f, spins = 0;
  for (;;) {
    f = __builtin_amdgcn_raw_buffer_load_b32(r, FLP_OFF + lane * 4, 0, SC1);
    if (allok(f)) break;
    if (spin_fail(spins, tmo)) break;
    __builtin_amdgcn_s_sleep(8);
  }
  return f & 8192u;
}
__device__ __forceinline__ void gather_m(__amdgpu_buffer_rsrc_t r, u32 base, u32 flip, bool first, char* smem, int wave, int lane, u32* tmo) {
  const u32 l0 = (wave * 64 + lane) * 16, l1 = (512 + wave * 8 + (lane & 7)) * 16;
  const u32 o0 = first ? M0_OFF + l0 : base + homap(HO_M + l0, flip), o1 = first ? M0_OFF + l1 : base + homap(HO_M + l1, flip);
  u32 spins = 0;
  u32x4 v, w;
  for (;;) {
    v = __builtin_amdgcn_raw_buffer_load_b128(r, o0, 0, SC1);
    w = __builtin_amdgcn_raw_buffer_load_b128(r, o1, 0, SC1);
    if (allok(mx(mx4(v), mx4(w)))) break;
    if (spin_fail(spins, tmo)) break;
  }
  *(u32x4*)(smem + L_M + l0) = v;
  if (lane < 8) *(u32x4*)(smem + L_M + l1) = w;
  wait_vm();
}

__device__ __forceinline__ void load_wqk(const Args& a, int h, u32x4 (&W)[2][KS], char* smem) {
  const int tid = threadIdx.x, lane = tid & 63, wave = __builtin_amdgcn_readfirstlane(tid >> 6);
  const int c16 = lane & 15, g = lane >> 4;
  {
    u32x4 st[18];
    auto ldw = [&](int hf) {
#pragma unroll
      for (int i = 0; i < 18; ++i) {
        const int pc = i * 512 + tid;
        st[i] = *(const u32x4*)(((pc & 16) ? a.wk : a.wq) + (size_t)(hf * 288 + (pc >> 5)) * (NH * DH) + h * DH + (pc & 15) * 8);
      }
    };
    ldw(0);
#pragma unroll
    for (int hf = 0; hf < 2; ++hf) {
#pragma unroll
      for (int i = 0; i < 18; ++i) { const int pc = i * 512 + tid; *(u32x4*)(smem + (pc >> 5) * 528 + (pc & 31) * 16) = st[i]; }
      if (hf == 0) ldw(1);
      bar();
      const char* wl = smem + g * 8 * 528 + wave * 64 + c16 * 4;
#pragma unroll
      for (int t = 0; t < 9; ++t) {
#pragma unroll
        for (int p = 0; p < 4; ++p) {
          const u32 d0 = *(const u32*)(wl + (t * 32 + 2 * p) * 528), d1 = *(const u32*)(wl + (t * 32 + 2 * p + 1) * 528);
          W[0][hf * 9 + t][p] = plo(d0, d1); W[1][hf * 9 + t][p] = phi(d0, d1);
        }
        asm volatile("" : "+v"(W[0][hf * 9 + t]), "+v"(W[1][hf * 9 + t]) :: "memory");
      }
      bar();
    }
  }
}

__device__ __forceinline__ void role_a(const Args& a, int h, char* smem) {
  const int tid = threadIdx.x, lane = tid & 63, wave = __builtin_amdgcn_readfirstlane(tid >> 6);
  const int c16 = lane & 15, g = lane >> 4;
  unsigned char* ws = a.ws;
  u32* tmo = (u32*)(ws + CTL_OFF) + 64;
  __amdgpu_buffer_rsrc_t r = __builtin_amdgcn_make_buffer_rsrc(ws, 0, WS_END, 0x00020000);
  STAMP(64, 0);
  u32x4 W[2][KS];
  load_wqk(a, h, W, smem);
  const u32 flips = get_flips(r, lane, tmo);
  const u32 lx = (((h >> 1) * 4 + g) * 16 + (h & 1) * 8 + (lane & 7)) * 16, lm = HO_PM + h * 128 + g * 64 + (lane & 7) * 8;
  STAMP(64, 1); STAMPC(65, 0); STAMP(65, 1);
  u32x4 kf[8];
  u32 fl = POISON;
  auto q_load = [&](int seg) {
    const u32 fo = FLQ_OFF + seg * 128 + h * 4;
    u32 spins = 0;
    while (!allok(fl)) {
      fl = __builtin_amdgcn_raw_buffer_load_b32(r, fo, 0, SC1);
      if (spin_fail(spins, tmo)) break;
    }
    asm volatile("" ::: "memory");
    const u32 qo = QX_OFF + ((seg * NH + h) * 16 + (wave - 1) * 4) * 1024 + lane * 16;
#pragma unroll
    for (int w = 0; w < 4; ++w) kf[w] = __builtin_amdgcn_raw_buffer_load_b128(r, qo + w * 1024, 0, SC1);
    if (wave == 3) {
#pragma unroll
      for (int w = 4; w < 8; ++w) kf[w] = __builtin_amdgcn_raw_buffer_load_b128(r, qo + w * 1024, 0, SC1);
#pragma unroll
      for (int w = 0; w < 8; ++w) *(u32x4*)(smem + L_QX + (seg & 1) * 16384 + 8192 + w * 1024 + lane * 16) = kf[w];
    }
    fl = seg + 1 < NSEG ? __builtin_amdgcn_raw_buffer_load_b32(r, fo + 128, 0, SC1) : 0u;
  };
  if (wave >= 1 && wave <= 3) q_load(0);
  for (int s = 0; s < NSEG; ++s) {
    const u32 flip = __builtin_amdgcn_readlane(flips, s), base = HO_OFF + s * 65536;
    STAMP(s, 0);
    gather_m(r, base, flip, s == 0, smem, wave, lane, tmo);
    STAMP(s, 1);
    bar();
    STAMP(s, 2);
    {
      f32x4 c0, c1;
      proj2<true, 512>(W[0], W[1], smem + L_M + g * 128 + (lane & 7) * 16, c0, c1);
      u32x4 o = {pk(c0[0], c1[0]), pk(c0[1], c1[1]), pk(c0[2], c1[2]), pk(c0[3], c1[3])};
      *(u32x4*)(smem + L_QM + wave * 1024 + lane * 16) = o;
    }
    if (wave == 0) {
      __builtin_amdgcn_s_setprio(3);
#pragma unroll
      for (int w = 0; w < 8; ++w) kf[w] = *(const u32x4*)(smem + L_QX + (s & 1) * 16384 + 8192 + lane * 16 + w * 1024);
    }
    bar();
    STAMP(s, 3);
    const float C2 = 0.08838834764831845f * 1.4426950408889634f;
    if (wave == 0) {
      if (s < NSEG - 1) {
        const char* qb = smem + L_QM + lane * 16;
        f32x4 s0 = {0.f, 0.f, 0.f, 0.f}, s1 = s0, s2 = s0;
#pragma unroll
        for (int w = 0; w < 4; ++w) {
          const u32x4 qf = *(const u32x4*)(qb + w * 1024);
          s2 = mfma(*(const u32x4*)(qb + 4096 + w * 1024), qf, s2);
          s0 = mfma(kf[w], qf, s0);
          s1 = mfma(kf[4 + w], qf, s1);
        }
        const bool hi = c16 >= 8;
        float v[6];
#pragma unroll
        for (int i = 0; i < 4; ++i) v[i] = hi ? s1[i] : s0[i];
        v[4] = g < 2 ? (hi ? s2[2] : s2[0]) : -INFINITY;
        v[5] = g < 2 ? (hi ? s2[3] : s2[1]) : -INFINITY;
        float m = vmax(vmax3(v[0], v[1], v[2]), vmax3(v[3], v[4], v[5]));
        m = xmax(m);
        m = vmax(m, ror8(m)) * C2;
#pragma unroll
        for (int i = 0; i < 6; ++i) v[i] = __builtin_amdgcn_exp2f(__builtin_fmaf(v[i], C2, -m));
        float sum = ((v[0] + v[1]) + (v[2] + v[3])) + (v[4] + v[5]);
        sum += ror8(sum);
        const float inv = __builtin_amdgcn_rcpf(xsum(sum));
        const u32x2 px = {pk(v[0] * inv, v[1] * inv) & 0x7FFF7FFFu, pk(v[2] * inv, v[3] * inv) & 0x7FFF7FFFu};
        const u32 pm = pk(v[4] * inv, v[5] * inv) & 0x7FFF7FFFu;
        __builtin_amdgcn_raw_buffer_store_b64(px, r, base + homap(lx + (c16 >> 3) * 8, flip), 0, SC1);
        if (g < 2) __builtin_amdgcn_raw_buffer_store_b32(pm, r, base + homap(lm + (c16 >> 3) * 4, flip), 0, SC1);
      }
      __builtin_amdgcn_s_setprio(0);
    } else if (wave < 3) {
      const char* qx = smem + L_QX + (s & 1) * 16384 + lane * 16;
      f32x4 s0 = {0.f, 0.f, 0.f, 0.f}, s1 = s0, s2 = s0;
#pragma unroll
      for (int w = 0; w < 4; ++w) {
        s2 = mfma(*(const u32x4*)(smem + L_QM + 4096 + lane * 16 + w * 1024), kf[w], s2);
        s0 = mfma(*(const u32x4*)(qx + 8192 + w * 1024), kf[w], s0);
        s1 = mfma(*(const u32x4*)(qx + 12288 + w * 1024), kf[w], s1);
      }
      const int qi = (wave - 1) * 16 + c16;
      const float NEG = -1.0e9f * 1.4426950408889634f;
      float v[12];
#pragma unroll
      for (int i = 0; i < 4; ++i) {
        v[i] = (4 * g + i <= qi) ? s0[i] * C2 : NEG;
        v[4 + i] = (16 + 4 * g + i <= qi) ? s1[i] * C2 : NEG;
        v[8 + i] = g < 2 ? s2[i] * C2 : -INFINITY;
      }
      float m = v[0];
#pragma unroll
      for (int i = 1; i < 12; ++i) m = fmaxf(m, v[i]);
      m = xmax(m);
      float sum = 0.f;
#pragma unroll
      for (int i = 0; i < 12; ++i) { v[i] = __builtin_amdgcn_exp2f(v[i] - m); sum += v[i]; }
      const float inv = __builtin_amdgcn_rcpf(xsum(sum));
      u32x4 px; u32x2 pm;
#pragma unroll
      for (int i = 0; i < 4; ++i) px[i] = pk(v[2 * i] * inv, v[2 * i + 1] * inv) & 0x7FFF7FFFu;
#pragma unroll
      for (int i = 0; i < 2; ++i) pm[i] = pk(v[8 + 2 * i] * inv, v[9 + 2 * i] * inv) & 0x7FFF7FFFu;
      const u32 tb = PT_OFF + s * PT_STEP + ((wave - 1) * 2 + (c16 >> 3)) * 20480;
      __builtin_amdgcn_raw_buffer_store_b128(px, r, tb + lx, 0, SC1);
      if (g < 2) __builtin_amdgcn_raw_buffer_store_b64(pm, r, tb + lm, 0, SC1);
    }
    STAMP(s, 4);
    if (wave >= 1 && wave <= 3 && s + 1 < NSEG) q_load(s + 1);
    bar();
  }
  STAMPC(65, 2); STAMP(65, 3);
}

__device__ __forceinline__ void role_b(const Args& a, int j, char* smem) {
  const int tid = threadIdx.x, lane = tid & 63, wave = __builtin_amdgcn_readfirstlane(tid >> 6);
  const int c16 = lane & 15, g = lane >> 4;
  unsigned char* ws = a.ws;
  u32* tmo = (u32*)(ws + CTL_OFF) + 64;
  __amdgpu_buffer_rsrc_t r = __builtin_amdgcn_make_buffer_rsrc(ws, 0, WS_END, 0x00020000);
  STAMP(64, 0);
  u32x4 W[2][KS];
  {
    const unsigned char* wsrc = ws + WVO_OFF + (size_t)j * WVO_J + wave * 2 * KS * 1024 + lane * 16;
#pragma unroll
    for (int tt = 0; tt < 2; ++tt)
#pragma unroll
      for (int t = 0; t < KS; ++t) W[tt][t] = *(const u32x4*)(wsrc + (tt * KS + t) * 1024);
  }
  wait_vm();
  bar();
  const u32 flips = get_flips(r, lane, tmo);
  STAMP(64, 1);
  u32x4 nx0, nx1;
  u32 fl = POISON;
  auto ux_load = [&](int seg) {
    const u32 fo = FLG_OFF + seg * 128 + wave * 16 + (lane & 3) * 4;
    u32 spins = 0;
    while (!allok(fl)) {
      fl = __builtin_amdgcn_raw_buffer_load_b32(r, fo, 0, SC1);
      if (spin_fail(spins, tmo)) break;
    }
    asm volatile("" ::: "memory");
    const u32 uo = UX_OFF + ((seg * NB + j) * 16 + wave * 2) * 1024 + lane * 16;
    nx0 = __builtin_amdgcn_raw_buffer_load_b128(r, uo, 0, SC1);
    nx1 = __builtin_amdgcn_raw_buffer_load_b128(r, uo + 1024, 0, SC1);
    fl = seg + 1 < NSEG ? __builtin_amdgcn_raw_buffer_load_b32(r, fo + 128, 0, SC1) : 0u;
  };
  const u32 lx = wave * 2048 + lane * 16;
  const u32 lm = HO_PM + (wave * 4 + (c16 >> 3)) * 128 + (g & 1) * 64 + (lane & 7) * 8;
  const bool diag = (c16 >> 3) == (g >> 1);
  const u32 redo = ((wave * 2 + (g >> 1)) * 64 + (lane & 7) * 8 + (g & 1) * 4) * 4;
  for (int s = 0; s < NSEG; ++s) {
    const u32 flip = __builtin_amdgcn_readlane(flips, s), base = HO_OFF + s * 65536;
    STAMP(s, 0);
    gather_m(r, base, flip, s == 0, smem, wave, lane, tmo);
    bar();
    STAMP(s, 1);
    u32x4 um;
    {
      f32x4 c0, c1;
      proj2<false, 512>(W[0], W[1], smem + L_M + g * 128 + (lane & 7) * 16, c0, c1);
      um = u32x4{pk(c0[0], c0[1]), pk(c0[2], c0[3]), pk(c1[0], c1[1]), pk(c1[2], c1[3])};
      if (g < 2) __builtin_amdgcn_raw_buffer_store_b128(um, r, UM_OFF + ((s * NB + j) * 8 + wave) * 512 + lane * 16, 0, SC1);
      else um = u32x4{0, 0, 0, 0};
    }
    if (s + 1 < NSEG) {
      if (s == 0) ux_load(0);
      STAMP(s, 2);
      const u32 o0 = base + homap(lx, flip), o1 = o0 + 1024, o2 = base + homap(lm, flip), o3 = o2 + 256;
      u32x4 p0, p1; u32x2 q0, q1;
      u32 spins = 0;
      for (;;) {
        p0 = __builtin_amdgcn_raw_buffer_load_b128(r, o0, 0, SC1);
        p1 = __builtin_amdgcn_raw_buffer_load_b128(r, o1, 0, SC1);
        q0 = __builtin_amdgcn_raw_buffer_load_b64(r, o2, 0, SC1);
        q1 = __builtin_amdgcn_raw_buffer_load_b64(r, o3, 0, SC1);
        if (allok(mx(mx(mx4(p0), mx4(p1)), mx(mx2(q0), mx2(q1))))) break;
        if (spin_fail(spins, tmo)) break;
      }
      STAMP(s, 3);
      f32x4 d = {0.f, 0.f, 0.f, 0.f}, e = d;
      const u32x4 pmv = {q0.x, q0.y, q1.x, q1.y};
      d = mfma(nx0, p0, d); e = mfma(nx1, p1, e); d = mfma(um, pmv, d);
      d += e;
      if (diag) *(f32x4*)(smem + L_QM + redo) = d;
    }
    wait_vm();
    bar();
    if (wave == 0 && s + 1 < NSEG) {
      const char* rp = smem + L_QM + (lane >> 5) * 2048 + (lane & 31) * 8;
      f32x2 acc = *(const f32x2*)rp;
#pragma unroll
      for (int p = 1; p < 8; ++p) acc += *(const f32x2*)(rp + p * 256);
      auto t0 = __builtin_amdgcn_permlane32_swap(__float_as_uint(acc.x), __float_as_uint(acc.x), false, false);
      auto t1 = __builtin_amdgcn_permlane32_swap(__float_as_uint(acc.y), __float_as_uint(acc.y), false, false);
      const u32 o = pks(__uint_as_float(t0[0]) + __uint_as_float(t0[1]), __uint_as_float(t1[0]) + __uint_as_float(t1[1]));
      const u32 nflip = __builtin_amdgcn_readlane(flips, s + 1);
      if (lane < 32) __builtin_amdgcn_raw_buffer_store_b32(o, r, base + 65536 + homap(HO_M + j * 128 + lane * 4, nflip), 0, SC1);
    }
    STAMP(s, 4);
    if (tid == 64) __builtin_amdgcn_raw_buffer_store_b32(1u, r, FLM_OFF + s * 3072 + (j / 6) * 128 + (j % 6) * 4, 0, SC1);
    if (s + 2 < NSEG) ux_load(s + 1);
    STAMP(s, 5);
  }
  STAMP(65, 0);
}

__device__ __forceinline__ void role_o(const Args& a, int o24, char* smem) {
  const int tid = threadIdx.x, lane = tid & 63, wave = __builtin_amdgcn_readfirstlane(tid >> 6);
  const int c16 = lane & 15, g = lane >> 4, o = o24 >> 1;
  unsigned char* ws = a.ws;
  u32* tmo = (u32*)(ws + CTL_OFF) + 64;
  __amdgpu_buffer_rsrc_t r = __builtin_amdgcn_make_buffer_rsrc(ws, 0, WS_END, 0x00020000);
  STAMP(64, 0);
  if (wave < 3 && o24 * 3 + wave < NSEG) {
    const int blk = o24 * 3 + wave;
    u32 best[2] = {~0u, ~0u};
    for (int rep = 0; rep < 4; ++rep)
      for (int f = 0; f < 2; ++f) {
        u32* p = (u32*)(ws + HO_OFF + blk * 65536 + homap(32768 - 128 * (rep + 1), f * 8192));
        __hip_atomic_store(p, POISON, __ATOMIC_RELAXED, __HIP_MEMORY_SCOPE_AGENT);
        wait_vm();
        u64 t0, t1;
        asm volatile("s_memtime %0\n\ts_waitcnt lgkmcnt(0)" : "=s"(t0) :: "memory");
        u32 v = __hip_atomic_load(p, __ATOMIC_RELAXED, __HIP_MEMORY_SCOPE_AGENT);
        asm volatile("s_waitcnt vmcnt(0)" : "+v"(v) :: "memory");
        asm volatile("s_memtime %0\n\ts_waitcnt lgkmcnt(0)" : "=s"(t1) :: "memory");
        const u32 d = (u32)(t1 - t0);
        best[f] = d < best[f] ? d : best[f];
      }
    if (lane == 0) __builtin_amdgcn_raw_buffer_store_b32(best[1] < best[0] ? 8192u : 0u, r, FLP_OFF + blk * 4, 0, SC1);
  }
  STAMP(64, 1);
  const u32 lx = wave * 2048 + lane * 16;
  const u32 lm = HO_PM + (wave * 4 + (c16 >> 3)) * 128 + (g & 1) * 64 + (lane & 7) * 8;
  const bool diag = (c16 >> 3) == (g >> 1);
  const u32 redo = ((wave * 2 + (g >> 1)) * 64 + (lane & 7) * 8 + (g & 1) * 4) * 4;
  for (int s = o24 & 1; s < NSEG; s += 2) {
    u32 spins = 0;
    while (!allok(__builtin_amdgcn_raw_buffer_load_b32(r, FLG_OFF + s * 128 + wave * 16 + (lane & 3) * 4, 0, SC1))) {
      if (spin_fail(spins, tmo)) break;
      __builtin_amdgcn_s_sleep(16);
    }
    asm volatile("" ::: "memory");
    u32x4 x0[6], x1[6], um[6];
#pragma unroll
    for (int c = 0; c < 6; ++c) {
      const u32 uo = UX_OFF + ((s * NB + o * 6 + c) * 16 + wave * 2) * 1024 + lane * 16;
      x0[c] = __builtin_amdgcn_raw_buffer_load_b128(r, uo, 0, SC1);
      x1[c] = __builtin_amdgcn_raw_buffer_load_b128(r, uo + 1024, 0, SC1);
    }
    if (wave == 0) {
      while (!allok(__builtin_amdgcn_raw_buffer_load_b32(r, FLM_OFF + s * 3072 + o * 128 + (lane % 6) * 4, 0, SC1))) {
        if (spin_fail(spins, tmo)) break;
        __builtin_amdgcn_s_sleep(4);
      }
    }
    bar();
#pragma unroll
    for (int c = 0; c < 6; ++c)
      um[c] = __builtin_amdgcn_raw_buffer_load_b128(r, UM_OFF + ((s * NB + o * 6 + c) * 8 + wave) * 512 + (lane & 31) * 16, 0, SC1);
    u32x4 p0[4], p1[4]; u32x2 q0[4], q1[4];
    for (;;) {
      u32 m = 0;
#pragma unroll
      for (int u = 0; u < 4; ++u) {
        const u32 tb = PT_OFF + s * PT_STEP + u * 20480;
        p0[u] = __builtin_amdgcn_raw_buffer_load_b128(r, tb + lx, 0, SC1);
        p1[u] = __builtin_amdgcn_raw_buffer_load_b128(r, tb + lx + 1024, 0, SC1);
        q0[u] = __builtin_amdgcn_raw_buffer_load_b64(r, tb + lm, 0, SC1);
        q1[u] = __builtin_amdgcn_raw_buffer_load_b64(r, tb + lm + 256, 0, SC1);
      }
#pragma unroll
      for (int u = 0; u < 4; ++u) m = mx(m, mx(mx(mx4(p0[u]), mx4(p1[u])), mx(mx2(q0[u]), mx2(q1[u]))));
      if (allok(m)) break;
      if (spin_fail(spins, tmo)) break;
    }
#pragma unroll
    for (int c = 0; c < 6; ++c) {
      if (g >= 2) um[c] = u32x4{0, 0, 0, 0};
#pragma unroll
      for (int u = 0; u < 4; ++u) {
        f32x4 d = {0.f, 0.f, 0.f, 0.f};
        const u32x4 pmv = {q0[u].x, q0[u].y, q1[u].x, q1[u].y};
        d = mfma(x0[c], p0[u], d); d = mfma(x1[c], p1[u], d); d = mfma(um[c], pmv, d);
        if (diag) *(f32x4*)(smem + (c * 4 + u) * 4096 + redo) = d;
      }
    }
    bar();
    if (tid < 384) {
      const char* rp = smem + (tid >> 4) * 4096 + (tid & 15) * 16;
      f32x4 acc = *(const f32x4*)rp;
#pragma unroll
      for (int p = 1; p < 16; ++p) acc += *(const f32x4*)(rp + p * 256);
      *(u32x2*)(ws + AO_OFF + ((size_t)(s * 32 + ((tid & 63) >> 1)) * DM + (o * 6 + (tid >> 6)) * 8 + (tid & 1) * 4) * 2) = u32x2{pk(acc[0], acc[1]), pk(acc[2], acc[3])};
    }
    STAMP(s, 0);
  }
}

__device__ __forceinline__ void role_v(const Args& a, int h, char* smem) {
  const int tid = threadIdx.x, lane = tid & 63, wave = __builtin_amdgcn_readfirstlane(tid >> 6);
  const int c16 = lane & 15, g = lane >> 4;
  unsigned char* ws = a.ws;
  __amdgpu_buffer_rsrc_t r = __builtin_amdgcn_make_buffer_rsrc(ws, 0, WS_END, 0x00020000);
  STAMP(64, 0);
  u32x4 WV[KS], WO[5][4];
  {
    const u32 sel = (c16 & 1) ? 0x07060302u : 0x05040100u;
    u32x4 st[18];
#pragma unroll
    for (int i = 0; i < 18; ++i) { const int pc = i * 512 + tid; st[i] = *(const u32x4*)(a.wv + (size_t)(pc >> 4) * (NH * DH) + h * DH + (pc & 15) * 8); }
#pragma unroll
    for (int i = 0; i < 18; ++i) { const int pc = i * 512 + tid; *(u32x4*)(smem + (pc >> 4) * 272 + (pc & 15) * 16) = st[i]; }
#pragma unroll
    for (int i = 0; i < 18; ++i) st[i] = *(const u32x4*)(a.wo + (size_t)h * DH * DM + (i * 512 + tid) * 8);
    bar();
    {
      const char* wl = smem + g * 8 * 272 + wave * 32 + (c16 >> 1) * 4;
#pragma unroll
      for (int t = 0; t < KS; ++t) {
#pragma unroll
        for (int p = 0; p < 4; ++p)
          WV[t][p] = __builtin_amdgcn_perm(*(const u32*)(wl + (t * 32 + 2 * p + 1) * 272), *(const u32*)(wl + (t * 32 + 2 * p) * 272), sel);
        asm volatile("" : "+v"(WV[t]) :: "memory");
      }
    }
    bar();
#pragma unroll
    for (int i = 0; i < 18; ++i) { const int pc = i * 512 + tid; *(u32x4*)(smem + (pc / 72) * 1168 + (pc % 72) * 16) = st[i]; }
    bar();
#pragma unroll
    for (int i = 0; i < 5; ++i) {
      const int ct = wave + 8 * i < 36 ? wave + 8 * i : 0;
      const char* wl = smem + g * 4 * 1168 + ct * 32 + (c16 >> 1) * 4;
#pragma unroll
      for (int es = 0; es < 4; ++es) {
#pragma unroll
        for (int p = 0; p < 4; ++p) {
          const int e0 = 32 * es + 16 * (p >> 1) + 2 * (p & 1);
          WO[i][es][p] = __builtin_amdgcn_perm(*(const u32*)(wl + (e0 + 1) * 1168), *(const u32*)(wl + e0 * 1168), sel);
        }
        asm volatile("" : "+v"(WO[i][es]) :: "memory");
      }
    }
    bar();
  }
  STAMP(64, 1);
  stage_x(ws, 0, wave, lane);
  for (int sg = 0; sg < NSEG; ++sg) {
    wait_vm();
    bar();
    if (sg > 0 && tid == 0) __builtin_amdgcn_raw_buffer_store_b32(1u, r, FLG_OFF + (sg - 1) * 128 + h * 4, 0, SC1);
    if (sg + 1 < NSEG) stage_x(ws, sg + 1, wave, lane);
    {
      const char* xb = smem + L_X + (sg & 1) * XF_SEG + lane * 16;
      f32x4 c0 = {0.f, 0.f, 0.f, 0.f}, c1 = c0;
#pragma unroll
      for (int t = 0; t < KS; ++t) {
        c0 = mfma(WV[t], *(const u32x4*)(xb + t * 1024), c0);
        c1 = mfma(WV[t], *(const u32x4*)(xb + (KS + t) * 1024), c1);
      }
      __builtin_amdgcn_sched_group_barrier(0x100, 8, 0);
#pragma unroll
      for (int t = 0; t < KS - 4; ++t) { __builtin_amdgcn_sched_group_barrier(0x008, 2, 0); __builtin_amdgcn_sched_group_barrier(0x100, 2, 0); }
      __builtin_amdgcn_sched_group_barrier(0x008, 8, 0);
      char* vt = smem + L_QM + (wave >> 1) * 1024 + lane * 16 + (wave & 1) * 8;
      *(u32x2*)vt = u32x2{pk(c0[0], c0[1]), pk(c0[2], c0[3])};
      *(u32x2*)(vt + 4096) = u32x2{pk(c1[0], c1[1]), pk(c1[2], c1[3])};
    }
    bar();
    u32x4 vf[2][4];
#pragma unroll
    for (int rt = 0; rt < 2; ++rt)
#pragma unroll
      for (int es = 0; es < 4; ++es) vf[rt][es] = *(const u32x4*)(smem + L_QM + rt * 4096 + es * 1024 + lane * 16);
#pragma unroll
    for (int i = 0; i < 5; ++i) {
      const int ct = wave + 8 * i;
      if (ct < 36) {
        f32x4 d0 = {0.f, 0.f, 0.f, 0.f}, d1 = d0;
#pragma unroll
        for (int es = 0; es < 4; ++es) { d0 = mfma(vf[0][es], WO[i][es], d0); d1 = mfma(vf[1][es], WO[i][es], d1); }
        const u32x4 o = {pk(d0[0], d0[1]), pk(d0[2], d0[3]), pk(d1[0], d1[1]), pk(d1[2], d1[3])};
        __builtin_amdgcn_raw_buffer_store_b128(o, r, UX_OFF + ((sg * NB + ct * 2 + (c16 >> 3)) * 16 + (h >> 1)) * 1024 + (g * 16 + (h & 1) * 8 + (lane & 7)) * 16, 0, SC1);
      }
    }
    if (sg == 0) STAMP(64, 2);
  }
  wait_vm();
  bar();
  if (tid == 0) __builtin_amdgcn_raw_buffer_store_b32(1u, r, FLG_OFF + (NSEG - 1) * 128 + h * 4, 0, SC1);
  STAMP(64, 3);
}

__device__ __forceinline__ float gelu_tanh(float x) {
  const float u = 0.7978845608028654f * (x + 0.044715f * x * x * x);
  const float e = __expf(2.f * u);
  return 0.5f * x * (2.f - 2.f / (e + 1.f));
}

__device__ __forceinline__ void role_f(const Args& a, int f, char* smem) {
  const int tid = threadIdx.x, lane = tid & 63, wave = __builtin_amdgcn_readfirstlane(tid >> 6);
  const int c16 = lane & 15, g = lane >> 4;
  unsigned char* ws = a.ws;
  u32* tmo = (u32*)(ws + CTL_OFF) + 64;
  u32* cnt = (u32*)(ws + CTL_OFF) + 128;
  __amdgpu_buffer_rsrc_t r = __builtin_amdgcn_make_buffer_rsrc(ws, 0, WS_END, 0x00020000);
  STAMP(64, 0);
  auto stage = [&](int sg) {
#pragma unroll
    for (int i = 0; i < 5; ++i) {
      const int p = min(i * 8 + wave, 35);
      glds16(ws + XF_OFF + sg * XF_SEG + p * 1024 + lane * 16, (sg & 3) * XF_SEG + p * 1024);
    }
  };
  if (f >= 64) {
    const int h = f - 64;
    u32x4 WW[2][KS];
    load_wqk(a, h, WW, smem);
    wait_vm();
    stage(0); stage(1);
    for (int sg = 0; sg < NSEG; ++sg) {
      if (sg < 4) wait_vm();
      else asm volatile("s_waitcnt vmcnt(5)" ::: "memory");
      bar();
      if (sg >= 1 && tid == 0) __builtin_amdgcn_raw_buffer_store_b32(1u, r, FLQ_OFF + (sg < 4 ? sg - 1 : sg - 2) * 128 + h * 4, 0, SC1);
      stage(min(sg + 2, NSEG - 1));
#pragma unroll
      for (int rt = 0; rt < 2; ++rt) {
        f32x4 c0, c1;
        proj2<true, 1024>(WW[0], WW[1], smem + (sg & 3) * XF_SEG + rt * KS * 1024 + lane * 16, c0, c1);
        const u32x4 o = {pk(c0[0], c1[0]), pk(c0[1], c1[1]), pk(c0[2], c1[2]), pk(c0[3], c1[3])};
        __builtin_amdgcn_raw_buffer_store_b128(o, r, QX_OFF + ((sg * NH + h) * 16 + (wave >> 2) * 8 + rt * 4 + (wave & 3)) * 1024 + lane * 16, 0, SC1);
      }
    }
    wait_vm();
    bar();
    if (tid < 2) __builtin_amdgcn_raw_buffer_store_b32(1u, r, FLQ_OFF + (NSEG - 2 + tid) * 128 + h * 4, 0, SC1);
  }
  for (int it = f * 27 + wave; it < f * 27 + 27; it += 8) {
    const int ks = it / 18, bb = it % 18;
    u32 d[8];
#pragma unroll
    for (int jj = 0; jj < 8; ++jj)
      d[jj] = *(const u32*)(a.wd + (size_t)(ks * 32 + (jj >> 2) * 16 + g * 4 + (jj & 3)) * DM + bb * 32 + c16 * 2);
    const u32x4 lo = {plo(d[0], d[1]), plo(d[2], d[3]), plo(d[4], d[5]), plo(d[6], d[7])};
    const u32x4 hi = {phi(d[0], d[1]), phi(d[2], d[3]), phi(d[4], d[5]), phi(d[6], d[7])};
    __builtin_amdgcn_raw_buffer_store_b128(lo, r, WDF_OFF + (bb * 144 + ks) * 2048 + lane * 16, 0, SC1);
    __builtin_amdgcn_raw_buffer_store_b128(hi, r, WDF_OFF + (bb * 144 + ks) * 2048 + 1024 + lane * 16, 0, SC1);
  }
#pragma unroll 1
  for (int job = 0; job < 3 && f < 64; ++job) {
    int tid = threadIdx.x;
    asm volatile("" : "+v"(tid));
    const int hv = wave & 3, rt = wave >> 2;
    const int u0 = f * 72, sl = (u0 >> 6) + job;
    const int q0 = job == 0 ? (u0 & 63) : 0, q1 = min(u0 + 72 - sl * 64, 64);
    if (q1 <= q0) break;
    u32x4 WW[2][KS];
#pragma unroll
    for (int mat = 0; mat < 2; ++mat) {
      __syncthreads();
      u32x4 st[9];
#pragma unroll
      for (int i = 0; i < 9; ++i) {
        const int pc = i * 512 + tid;
        st[i] = *(const u32x4*)((mat ? a.wu : a.wg) + (size_t)(pc >> 3) * FF + sl * 64 + (pc & 7) * 8);
      }
#pragma unroll
      for (int i = 0; i < 9; ++i) { const int pc = i * 512 + tid; *(u32x4*)(smem + (pc >> 3) * 144 + (pc & 7) * 16) = st[i]; }
      __syncthreads();
      const char* wl = smem + g * 8 * 144 + hv * 32 + (c16 >> 1) * 4;
      const u32 sel = (c16 & 1) ? 0x07060302u : 0x05040100u;
#pragma unroll
      for (int t = 0; t < KS; ++t) {
#pragma unroll
        for (int p = 0; p < 4; ++p) {
          const int o = (t * 32 + 2 * p) * 144;
          WW[mat][t][p] = __builtin_amdgcn_perm(*(const u32*)(wl + o + 144), *(const u32*)(wl + o), sel);
        }
        asm volatile("" : "+v"(WW[mat][t]) :: "memory");
      }
    }
    __syncthreads();
    if (job == 0) STAMP(64, 1);
    stage(q0); stage(min(q0 + 1, q1 - 1));
    for (int sg = q0; sg < q1; ++sg) {
      asm volatile("s_waitcnt vmcnt(5)" ::: "memory");
      bar();
      stage(min(sg + 2, q1 - 1));
      f32x4 cg, cu;
      proj2<true, 1024>(WW[0], WW[1], smem + (sg & 3) * XF_SEG + rt * KS * 1024 + lane * 16, cg, cu);
      float hh[4];
#pragma unroll
      for (int i = 0; i < 4; ++i) hh[i] = bfr(gelu_tanh(bfr(cg[i]))) * bfr(cu[i]);
      const u32x2 o = {pk(hh[0], hh[1]), pk(hh[2], hh[3])};
      __builtin_amdgcn_raw_buffer_store_b64(o, r, H_OFF + ((sg * 2 + rt) * 144 + sl * 2 + (hv >> 1)) * 1024 + lane * 16 + (hv & 1) * 8, 0, SC1);
    }
    wait_vm();
  }
  wait_vm();
  __syncthreads();
  STAMP(64, 2);
  if (tid == 0) {
    __hip_atomic_fetch_add(cnt, 1u, __ATOMIC_RELAXED, __HIP_MEMORY_SCOPE_AGENT);
    u32 spins = 0;
    while (__hip_atomic_load(cnt, __ATOMIC_RELAXED, __HIP_MEMORY_SCOPE_AGENT) < (u32)NF) {
      __builtin_amdgcn_s_sleep(8);
      if (spin_fail(spins, tmo)) break;
    }
    __builtin_amdgcn_fence(__ATOMIC_ACQUIRE, "agent");
    wait_vm();
  }
  __syncthreads();
  STAMP(64, 3);
  {
    const int tm = f / 6, b3 = (f % 6) * 3, tg = wave & 3, kh = wave >> 2;
    const unsigned char* hb = ws + H_OFF + ((size_t)(tm * 8 + tg * 2) * 144 + kh * 72) * 1024 + lane * 16;
    const unsigned char* wb = ws + WDF_OFF + (size_t)(b3 * 144 + kh * 72) * 2048 + lane * 16;
    f32x4 acc[2][6];
#pragma unroll
    for (int u = 0; u < 2; ++u)
#pragma unroll
      for (int i = 0; i < 6; ++i) acc[u][i] = f32x4{0.f, 0.f, 0.f, 0.f};
    u32x4 hf[2][2][2], wf[2][2][6];
    auto ld = [&](int st, int t0) {
#pragma unroll
      for (int t = 0; t < 2; ++t) {
        hf[st][t][0] = *(const u32x4*)(hb + (t0 + t) * 1024); hf[st][t][1] = *(const u32x4*)(hb + (144 + t0 + t) * 1024);
#pragma unroll
        for (int i = 0; i < 6; ++i) wf[st][t][i] = *(const u32x4*)(wb + ((i >> 1) * 144 + t0 + t) * 2048 + (i & 1) * 1024);
      }
    };
    auto mm = [&](int st) {
#pragma unroll
      for (int t = 0; t < 2; ++t)
#pragma unroll
        for (int i = 0; i < 6; ++i) { acc[0][i] = mfma(wf[st][t][i], hf[st][t][0], acc[0][i]); acc[1][i] = mfma(wf[st][t][i], hf[st][t][1], acc[1][i]); }
    };
    ld(0, 0);
#pragma unroll 1
    for (int t0 = 0; t0 < 72; t0 += 4) {
      ld(1, t0 + 2);
      __builtin_amdgcn_sched_barrier(0);
      mm(0);
      __builtin_amdgcn_sched_barrier(0);
      ld(0, min(t0 + 4, 70));
      __builtin_amdgcn_sched_barrier(0);
      mm(1);
      __builtin_amdgcn_sched_barrier(0);
    }
    STAMP(64, 4);
    if (kh == 1) {
#pragma unroll
      for (int u = 0; u < 2; ++u)
#pragma unroll
        for (int i = 0; i < 6; ++i) *(f32x4*)(smem + ((tg * 12 + u * 6 + i) * 64 + lane) * 16) = acc[u][i];
    }
    __syncthreads();
    if (kh == 0) {
#pragma unroll
      for (int u = 0; u < 2; ++u)
#pragma unroll
        for (int b = 0; b < 3; ++b) {
          const f32x4 e0 = acc[u][2 * b] + *(const f32x4*)(smem + ((tg * 12 + u * 6 + 2 * b) * 64 + lane) * 16);
          const f32x4 e1 = acc[u][2 * b + 1] + *(const f32x4*)(smem + ((tg * 12 + u * 6 + 2 * b + 1) * 64 + lane) * 16);
          const u32x4 o = {pk(e0[0], e1[0]), pk(e0[1], e1[1]), pk(e0[2], e1[2]), pk(e0[3], e1[3])};
          *(u32x4*)(ws + FO_OFF + ((size_t)(tm * 128 + tg * 32 + u * 16 + c16) * DM + (b3 + b) * 32 + g * 8) * 2) = o;
        }
    }
  }
}

__global__ void __launch_bounds__(512, 2) k2_main(Args a) {
  extern __shared__ __attribute__((aligned(16))) char smem[];
  if (threadIdx.x == 0) {
    u32* ctl = (u32*)(a.ws + CTL_OFF);
    const u32 x = __builtin_amdgcn_s_getreg((4 - 1) << 11 | 0 << 6 | 20) & 7u;
    const u32 rank = __hip_atomic_fetch_add(ctl + 256 + x * 32, 1u, __ATOMIC_RELAXED, __HIP_MEMORY_SCOPE_AGENT);
    u32 slot = rank * 8 + x;
    if (rank >= 32) {
      u32 k = __hip_atomic_fetch_add(ctl + 192, 1u, __ATOMIC_RELAXED, __HIP_MEMORY_SCOPE_AGENT), spins = 0;
      for (;;) {
        u32 tot = 0;
        for (int i = 0; i < 8; ++i) tot += __hip_atomic_load(ctl + 256 + i * 32, __ATOMIC_RELAXED, __HIP_MEMORY_SCOPE_AGENT);
        if (tot >= 256u || spin_fail(spins, ctl + 64)) break;
      }
      slot = 256;
      for (int i = 0; i < 8 && slot == 256; ++i) {
        const u32 c = __hip_atomic_load(ctl + 256 + i * 32, __ATOMIC_RELAXED, __HIP_MEMORY_SCOPE_AGENT), fr = c < 32u ? 32u - c : 0u;
        if (k < fr) slot = (c + k) * 8 + i; else k -= fr;
      }
    }
    *(u32*)smem = slot;
  }
  __syncthreads();
  const int b = __builtin_amdgcn_readfirstlane(*(const u32*)smem), rr = b & 7, q = b >> 3;
  __syncthreads();
  if (b >= 256) return;
  if (rr == 0) role_a(a, q, smem);
  else if (rr < 4 && q < 24) role_b(a, (rr - 1) * 24 + q, smem);
  else if (rr == 4) role_v(a, q, smem);
  else if (rr > 4) role_f(a, (rr - 5) * 32 + q, smem);
  else role_o(a, (rr - 1) * 8 + (q - 24), smem);
  DUMP();
}

__global__ void __launch_bounds__(256) k3_final(Args a) {
  const int i = blockIdx.x * 256 + threadIdx.x;
  const u32x4 x = ((const u32x4*)a.x)[i], p = ((const u32x4*)(a.ws + AO_OFF))[i], f = ((const u32x4*)(a.ws + FO_OFF))[i];
  u32x4 o;
#pragma unroll
  for (int k = 0; k < 4; ++k)
    o[k] = pk(bfr(bflo(x[k]) + bflo(p[k])) + bflo(f[k]), bfr(bfhi(x[k]) + bfhi(p[k])) + bfhi(f[k]));
  ((u32x4*)a.out)[i] = o;
}

extern "C" void kernel_launch(void* const* d_in, const int* in_sizes, int n_in, void* d_out, int out_size, void* d_ws,
                              size_t ws_size, hipStream_t stream) {
  Args a;
  a.x = (const u16*)d_in[0]; a.mem0 = (const u16*)d_in[1]; a.wq = (const u16*)d_in[2]; a.wk = (const u16*)d_in[3];
  a.wv = (const u16*)d_in[4]; a.wo = (const u16*)d_in[5]; a.wg = (const u16*)d_in[8]; a.wu = (const u16*)d_in[9];
  a.wd = (const u16*)d_in[10];
  a.ws = (unsigned char*)(((uintptr_t)d_ws + 65535) & ~(uintptr_t)65535);
  a.out = (u16*)d_out;
  (void)hipFuncSetAttribute((const void*)k2_main, hipFuncAttributeMaxDynamicSharedMemorySize, LDS_BYTES);
  k1_prep<<<K1_GRID, 256, 0, stream>>>(a);
  k2_main<<<256, 512, LDS_BYTES, stream>>>(a);
  k3_final<<<S_ * DM / 8 / 256, 256, 0, stream>>>(a);
}
```

```cpp
#include <hip/hip_runtime.h>
#include <hip/hip_bf16.h>

typedef unsigned u32;
typedef unsigned long long u64;
typedef unsigned short u16;
typedef __attribute__((ext_vector_type(8))) __bf16 bf16x8;
typedef __attribute__((ext_vector_type(2))) __bf16 bf16x2;
typedef __attribute__((ext_vector_type(4))) float f32x4;
typedef __attribute__((ext_vector_type(2))) float f32x2;
typedef __attribute__((ext_vector_type(4))) unsigned u32x4;
typedef __attribute__((ext_vector_type(2))) unsigned u32x2;

constexpr int S_ = 2048, DM = 576, NH = 32, DH = 128, FF = 4608, NSEG = 64, KS = 18;
constexpr int NB = 72, NF = 96;
constexpr u32 HO_OFF = 0;
constexpr u32 PT_OFF = HO_OFF + NSEG * 65536;
constexpr u32 PT_STEP = 4 * 20480;
constexpr u32 FLG_OFF = PT_OFF + NSEG * PT_STEP;
constexpr u32 FLM_OFF = FLG_OFF + NSEG * 128;
constexpr u32 FLQ_OFF = FLM_OFF + NSEG * 3072;
constexpr u32 FLA_OFF = FLQ_OFF + NSEG * 128;
constexpr u32 FLF_OFF = FLA_OFF + NSEG * 128;
constexpr int SPLIT = 48;
constexpr u32 FLP_OFF = FLF_OFF + NF * 128;
constexpr u32 POISON_BYTES = FLP_OFF + 256;
constexpr u32 CTL_OFF = (POISON_BYTES + 4095) & ~4095u;
constexpr u32 M0_OFF = CTL_OFF + 4096;
constexpr u32 XF_OFF = M0_OFF + 12288;
constexpr u32 XF_SEG = 2 * KS * 1024;
constexpr u32 WVO_OFF = XF_OFF + NSEG * XF_SEG;
constexpr u32 WVO_J = 16 * KS * 1024;
constexpr u32 UX_OFF = WVO_OFF + NB * WVO_J;
constexpr u32 UM_OFF = UX_OFF + NSEG * NB * 16384;
constexpr u32 QX_OFF = UM_OFF + NSEG * NB * 4096;
constexpr u32 H_OFF = QX_OFF + NSEG * NH * 16384;
constexpr u32 WDF_OFF = H_OFF + 128 * 144 * 1024;
constexpr u32 FO_OFF = WDF_OFF + 9 * 144 * 4096;
constexpr u32 AO_OFF = FO_OFF + S_ * DM * 2;
constexpr u32 WS_END = AO_OFF + S_ * DM * 2;
constexpr u32 POISON = 0xFFFFFFFFu;
constexpr int SC1 = 16;
constexpr u32 SPIN_CAP = 400000u;
constexpr int LDS_BYTES = 160 * 1024;

__device__ __forceinline__ u32 pk(float lo, float hi) {
  f32x2 v = {lo, hi};
  return __builtin_bit_cast(u32, __builtin_convertvector(v, bf16x2));
}
__device__ __forceinline__ u32 pks(float lo, float hi) { u32 r = pk(lo, hi); return r == POISON ? 0x7FC07FC0u : r; }
__device__ __forceinline__ float bfr(float x) { return (float)(__bf16)x; }
__device__ __forceinline__ float bflo(u32 w) { return __uint_as_float(w << 16); }
__device__ __forceinline__ float bfhi(u32 w) { return __uint_as_float(w & 0xFFFF0000u); }
__device__ __forceinline__ u32 fin(u32 x, u32 p, u32 f) {
  return pk(bfr(bflo(x) + bflo(p)) + bflo(f), bfr(bfhi(x) + bfhi(p)) + bfhi(f));
}
__device__ __forceinline__ f32x4 mfma(u32x4 a, u32x4 b, f32x4 c) {
  return __builtin_amdgcn_mfma_f32_16x16x32_bf16(__builtin_bit_cast(bf16x8, a), __builtin_bit_cast(bf16x8, b), c, 0, 0, 0);
}
__device__ __forceinline__ u32 plo(u32 a, u32 b) { return __builtin_amdgcn_perm(b, a, 0x05040100u); }
__device__ __forceinline__ u32 phi(u32 a, u32 b) { return __builtin_amdgcn_perm(b, a, 0x07060302u); }
__device__ __forceinline__ float vmax(float a, float b) { float r; asm("v_max_f32 %0, %1, %2" : "=v"(r) : "v"(a), "v"(b)); return r; }
__device__ __forceinline__ float vmax3(float a, float b, float c) { float r; asm("v_max3_f32 %0, %1, %2, %3" : "=v"(r) : "v"(a), "v"(b), "v"(c)); return r; }
__device__ __forceinline__ float xmax(float x) {
  auto s = __builtin_amdgcn_permlane16_swap(__float_as_uint(x), __float_as_uint(x), false, false);
  x = vmax(__uint_as_float(s[0]), __uint_as_float(s[1]));
  auto t = __builtin_amdgcn_permlane32_swap(__float_as_uint(x), __float_as_uint(x), false, false);
  return vmax(__uint_as_float(t[0]), __uint_as_float(t[1]));
}
__device__ __forceinline__ float xsum(float x) {
  auto s = __builtin_amdgcn_permlane16_swap(__float_as_uint(x), __float_as_uint(x), false, false);
  x = __uint_as_float(s[0]) + __uint_as_float(s[1]);
  auto t = __builtin_amdgcn_permlane32_swap(__float_as_uint(x), __float_as_uint(x), false, false);
  return __uint_as_float(t[0]) + __uint_as_float(t[1]);
}
__device__ __forceinline__ float ror8(float x) {
  return __builtin_bit_cast(float, __builtin_amdgcn_mov_dpp(__builtin_bit_cast(int, x), 0x128, 0xf, 0xf, true));
}
__device__ __forceinline__ u32 mx(u32 a, u32 b) { return a > b ? a : b; }
__device__ __forceinline__ u32 mx4(u32x4 v) { return mx(mx(v.x, v.y), mx(v.z, v.w)); }
__device__ __forceinline__ u32 mx2(u32x2 v) { return mx(v.x, v.y); }
__device__ __forceinline__ bool allok(u32 m) { return __all(m != POISON); }
__device__ __forceinline__ void glds16(const void* g, u32 lds) {
  u32 keep;
  asm volatile("s_mov_b32 %0, m0\n\ts_mov_b32 m0, %2\n\ts_nop 0\n\tglobal_load_lds_dwordx4 %1, off\n\ts_mov_b32 m0, %0"
               : "=&s"(keep) : "v"(g), "s"(lds) : "memory");
}
__device__ __forceinline__ void wait_vm() { asm volatile("s_waitcnt vmcnt(0)" ::: "memory"); }
__device__ __forceinline__ void bar() {
  asm volatile("s_waitcnt lgkmcnt(0)" ::: "memory");
  __builtin_amdgcn_s_barrier();
  asm volatile("" ::: "memory");
}
__device__ __forceinline__ bool spin_fail(u32& spins, u32* tmo) {
  ++spins;
  if (spins > SPIN_CAP) { __hip_atomic_store(tmo, 1u, __ATOMIC_RELAXED, __HIP_MEMORY_SCOPE_AGENT); return true; }
  if ((spins & 1023u) == 0u && __hip_atomic_load(tmo, __ATOMIC_RELAXED, __HIP_MEMORY_SCOPE_AGENT) != 0u) return true;
  return false;
}
__device__ __forceinline__ u32 homap(u32 lo, u32 flip) { return ((lo & ~8191u) << 1) + (lo & 8191u) + flip; }
constexpr u32 HO_PM = 16384, HO_M = 20480;

#define STAMP(step, k) do {} while (0)
#define STAMPC(step, k) do {} while (0)
#define DUMP() do {} while (0)
struct Args {
  const u16* x; const u16* mem0; const u16* wq; const u16* wk; const u16* wv; const u16* wo;
  const u16* wg; const u16* wu; const u16* wd; unsigned char* ws; u16* out;
};

constexpr int K1_WVO = 2592, K1_LN = 256, K1_LNB = K1_LN / 4, K1_JOBS = K1_WVO + K1_LN, K1_GRID = K1_JOBS / 4 + 8;
__global__ void __launch_bounds__(256) k1_prep(Args a) {
  const int lane = threadIdx.x & 63, wave = __builtin_amdgcn_readfirstlane(threadIdx.x >> 6);
  const int b = blockIdx.x;
  unsigned char* ws = a.ws;
  const int c16 = lane & 15, g = lane >> 4;
  if (b == K1_GRID - 1) {
    for (int i = threadIdx.x; i < 1024; i += 256) ((u32*)(ws + CTL_OFF))[i] = 0u;
    for (int i = threadIdx.x; i < 576; i += 256) {
      u32x4 v = *(const u32x4*)(a.mem0 + (i & 7) * DM + (i >> 3) * 8);
      if (v.x == POISON) v.x = 0x7FC07FC0u; if (v.y == POISON) v.y = 0x7FC07FC0u;
      if (v.z == POISON) v.z = 0x7FC07FC0u; if (v.w == POISON) v.w = 0x7FC07FC0u;
      *(u32x4*)(ws + M0_OFF + i * 16) = v;
    }
    return;
  }
  if (b >= K1_JOBS / 4) return;
  const int job = b * 4 + wave;
  auto poison = [&]() {
    const u32x4 pv = {POISON, POISON, POISON, POISON};
    for (u32 o = (job * 64 + lane) * 16; o < POISON_BYTES; o += K1_JOBS * 64 * 16) *(u32x4*)(ws + o) = pv;
  };
  if (b >= K1_LNB) {
    const int jx = ((b - K1_LNB) >> 3) * 4 + wave, h = (b & 7) * 4 + jx / 81, kp = (jx / 9) % 9, bb = jx % 9;
    u32x2 rw[4][8];
    u32x4 af[2][2][4];
#pragma unroll
    for (int es = 0; es < 4; ++es)
#pragma unroll
      for (int jj = 0; jj < 8; ++jj)
        rw[es][jj] = *(const u32x2*)(a.wo + (size_t)(h * DH + es * 32 + g * 8 + jj) * DM + bb * 64 + c16 * 4);
#pragma unroll
    for (int i = 0; i < 16; ++i) {
      const int k = (kp * 2 + (i >> 3)) * 32 + (c16 >> 2) * 8 + (c16 & 3) + ((i >> 2) & 1) * 4;
      af[i >> 3][(i >> 2) & 1][i & 3] = *(const u32x4*)(a.wv + (size_t)k * (NH * DH) + h * DH + (i & 3) * 32 + g * 8);
    }
    poison();
    __builtin_amdgcn_sched_barrier(0);
    u32x4 bf[4][4];
#pragma unroll
    for (int es = 0; es < 4; ++es)
#pragma unroll
      for (int p = 0; p < 4; ++p) {
        bf[es][0][p] = plo(rw[es][2 * p].x, rw[es][2 * p + 1].x); bf[es][1][p] = phi(rw[es][2 * p].x, rw[es][2 * p + 1].x);
        bf[es][2][p] = plo(rw[es][2 * p].y, rw[es][2 * p + 1].y); bf[es][3][p] = phi(rw[es][2 * p].y, rw[es][2 * p + 1].y);
      }
#pragma unroll
    for (int tt = 0; tt < 2; ++tt) {
      const int t = kp * 2 + tt;
      f32x4 acc[2][4];
#pragma unroll
      for (int rt = 0; rt < 2; ++rt)
#pragma unroll
        for (int i = 0; i < 4; ++i) {
          acc[rt][i] = f32x4{0.f, 0.f, 0.f, 0.f};
#pragma unroll
          for (int es = 0; es < 4; ++es) acc[rt][i] = mfma(af[tt][rt][es], bf[es][i], acc[rt][i]);
        }
      const int j = bb * 8 + (c16 >> 1);
      unsigned char* dst = ws + WVO_OFF + (size_t)j * WVO_J + ((h >> 1) * KS + t) * 1024 + (g * 16 + (h & 1) * 8 + (c16 & 1) * 4) * 16;
#pragma unroll
      for (int i = 0; i < 4; ++i) {
        u32x4 v = {pk(acc[0][i][0], acc[0][i][1]), pk(acc[0][i][2], acc[0][i][3]), pk(acc[1][i][0], acc[1][i][1]), pk(acc[1][i][2], acc[1][i][3])};
        *(u32x4*)(dst + i * 16) = v;
      }
    }
  } else {
    u32x4 vv[2][5];
#pragma unroll
    for (int rr = 0; rr < 2; ++rr)
#pragma unroll
    for (int i = 0; i < 5; ++i) {
      vv[rr][i] = u32x4{0, 0, 0, 0};
      if (i < 4 || c16 < 8) vv[rr][i] = *(const u32x4*)(a.x + (size_t)(job * 8 + rr * 4 + g) * DM + (i * 16 + c16) * 8);
    }
    poison();
#pragma unroll
    for (int rr = 0; rr < 2; ++rr) {
    const int row = job * 8 + rr * 4 + g;
    const u32x4 (&v)[5] = vv[rr];
    float sm = 0.f;
#pragma unroll
    for (int i = 0; i < 20; ++i) sm += bflo(v[i >> 2][i & 3]) + bfhi(v[i >> 2][i & 3]);
#pragma unroll
    for (int o = 1; o < 16; o <<= 1) sm += __shfl_xor(sm, o);
    const float mean = sm * (1.f / DM);
    float q = 0.f;
#pragma unroll
    for (int i = 0; i < 20; ++i) {
      const float d0 = bflo(v[i >> 2][i & 3]) - mean, d1 = bfhi(v[i >> 2][i & 3]) - mean;
      q += (i < 16 || c16 < 8) ? d0 * d0 + d1 * d1 : 0.f;
    }
#pragma unroll
    for (int o = 1; o < 16; o <<= 1) q += __shfl_xor(q, o);
    const float mb = bfr(mean), den = bfr(sqrtf(bfr(bfr(q * (1.f / DM)) + 1e-5f)));
#pragma unroll
    for (int i = 0; i < 5; ++i) {
      u32x4 o;
#pragma unroll
      for (int k = 0; k < 4; ++k) o[k] = pk(bfr(bflo(v[i][k]) - mb) / den, bfr(bfhi(v[i][k]) - mb) / den);
      const int ch = i * 16 + c16;
      if (i < 4 || c16 < 8) *(u32x4*)(ws + XF_OFF + ((row >> 4) * KS + (ch >> 2)) * 1024 + ((ch & 3) * 16 + (row & 15)) * 16) = o;
    }
    }
  }
}

constexpr u32 L_X = 0;
constexpr u32 L_M = 2 * XF_SEG;
constexpr u32 L_QM = L_M + 9216;
constexpr u32 L_QX = L_QM + 8192;

template <bool WA, int ST>
__device__ __forceinline__ void proj2(const u32x4 (&W0)[KS], const u32x4 (&W1)[KS], const char* xb, f32x4& c0, f32x4& c1) {
  u32x4 xf[KS];
#pragma unroll
  for (int t = 0; t < KS; ++t) xf[t] = *(const u32x4*)(xb + t * ST);
  c0 = f32x4{0.f, 0.f, 0.f, 0.f}; c1 = c0;
#pragma unroll
  for (int t = 0; t < KS; ++t) {
    c0 = WA ? mfma(W0[t], xf[t], c0) : mfma(xf[t], W0[t], c0);
    c1 = WA ? mfma(W1[t], xf[t], c1) : mfma(xf[t], W1[t], c1);
  }
  __builtin_amdgcn_sched_group_barrier(0x100, 6, 0);
#pragma unroll
  for (int t = 0; t < KS - 6; ++t) { __builtin_amdgcn_sched_group_barrier(0x008, 2, 0); __builtin_amdgcn_sched_group_barrier(0x100, 1, 0); }
  __builtin_amdgcn_sched_group_barrier(0x008, 12, 0);
}
__device__ __forceinline__ void stage_x(const unsigned char* ws, int seg, int wave, int lane) {
  const unsigned char* src = ws + XF_OFF + seg * XF_SEG + lane * 16;
  const u32 dst = L_X + (seg & 1) * XF_SEG;
#pragma unroll
  for (int i = 0; i < 5; ++i) {
    const int p = i * 8 + wave;
    if (p < 36) glds16(src + p * 1024, dst + p * 1024);
  }
}
__device__ __forceinline__ u32 get_flips(__amdgpu_buffer_rsrc_t r, int lane, u32* tmo) {
  u32 f, spins = 0;
  for (;;) {
    f = __builtin_amdgcn_raw_buffer_load_b32(r, FLP_OFF + lane * 4, 0, SC1);
    if (allok(f)) break;
    if (spin_fail(spins, tmo)) break;
    __builtin_amdgcn_s_sleep(8);
  }
  return f & 8192u;
}
__device__ __forceinline__ void gather_m(__amdgpu_buffer_rsrc_t r, u32 base, u32 flip, bool first, char* smem, int wave, int lane, u32* tmo) {
  const u32 l0 = (wave * 64 + lane) * 16, l1 = (512 + wave * 8 + (lane & 7)) * 16;
  const u32 o0 = first ? M0_OFF + l0 : base + homap(HO_M + l0, flip), o1 = first ? M0_OFF + l1 : base + homap(HO_M + l1, flip);
  u32 spins = 0;
  u32x4 v, w;
  for (;;) {
    v = __builtin_amdgcn_raw_buffer_load_b128(r, o0, 0, SC1);
    w = __builtin_amdgcn_raw_buffer_load_b128(r, o1, 0, SC1);
    if (allok(mx(mx4(v), mx4(w)))) break;
    if (spin_fail(spins, tmo)) break;
  }
  *(u32x4*)(smem + L_M + l0) = v;
  if (lane < 8) *(u32x4*)(smem + L_M + l1) = w;
  wait_vm();
}

__device__ __forceinline__ void load_wqk(const Args& a, int h, u32x4 (&W)[2][KS], char* smem) {
  const int tid = threadIdx.x, lane = tid & 63, wave = __builtin_amdgcn_readfirstlane(tid >> 6);
  const int c16 = lane & 15, g = lane >> 4;
  {
    u32x4 st[18];
    auto ldw = [&](int hf) {
#pragma unroll
      for (int i = 0; i < 18; ++i) {
        const int pc = i * 512 + tid;
        st[i] = *(const u32x4*)(((pc & 16) ? a.wk : a.wq) + (size_t)(hf * 288 + (pc >> 5)) * (NH * DH) + h * DH + (pc & 15) * 8);
      }
    };
    ldw(0);
#pragma unroll
    for (int hf = 0; hf < 2; ++hf) {
#pragma unroll
      for (int i = 0; i < 18; ++i) { const int pc = i * 512 + tid; *(u32x4*)(smem + (pc >> 5) * 528 + (pc & 31) * 16) = st[i]; }
      if (hf == 0) ldw(1);
      bar();
      const char* wl = smem + g * 8 * 528 + wave * 64 + c16 * 4;
#pragma unroll
      for (int t = 0; t < 9; ++t) {
#pragma unroll
        for (int p = 0; p < 4; ++p) {
          const u32 d0 = *(const u32*)(wl + (t * 32 + 2 * p) * 528), d1 = *(const u32*)(wl + (t * 32 + 2 * p + 1) * 528);
          W[0][hf * 9 + t][p] = plo(d0, d1); W[1][hf * 9 + t][p] = phi(d0, d1);
        }
        asm volatile("" : "+v"(W[0][hf * 9 + t]), "+v"(W[1][hf * 9 + t]) :: "memory");
      }
      bar();
    }
  }
}

__device__ __forceinline__ void role_a(const Args& a, int h, char* smem) {
  const int tid = threadIdx.x, lane = tid & 63, wave = __builtin_amdgcn_readfirstlane(tid >> 6);
  const int c16 = lane & 15, g = lane >> 4;
  unsigned char* ws = a.ws;
  u32* tmo = (u32*)(ws + CTL_OFF) + 64;
  __amdgpu_buffer_rsrc_t r = __builtin_amdgcn_make_buffer_rsrc(ws, 0, WS_END, 0x00020000);
  STAMP(64, 0);
  u32x4 W[2][KS];
  load_wqk(a, h, W, smem);
  const u32 flips = get_flips(r, lane, tmo);
  const u32 lx = (((h >> 1) * 4 + g) * 16 + (h & 1) * 8 + (lane & 7)) * 16, lm = HO_PM + h * 128 + g * 64 + (lane & 7) * 8;
  STAMP(64, 1); STAMPC(65, 0); STAMP(65, 1);
  u32x4 kf[8];
  u32 fl = POISON;
  auto q_load = [&](int seg) {
    const u32 fo = FLQ_OFF + seg * 128 + h * 4;
    u32 spins = 0;
    while (!allok(fl)) {
      fl = __builtin_amdgcn_raw_buffer_load_b32(r, fo, 0, SC1);
      if (spin_fail(spins, tmo)) break;
    }
    asm volatile("" ::: "memory");
    const u32 qo = QX_OFF + ((seg * NH + h) * 16 + (wave - 1) * 4) * 1024 + lane * 16;
#pragma unroll
    for (int w = 0; w < 4; ++w) kf[w] = __builtin_amdgcn_raw_buffer_load_b128(r, qo + w * 1024, 0, SC1);
    if (wave == 3) {
#pragma unroll
      for (int w = 4; w < 8; ++w) kf[w] = __builtin_amdgcn_raw_buffer_load_b128(r, qo + w * 1024, 0, SC1);
#pragma unroll
      for (int w = 0; w < 8; ++w) *(u32x4*)(smem + L_QX + (seg & 1) * 16384 + 8192 + w * 1024 + lane * 16) = kf[w];
    }
    fl = seg + 1 < NSEG ? __builtin_amdgcn_raw_buffer_load_b32(r, fo + 128, 0, SC1) : 0u;
  };
  if (wave >= 1 && wave <= 3) q_load(0);
  for (int s = 0; s < NSEG; ++s) {
    const u32 flip = __builtin_amdgcn_readlane(flips, s), base = HO_OFF + s * 65536;
    STAMP(s, 0);
    gather_m(r, base, flip, s == 0, smem, wave, lane, tmo);
    STAMP(s, 1);
    bar();
    STAMP(s, 2);
    {
      f32x4 c0, c1;
      proj2<true, 512>(W[0], W[1], smem + L_M + g * 128 + (lane & 7) * 16, c0, c1);
      u32x4 o = {pk(c0[0], c1[0]), pk(c0[1], c1[1]), pk(c0[2], c1[2]), pk(c0[3], c1[3])};
      *(u32x4*)(smem + L_QM + wave * 1024 + lane * 16) = o;
    }
    if (wave == 0) {
      __builtin_amdgcn_s_setprio(3);
#pragma unroll
      for (int w = 0; w < 8; ++w) kf[w] = *(const u32x4*)(smem + L_QX + (s & 1) * 16384 + 8192 + lane * 16 + w * 1024);
    }
    bar();
    STAMP(s, 3);
    const float C2 = 0.08838834764831845f * 1.4426950408889634f;
    if (wave == 0) {
      if (s < NSEG - 1) {
        const char* qb = smem + L_QM + lane * 16;
        f32x4 s0 = {0.f, 0.f, 0.f, 0.f}, s1 = s0, s2 = s0;
#pragma unroll
        for (int w = 0; w < 4; ++w) {
          const u32x4 qf = *(const u32x4*)(qb + w * 1024);
          s2 = mfma(*(const u32x4*)(qb + 4096 + w * 1024), qf, s2);
          s0 = mfma(kf[w], qf, s0);
          s1 = mfma(kf[4 + w], qf, s1);
        }
        const bool hi = c16 >= 8;
        float v[6];
#pragma unroll
        for (int i = 0; i < 4; ++i) v[i] = hi ? s1[i] : s0[i];
        v[4] = g < 2 ? (hi ? s2[2] : s2[0]) : -INFINITY;
        v[5] = g < 2 ? (hi ? s2[3] : s2[1]) : -INFINITY;
        float m = vmax(vmax3(v[0], v[1], v[2]), vmax3(v[3], v[4], v[5]));
        m = xmax(m);
        m = vmax(m, ror8(m)) * C2;
#pragma unroll
        for (int i = 0; i < 6; ++i) v[i] = __builtin_amdgcn_exp2f(__builtin_fmaf(v[i], C2, -m));
        float sum = ((v[0] + v[1]) + (v[2] + v[3])) + (v[4] + v[5]);
        sum += ror8(sum);
        const float inv = __builtin_amdgcn_rcpf(xsum(sum));
        const u32x2 px = {pk(v[0] * inv, v[1] * inv) & 0x7FFF7FFFu, pk(v[2] * inv, v[3] * inv) & 0x7FFF7FFFu};
        const u32 pm = pk(v[4] * inv, v[5] * inv) & 0x7FFF7FFFu;
        __builtin_amdgcn_raw_buffer_store_b64(px, r, base + homap(lx + (c16 >> 3) * 8, flip), 0, SC1);
        if (g < 2) __builtin_amdgcn_raw_buffer_store_b32(pm, r, base + homap(lm + (c16 >> 3) * 4, flip), 0, SC1);
      }
      __builtin_amdgcn_s_setprio(0);
    } else if (wave < 3) {
      const char* qx = smem + L_QX + (s & 1) * 16384 + lane * 16;
      f32x4 s0 = {0.f, 0.f, 0.f, 0.f}, s1 = s0, s2 = s0;
#pragma unroll
      for (int w = 0; w < 4; ++w) {
        s2 = mfma(*(const u32x4*)(smem + L_QM + 4096 + lane * 16 + w * 1024), kf[w], s2);
        s0 = mfma(*(const u32x4*)(qx + 8192 + w * 1024), kf[w], s0);
        s1 = mfma(*(const u32x4*)(qx + 12288 + w * 1024), kf[w], s1);
      }
      const int qi = (wave - 1) * 16 + c16;
      const float NEG = -1.0e9f * 1.4426950408889634f;
      float v[12];
#pragma unroll
      for (int i = 0; i < 4; ++i) {
        v[i] = (4 * g + i <= qi) ? s0[i] * C2 : NEG;
        v[4 + i] = (16 + 4 * g + i <= qi) ? s1[i] * C2 : NEG;
        v[8 + i] = g < 2 ? s2[i] * C2 : -INFINITY;
      }
      float m = v[0];
#pragma unroll
      for (int i = 1; i < 12; ++i) m = fmaxf(m, v[i]);
      m = xmax(m);
      float sum = 0.f;
#pragma unroll
      for (int i = 0; i < 12; ++i) { v[i] = __builtin_amdgcn_exp2f(v[i] - m); sum += v[i]; }
      const float inv = __builtin_amdgcn_rcpf(xsum(sum));
      u32x4 px; u32x2 pm;
#pragma unroll
      for (int i = 0; i < 4; ++i) px[i] = pk(v[2 * i] * inv, v[2 * i + 1] * inv) & 0x7FFF7FFFu;
#pragma unroll
      for (int i = 0; i < 2; ++i) pm[i] = pk(v[8 + 2 * i] * inv, v[9 + 2 * i] * inv) & 0x7FFF7FFFu;
      const u32 tb = PT_OFF + s * PT_STEP + ((wave - 1) * 2 + (c16 >> 3)) * 20480;
      __builtin_amdgcn_raw_buffer_store_b128(px, r, tb + lx, 0, SC1);
      if (g < 2) __builtin_amdgcn_raw_buffer_store_b64(pm, r, tb + lm, 0, SC1);
    }
    STAMP(s, 4);
    if (wave >= 1 && wave <= 3 && s + 1 < NSEG) q_load(s + 1);
    bar();
  }
  STAMPC(65, 2); STAMP(65, 3);
}

__device__ __forceinline__ void role_b(const Args& a, int j, char* smem) {
  const int tid = threadIdx.x, lane = tid & 63, wave = __builtin_amdgcn_readfirstlane(tid >> 6);
  const int c16 = lane & 15, g = lane >> 4;
  unsigned char* ws = a.ws;
  u32* tmo = (u32*)(ws + CTL_OFF) + 64;
  __amdgpu_buffer_rsrc_t r = __builtin_amdgcn_make_buffer_rsrc(ws, 0, WS_END, 0x00020000);
  STAMP(64, 0);
  u32x4 W[2][KS];
  {
    const unsigned char* wsrc = ws + WVO_OFF + (size_t)j * WVO_J + wave * 2 * KS * 1024 + lane * 16;
#pragma unroll
    for (int tt = 0; tt < 2; ++tt)
#pragma unroll
      for (int t = 0; t < KS; ++t) W[tt][t] = *(const u32x4*)(wsrc + (tt * KS + t) * 1024);
  }
  wait_vm();
  bar();
  const u32 flips = get_flips(r, lane, tmo);
  STAMP(64, 1);
  u32x4 nx0, nx1;
  u32 fl = POISON;
  auto ux_load = [&](int seg) {
    const u32 fo = FLG_OFF + seg * 128 + wave * 16 + (lane & 3) * 4;
    u32 spins = 0;
    while (!allok(fl)) {
      fl = __builtin_amdgcn_raw_buffer_load_b32(r, fo, 0, SC1);
      if (spin_fail(spins, tmo)) break;
    }
    asm volatile("" ::: "memory");
    const u32 uo = UX_OFF + ((seg * NB + j) * 16 + wave * 2) * 1024 + lane * 16;
    nx0 = __builtin_amdgcn_raw_buffer_load_b128(r, uo, 0, SC1);
    nx1 = __builtin_amdgcn_raw_buffer_load_b128(r, uo + 1024, 0, SC1);
    fl = seg + 1 < NSEG ? __builtin_amdgcn_raw_buffer_load_b32(r, fo + 128, 0, SC1) : 0u;
  };
  const u32 lx = wave * 2048 + lane * 16;
  const u32 lm = HO_PM + (wave * 4 + (c16 >> 3)) * 128 + (g & 1) * 64 + (lane & 7) * 8;
  const bool diag = (c16 >> 3) == (g >> 1);
  const u32 redo = ((wave * 2 + (g >> 1)) * 64 + (lane & 7) * 8 + (g & 1) * 4) * 4;
  for (int s = 0; s < NSEG; ++s) {
    const u32 flip = __builtin_amdgcn_readlane(flips, s), base = HO_OFF + s * 65536;
    STAMP(s, 0);
    gather_m(r, base, flip, s == 0, smem, wave, lane, tmo);
    bar();
    STAMP(s, 1);
    u32x4 um;
    {
      f32x4 c0, c1;
      proj2<false, 512>(W[0], W[1], smem + L_M + g * 128 + (lane & 7) * 16, c0, c1);
      um = u32x4{pk(c0[0], c0[1]), pk(c0[2], c0[3]), pk(c1[0], c1[1]), pk(c1[2], c1[3])};
      if (g < 2) __builtin_amdgcn_raw_buffer_store_b128(um, r, UM_OFF + ((s * NB + j) * 8 + wave) * 512 + lane * 16, 0, SC1);
      else um = u32x4{0, 0, 0, 0};
    }
    if (s + 1 < NSEG) {
      if (s == 0) ux_load(0);
      STAMP(s, 2);
      const u32 o0 = base + homap(lx, flip), o1 = o0 + 1024, o2 = base + homap(lm, flip), o3 = o2 + 256;
      u32x4 p0, p1; u32x2 q0, q1;
      u32 spins = 0;
      for (;;) {
        p0 = __builtin_amdgcn_raw_buffer_load_b128(r, o0, 0, SC1);
        p1 = __builtin_amdgcn_raw_buffer_load_b128(r, o1, 0, SC1);
        q0 = __builtin_amdgcn_raw_buffer_load_b64(r, o2, 0, SC1);
        q1 = __builtin_amdgcn_raw_buffer_load_b64(r, o3, 0, SC1);
        if (allok(mx(mx(mx4(p0), mx4(p1)), mx(mx2(q0), mx2(q1))))) break;
        if (spin_fail(spins, tmo)) break;
      }
      STAMP(s, 3);
      f32x4 d = {0.f, 0.f, 0.f, 0.f}, e = d;
      const u32x4 pmv = {q0.x, q0.y, q1.x, q1.y};
      d = mfma(nx0, p0, d); e = mfma(nx1, p1, e); d = mfma(um, pmv, d);
      d += e;
      if (diag) *(f32x4*)(smem + L_QM + redo) = d;
    }
    wait_vm();
    bar();
    if (wave == 0 && s + 1 < NSEG) {
      const char* rp = smem + L_QM + (lane >> 5) * 2048 + (lane & 31) * 8;
      f32x2 acc = *(const f32x2*)rp;
#pragma unroll
      for (int p = 1; p < 8; ++p) acc += *(const f32x2*)(rp + p * 256);
      auto t0 = __builtin_amdgcn_permlane32_swap(__float_as_uint(acc.x), __float_as_uint(acc.x), false, false);
      auto t1 = __builtin_amdgcn_permlane32_swap(__float_as_uint(acc.y), __float_as_uint(acc.y), false, false);
      const u32 o = pks(__uint_as_float(t0[0]) + __uint_as_float(t0[1]), __uint_as_float(t1[0]) + __uint_as_float(t1[1]));
      const u32 nflip = __builtin_amdgcn_readlane(flips, s + 1);
      if (lane < 32) __builtin_amdgcn_raw_buffer_store_b32(o, r, base + 65536 + homap(HO_M + j * 128 + lane * 4, nflip), 0, SC1);
    }
    STAMP(s, 4);
    if (tid == 64) __builtin_amdgcn_raw_buffer_store_b32(1u, r, FLM_OFF + s * 3072 + (j / 6) * 128 + (j % 6) * 4, 0, SC1);
    if (s + 2 < NSEG) ux_load(s + 1);
    STAMP(s, 5);
  }
  STAMP(65, 0);
}

__device__ __forceinline__ void role_o(const Args& a, int o24, char* smem) {
  const int tid = threadIdx.x, lane = tid & 63, wave = __builtin_amdgcn_readfirstlane(tid >> 6);
  const int c16 = lane & 15, g = lane >> 4, o = o24 >> 1;
  unsigned char* ws = a.ws;
  u32* tmo = (u32*)(ws + CTL_OFF) + 64;
  __amdgpu_buffer_rsrc_t r = __builtin_amdgcn_make_buffer_rsrc(ws, 0, WS_END, 0x00020000);
  STAMP(64, 0);
  if (wave < 3 && o24 * 3 + wave < NSEG) {
    const int blk = o24 * 3 + wave;
    u32 best[2] = {~0u, ~0u};
    for (int rep = 0; rep < 4; ++rep)
      for (int f = 0; f < 2; ++f) {
        u32* p = (u32*)(ws + HO_OFF + blk * 65536 + homap(32768 - 128 * (rep + 1), f * 8192));
        __hip_atomic_store(p, POISON, __ATOMIC_RELAXED, __HIP_MEMORY_SCOPE_AGENT);
        wait_vm();
        u64 t0, t1;
        asm volatile("s_memtime %0\n\ts_waitcnt lgkmcnt(0)" : "=s"(t0) :: "memory");
        u32 v = __hip_atomic_load(p, __ATOMIC_RELAXED, __HIP_MEMORY_SCOPE_AGENT);
        asm volatile("s_waitcnt vmcnt(0)" : "+v"(v) :: "memory");
        asm volatile("s_memtime %0\n\ts_waitcnt lgkmcnt(0)" : "=s"(t1) :: "memory");
        const u32 d = (u32)(t1 - t0);
        best[f] = d < best[f] ? d : best[f];
      }
    if (lane == 0) __builtin_amdgcn_raw_buffer_store_b32(best[1] < best[0] ? 8192u : 0u, r, FLP_OFF + blk * 4, 0, SC1);
  }
  STAMP(64, 1);
  const u32 lx = wave * 2048 + lane * 16;
  const u32 lm = HO_PM + (wave * 4 + (c16 >> 3)) * 128 + (g & 1) * 64 + (lane & 7) * 8;
  const bool diag = (c16 >> 3) == (g >> 1);
  const u32 redo = ((wave * 2 + (g >> 1)) * 64 + (lane & 7) * 8 + (g & 1) * 4) * 4;
  for (int s = o24 & 1; s < NSEG; s += 2) {
    u32 spins = 0;
    while (!allok(__builtin_amdgcn_raw_buffer_load_b32(r, FLG_OFF + s * 128 + wave * 16 + (lane & 3) * 4, 0, SC1))) {
      if (spin_fail(spins, tmo)) break;
      __builtin_amdgcn_s_sleep(16);
    }
    asm volatile("" ::: "memory");
    u32x4 x0[6], x1[6], um[6];
#pragma unroll
    for (int c = 0; c < 6; ++c) {
      const u32 uo = UX_OFF + ((s * NB + o * 6 + c) * 16 + wave * 2) * 1024 + lane * 16;
      x0[c] = __builtin_amdgcn_raw_buffer_load_b128(r, uo, 0, SC1);
      x1[c] = __builtin_amdgcn_raw_buffer_load_b128(r, uo + 1024, 0, SC1);
    }
    if (wave == 0) {
      const u32 fo = (lane < 32 || s < SPLIT) ? FLM_OFF + s * 3072 + o * 128 + (lane % 6) * 4 : FLF_OFF + ((s >> 2) * 6 + (o >> 1)) * 128;
      while (!allok(__builtin_amdgcn_raw_buffer_load_b32(r, fo, 0, SC1))) {
        if (spin_fail(spins, tmo)) break;
        __builtin_amdgcn_s_sleep(4);
      }
    }
    wait_vm();
    bar();
    if (tid == 0 && s >= 2 && s - 2 < SPLIT) __builtin_amdgcn_raw_buffer_store_b32(1u, r, FLA_OFF + (s - 2) * 128 + o * 4, 0, SC1);
    const u32 eo = ((s * 32 + ((tid & 63) >> 1)) * DM + (o * 6 + (tid >> 6)) * 8 + (tid & 1) * 4) * 2;
    u32x2 xv = {0, 0}, fv = {0, 0};
    if (s >= SPLIT && tid < 384) { xv = *(const u32x2*)((const char*)a.x + eo); fv = __builtin_amdgcn_raw_buffer_load_b64(r, FO_OFF + eo, 0, SC1); }
#pragma unroll
    for (int c = 0; c < 6; ++c)
      um[c] = __builtin_amdgcn_raw_buffer_load_b128(r, UM_OFF + ((s * NB + o * 6 + c) * 8 + wave) * 512 + (lane & 31) * 16, 0, SC1);
    u32x4 p0[4], p1[4]; u32x2 q0[4], q1[4];
    for (;;) {
      u32 m = 0;
#pragma unroll
      for (int u = 0; u < 4; ++u) {
        const u32 tb = PT_OFF + s * PT_STEP + u * 20480;
        p0[u] = __builtin_amdgcn_raw_buffer_load_b128(r, tb + lx, 0, SC1);
        p1[u] = __builtin_amdgcn_raw_buffer_load_b128(r, tb + lx + 1024, 0, SC1);
        q0[u] = __builtin_amdgcn_raw_buffer_load_b64(r, tb + lm, 0, SC1);
        q1[u] = __builtin_amdgcn_raw_buffer_load_b64(r, tb + lm + 256, 0, SC1);
      }
#pragma unroll
      for (int u = 0; u < 4; ++u) m = mx(m, mx(mx(mx4(p0[u]), mx4(p1[u])), mx(mx2(q0[u]), mx2(q1[u]))));
      if (allok(m)) break;
      if (spin_fail(spins, tmo)) break;
    }
#pragma unroll
    for (int c = 0; c < 6; ++c) {
      if (g >= 2) um[c] = u32x4{0, 0, 0, 0};
#pragma unroll
      for (int u = 0; u < 4; ++u) {
        f32x4 d = {0.f, 0.f, 0.f, 0.f};
        const u32x4 pmv = {q0[u].x, q0[u].y, q1[u].x, q1[u].y};
        d = mfma(x0[c], p0[u], d); d = mfma(x1[c], p1[u], d); d = mfma(um[c], pmv, d);
        if (diag) *(f32x4*)(smem + (c * 4 + u) * 4096 + redo) = d;
      }
    }
    bar();
    if (tid < 384) {
      const char* rp = smem + (tid >> 4) * 4096 + (tid & 15) * 16;
      f32x4 acc = *(const f32x4*)rp;
#pragma unroll
      for (int p = 1; p < 16; ++p) acc += *(const f32x4*)(rp + p * 256);
      const u32x2 av = {pk(acc[0], acc[1]), pk(acc[2], acc[3])};
      if (s < SPLIT) __builtin_amdgcn_raw_buffer_store_b64(av, r, AO_OFF + eo, 0, SC1);
      else *(u32x2*)((char*)a.out + eo) = u32x2{fin(xv.x, av.x, fv.x), fin(xv.y, av.y, fv.y)};
    }
    STAMP(s, 0);
  }
}

__device__ __forceinline__ void role_v(const Args& a, int h, char* smem) {
  const int tid = threadIdx.x, lane = tid & 63, wave = __builtin_amdgcn_readfirstlane(tid >> 6);
  const int c16 = lane & 15, g = lane >> 4;
  unsigned char* ws = a.ws;
  __amdgpu_buffer_rsrc_t r = __builtin_amdgcn_make_buffer_rsrc(ws, 0, WS_END, 0x00020000);
  STAMP(64, 0);
  u32x4 WV[KS], WO[5][4];
  {
    const u32 sel = (c16 & 1) ? 0x07060302u : 0x05040100u;
    u32x4 st[18];
#pragma unroll
    for (int i = 0; i < 18; ++i) { const int pc = i * 512 + tid; st[i] = *(const u32x4*)(a.wv + (size_t)(pc >> 4) * (NH * DH) + h * DH + (pc & 15) * 8); }
#pragma unroll
    for (int i = 0; i < 18; ++i) { const int pc = i * 512 + tid; *(u32x4*)(smem + (pc >> 4) * 272 + (pc & 15) * 16) = st[i]; }
#pragma unroll
    for (int i = 0; i < 18; ++i) st[i] = *(const u32x4*)(a.wo + (size_t)h * DH * DM + (i * 512 + tid) * 8);
    bar();
    {
      const char* wl = smem + g * 8 * 272 + wave * 32 + (c16 >> 1) * 4;
#pragma unroll
      for (int t = 0; t < KS; ++t) {
#pragma unroll
        for (int p = 0; p < 4; ++p)
          WV[t][p] = __builtin_amdgcn_perm(*(const u32*)(wl + (t * 32 + 2 * p + 1) * 272), *(const u32*)(wl + (t * 32 + 2 * p) * 272), sel);
        asm volatile("" : "+v"(WV[t]) :: "memory");
      }
    }
    bar();
#pragma unroll
    for (int i = 0; i < 18; ++i) { const int pc = i * 512 + tid; *(u32x4*)(smem + (pc / 72) * 1168 + (pc % 72) * 16) = st[i]; }
    bar();
#pragma unroll
    for (int i = 0; i < 5; ++i) {
      const int ct = wave + 8 * i < 36 ? wave + 8 * i : 0;
      const char* wl = smem + g * 4 * 1168 + ct * 32 + (c16 >> 1) * 4;
#pragma unroll
      for (int es = 0; es < 4; ++es) {
#pragma unroll
        for (int p = 0; p < 4; ++p) {
          const int e0 = 32 * es + 16 * (p >> 1) + 2 * (p & 1);
          WO[i][es][p] = __builtin_amdgcn_perm(*(const u32*)(wl + (e0 + 1) * 1168), *(const u32*)(wl + e0 * 1168), sel);
        }
        asm volatile("" : "+v"(WO[i][es]) :: "memory");
      }
    }
    bar();
  }
  STAMP(64, 1);
  stage_x(ws, 0, wave, lane);
  for (int sg = 0; sg < NSEG; ++sg) {
    wait_vm();
    bar();
    if (sg > 0 && tid == 0) __builtin_amdgcn_raw_buffer_store_b32(1u, r, FLG_OFF + (sg - 1) * 128 + h * 4, 0, SC1);
    if (sg + 1 < NSEG) stage_x(ws, sg + 1, wave, lane);
    {
      const char* xb = smem + L_X + (sg & 1) * XF_SEG + lane * 16;
      f32x4 c0 = {0.f, 0.f, 0.f, 0.f}, c1 = c0;
#pragma unroll
      for (int t = 0; t < KS; ++t) {
        c0 = mfma(WV[t], *(const u32x4*)(xb + t * 1024), c0);
        c1 = mfma(WV[t], *(const u32x4*)(xb + (KS + t) * 1024), c1);
      }
      __builtin_amdgcn_sched_group_barrier(0x100, 8, 0);
#pragma unroll
      for (int t = 0; t < KS - 4; ++t) { __builtin_amdgcn_sched_group_barrier(0x008, 2, 0); __builtin_amdgcn_sched_group_barrier(0x100, 2, 0); }
      __builtin_amdgcn_sched_group_barrier(0x008, 8, 0);
      char* vt = smem + L_QM + (wave >> 1) * 1024 + lane * 16 + (wave & 1) * 8;
      *(u32x2*)vt = u32x2{pk(c0[0], c0[1]), pk(c0[2], c0[3])};
      *(u32x2*)(vt + 4096) = u32x2{pk(c1[0], c1[1]), pk(c1[2], c1[3])};
    }
    bar();
    u32x4 vf[2][4];
#pragma unroll
    for (int rt = 0; rt < 2; ++rt)
#pragma unroll
      for (int es = 0; es < 4; ++es) vf[rt][es] = *(const u32x4*)(smem + L_QM + rt * 4096 + es * 1024 + lane * 16);
#pragma unroll
    for (int i = 0; i < 5; ++i) {
      const int ct = wave + 8 * i;
      if (ct < 36) {
        f32x4 d0 = {0.f, 0.f, 0.f, 0.f}, d1 = d0;
#pragma unroll
        for (int es = 0; es < 4; ++es) { d0 = mfma(vf[0][es], WO[i][es], d0); d1 = mfma(vf[1][es], WO[i][es], d1); }
        const u32x4 o = {pk(d0[0], d0[1]), pk(d0[2], d0[3]), pk(d1[0], d1[1]), pk(d1[2], d1[3])};
        __builtin_amdgcn_raw_buffer_store_b128(o, r, UX_OFF + ((sg * NB + ct * 2 + (c16 >> 3)) * 16 + (h >> 1)) * 1024 + (g * 16 + (h & 1) * 8 + (lane & 7)) * 16, 0, SC1);
      }
    }
    if (sg == 0) STAMP(64, 2);
  }
  wait_vm();
  bar();
  if (tid == 0) __builtin_amdgcn_raw_buffer_store_b32(1u, r, FLG_OFF + (NSEG - 1) * 128 + h * 4, 0, SC1);
  STAMP(64, 3);
}

__device__ __forceinline__ float gelu_tanh(float x) {
  const float u = 0.7978845608028654f * (x + 0.044715f * x * x * x);
  const float e = __expf(2.f * u);
  return 0.5f * x * (2.f - 2.f / (e + 1.f));
}

__device__ __forceinline__ void role_f(const Args& a, int f, char* smem) {
  const int tid = threadIdx.x, lane = tid & 63, wave = __builtin_amdgcn_readfirstlane(tid >> 6);
  const int c16 = lane & 15, g = lane >> 4;
  unsigned char* ws = a.ws;
  u32* tmo = (u32*)(ws + CTL_OFF) + 64;
  u32* cnt = (u32*)(ws + CTL_OFF) + 128;
  __amdgpu_buffer_rsrc_t r = __builtin_amdgcn_make_buffer_rsrc(ws, 0, WS_END, 0x00020000);
  STAMP(64, 0);
  auto stage = [&](int sg) {
#pragma unroll
    for (int i = 0; i < 5; ++i) {
      const int p = min(i * 8 + wave, 35);
      glds16(ws + XF_OFF + sg * XF_SEG + p * 1024 + lane * 16, (sg & 3) * XF_SEG + p * 1024);
    }
  };
  if (f >= 64) {
    const int h = f - 64;
    u32x4 WW[2][KS];
    load_wqk(a, h, WW, smem);
    wait_vm();
    stage(0); stage(1);
    for (int sg = 0; sg < NSEG; ++sg) {
      if (sg < 4) wait_vm();
      else asm volatile("s_waitcnt vmcnt(5)" ::: "memory");
      bar();
      if (sg >= 1 && tid == 0) __builtin_amdgcn_raw_buffer_store_b32(1u, r, FLQ_OFF + (sg < 4 ? sg - 1 : sg - 2) * 128 + h * 4, 0, SC1);
      stage(min(sg + 2, NSEG - 1));
#pragma unroll
      for (int rt = 0; rt < 2; ++rt) {
        f32x4 c0, c1;
        proj2<true, 1024>(WW[0], WW[1], smem + (sg & 3) * XF_SEG + rt * KS * 1024 + lane * 16, c0, c1);
        const u32x4 o = {pk(c0[0], c1[0]), pk(c0[1], c1[1]), pk(c0[2], c1[2]), pk(c0[3], c1[3])};
        __builtin_amdgcn_raw_buffer_store_b128(o, r, QX_OFF + ((sg * NH + h) * 16 + (wave >> 2) * 8 + rt * 4 + (wave & 3)) * 1024 + lane * 16, 0, SC1);
      }
    }
    wait_vm();
    bar();
    if (tid < 2) __builtin_amdgcn_raw_buffer_store_b32(1u, r, FLQ_OFF + (NSEG - 2 + tid) * 128 + h * 4, 0, SC1);
  }
  for (int it = f * 27 + wave; it < f * 27 + 27; it += 8) {
    const int ks = it / 18, bb = it % 18;
    u32 d[8];
#pragma unroll
    for (int jj = 0; jj < 8; ++jj)
      d[jj] = *(const u32*)(a.wd + (size_t)(ks * 32 + (jj >> 2) * 16 + g * 4 + (jj & 3)) * DM + bb * 32 + c16 * 2);
    const u32x4 lo = {plo(d[0], d[1]), plo(d[2], d[3]), plo(d[4], d[5]), plo(d[6], d[7])};
    const u32x4 hi = {phi(d[0], d[1]), phi(d[2], d[3]), phi(d[4], d[5]), phi(d[6], d[7])};
    __builtin_amdgcn_raw_buffer_store_b128(lo, r, WDF_OFF + (bb * 144 + ks) * 2048 + lane * 16, 0, SC1);
    __builtin_amdgcn_raw_buffer_store_b128(hi, r, WDF_OFF + (bb * 144 + ks) * 2048 + 1024 + lane * 16, 0, SC1);
  }
#pragma unroll 1
  for (int job = 0; job < 3 && f < 64; ++job) {
    int tid = threadIdx.x;
    asm volatile("" : "+v"(tid));
    const int hv = wave & 3, rt = wave >> 2;
    const int u0 = f * 72, sl = (u0 >> 6) + job;
    const int q0 = job == 0 ? (u0 & 63) : 0, q1 = min(u0 + 72 - sl * 64, 64);
    if (q1 <= q0) break;
    u32x4 WW[2][KS];
#pragma unroll
    for (int mat = 0; mat < 2; ++mat) {
      __syncthreads();
      u32x4 st[9];
#pragma unroll
      for (int i = 0; i < 9; ++i) {
        const int pc = i * 512 + tid;
        st[i] = *(const u32x4*)((mat ? a.wu : a.wg) + (size_t)(pc >> 3) * FF + sl * 64 + (pc & 7) * 8);
      }
#pragma unroll
      for (int i = 0; i < 9; ++i) { const int pc = i * 512 + tid; *(u32x4*)(smem + (pc >> 3) * 144 + (pc & 7) * 16) = st[i]; }
      __syncthreads();
      const char* wl = smem + g * 8 * 144 + hv * 32 + (c16 >> 1) * 4;
      const u32 sel = (c16 & 1) ? 0x07060302u : 0x05040100u;
#pragma unroll
      for (int t = 0; t < KS; ++t) {
#pragma unroll
        for (int p = 0; p < 4; ++p) {
          const int o = (t * 32 + 2 * p) * 144;
          WW[mat][t][p] = __builtin_amdgcn_perm(*(const u32*)(wl + o + 144), *(const u32*)(wl + o), sel);
        }
        asm volatile("" : "+v"(WW[mat][t]) :: "memory");
      }
    }
    __syncthreads();
    if (job == 0) STAMP(64, 1);
    stage(q0); stage(min(q0 + 1, q1 - 1));
    for (int sg = q0; sg < q1; ++sg) {
      asm volatile("s_waitcnt vmcnt(5)" ::: "memory");
      bar();
      stage(min(sg + 2, q1 - 1));
      f32x4 cg, cu;
      proj2<true, 1024>(WW[0], WW[1], smem + (sg & 3) * XF_SEG + rt * KS * 1024 + lane * 16, cg, cu);
      float hh[4];
#pragma unroll
      for (int i = 0; i < 4; ++i) hh[i] = bfr(gelu_tanh(bfr(cg[i]))) * bfr(cu[i]);
      const u32x2 o = {pk(hh[0], hh[1]), pk(hh[2], hh[3])};
      __builtin_amdgcn_raw_buffer_store_b64(o, r, H_OFF + ((sg * 2 + rt) * 144 + sl * 2 + (hv >> 1)) * 1024 + lane * 16 + (hv & 1) * 8, 0, SC1);
    }
    wait_vm();
  }
  wait_vm();
  __syncthreads();
  STAMP(64, 2);
  if (tid == 0) {
    __hip_atomic_fetch_add(cnt, 1u, __ATOMIC_RELAXED, __HIP_MEMORY_SCOPE_AGENT);
    u32 spins = 0;
    while (__hip_atomic_load(cnt, __ATOMIC_RELAXED, __HIP_MEMORY_SCOPE_AGENT) < (u32)NF) {
      __builtin_amdgcn_s_sleep(8);
      if (spin_fail(spins, tmo)) break;
    }
    __builtin_amdgcn_fence(__ATOMIC_ACQUIRE, "agent");
    wait_vm();
  }
  __syncthreads();
  STAMP(64, 3);
  {
    const int tm = f / 6, b3 = (f % 6) * 3, tg = wave & 3, kh = wave >> 2;
    const unsigned char* hb = ws + H_OFF + ((size_t)(tm * 8 + tg * 2) * 144 + kh * 72) * 1024 + lane * 16;
    const unsigned char* wb = ws + WDF_OFF + (size_t)(b3 * 144 + kh * 72) * 2048 + lane * 16;
    f32x4 acc[2][6];
#pragma unroll
    for (int u = 0; u < 2; ++u)
#pragma unroll
      for (int i = 0; i < 6; ++i) acc[u][i] = f32x4{0.f, 0.f, 0.f, 0.f};
    u32x4 hf[2][2][2], wf[2][2][6];
    auto ld = [&](int st, int t0) {
#pragma unroll
      for (int t = 0; t < 2; ++t) {
        hf[st][t][0] = *(const u32x4*)(hb + (t0 + t) * 1024); hf[st][t][1] = *(const u32x4*)(hb + (144 + t0 + t) * 1024);
#pragma unroll
        for (int i = 0; i < 6; ++i) wf[st][t][i] = *(const u32x4*)(wb + ((i >> 1) * 144 + t0 + t) * 2048 + (i & 1) * 1024);
      }
    };
    auto mm = [&](int st) {
#pragma unroll
      for (int t = 0; t < 2; ++t)
#pragma unroll
        for (int i = 0; i < 6; ++i) { acc[0][i] = mfma(wf[st][t][i], hf[st][t][0], acc[0][i]); acc[1][i] = mfma(wf[st][t][i], hf[st][t][1], acc[1][i]); }
    };
    ld(0, 0);
#pragma unroll 1
    for (int t0 = 0; t0 < 72; t0 += 4) {
      ld(1, t0 + 2);
      __builtin_amdgcn_sched_barrier(0);
      mm(0);
      __builtin_amdgcn_sched_barrier(0);
      ld(0, min(t0 + 4, 70));
      __builtin_amdgcn_sched_barrier(0);
      mm(1);
      __builtin_amdgcn_sched_barrier(0);
    }
    STAMP(64, 4);
    if (kh == 1) {
#pragma unroll
      for (int u = 0; u < 2; ++u)
#pragma unroll
        for (int i = 0; i < 6; ++i) *(f32x4*)(smem + ((tg * 12 + u * 6 + i) * 64 + lane) * 16) = acc[u][i];
    }
    const bool mine = tm * 4 < SPLIT;
    if (mine && wave == 0) {
      u32 spins = 0;
      while (!allok(__builtin_amdgcn_raw_buffer_load_b32(r, FLA_OFF + (tm * 4 + ((lane >> 1) & 3)) * 128 + ((f % 6) * 2 + (lane & 1)) * 4, 0, SC1))) {
        if (spin_fail(spins, tmo)) break;
        __builtin_amdgcn_s_sleep(16);
      }
    }
    __syncthreads();
    if (kh == 0) {
#pragma unroll
      for (int u = 0; u < 2; ++u)
#pragma unroll
        for (int b = 0; b < 3; ++b) {
          const f32x4 e0 = acc[u][2 * b] + *(const f32x4*)(smem + ((tg * 12 + u * 6 + 2 * b) * 64 + lane) * 16);
          const f32x4 e1 = acc[u][2 * b + 1] + *(const f32x4*)(smem + ((tg * 12 + u * 6 + 2 * b + 1) * 64 + lane) * 16);
          const u32x4 o = {pk(e0[0], e1[0]), pk(e0[1], e1[1]), pk(e0[2], e1[2]), pk(e0[3], e1[3])};
          const u32 eo = ((tm * 128 + tg * 32 + u * 16 + c16) * DM + (b3 + b) * 32 + g * 8) * 2;
          if (mine) {
            const u32x4 xv = *(const u32x4*)((const char*)a.x + eo), av = __builtin_amdgcn_raw_buffer_load_b128(r, AO_OFF + eo, 0, SC1);
            *(u32x4*)((char*)a.out + eo) = u32x4{fin(xv.x, av.x, o.x), fin(xv.y, av.y, o.y), fin(xv.z, av.z, o.z), fin(xv.w, av.w, o.w)};
          } else {
            __builtin_amdgcn_raw_buffer_store_b128(o, r, FO_OFF + eo, 0, SC1);
          }
        }
    }
    wait_vm();
    __syncthreads();
    if (!mine && tid == 0) __builtin_amdgcn_raw_buffer_store_b32(1u, r, FLF_OFF + f * 128, 0, SC1);
  }
}

__global__ void __launch_bounds__(512, 2) k2_main(Args a) {
  extern __shared__ __attribute__((aligned(16))) char smem[];
  if (threadIdx.x == 0) {
    u32* ctl = (u32*)(a.ws + CTL_OFF);
    const u32 x = __builtin_amdgcn_s_getreg((4 - 1) << 11 | 0 << 6 | 20) & 7u;
    const u32 rank = __hip_atomic_fetch_add(ctl + 256 + x * 32, 1u, __ATOMIC_RELAXED, __HIP_MEMORY_SCOPE_AGENT);
    u32 slot = rank * 8 + x;
    if (rank >= 32) {
      u32 k = __hip_atomic_fetch_add(ctl + 192, 1u, __ATOMIC_RELAXED, __HIP_MEMORY_SCOPE_AGENT), spins = 0;
      for (;;) {
        u32 tot = 0;
        for (int i = 0; i < 8; ++i) tot += __hip_atomic_load(ctl + 256 + i * 32, __ATOMIC_RELAXED, __HIP_MEMORY_SCOPE_AGENT);
        if (tot >= 256u || spin_fail(spins, ctl + 64)) break;
      }
      slot = 256;
      for (int i = 0; i < 8 && slot == 256; ++i) {
        const u32 c = __hip_atomic_load(ctl + 256 + i * 32, __ATOMIC_RELAXED, __HIP_MEMORY_SCOPE_AGENT), fr = c < 32u ? 32u - c : 0u;
        if (k < fr) slot = (c + k) * 8 + i; else k -= fr;
      }
    }
    *(u32*)smem = slot;
  }
  __syncthreads();
  const int b = __builtin_amdgcn_readfirstlane(*(const u32*)smem), rr = b & 7, q = b >> 3;
  __syncthreads();
  if (b >= 256) return;
  if (rr == 0) role_a(a, q, smem);
  else if (rr < 4 && q < 24) role_b(a, (rr - 1) * 24 + q, smem);
  else if (rr == 4) role_v(a, q, smem);
  else if (rr > 4) role_f(a, (rr - 5) * 32 + q, smem);
  else role_o(a, (rr - 1) * 8 + (q - 24), smem);
  DUMP();
}

extern "C" void kernel_launch(void* const* d_in, const int* in_sizes, int n_in, void* d_out, int out_size, void* d_ws,
                              size_t ws_size, hipStream_t stream) {
  Args a;
  a.x = (const u16*)d_in[0]; a.mem0 = (const u16*)d_in[1]; a.wq = (const u16*)d_in[2]; a.wk = (const u16*)d_in[3];
  a.wv = (const u16*)d_in[4]; a.wo = (const u16*)d_in[5]; a.wg = (const u16*)d_in[8]; a.wu = (const u16*)d_in[9];
  a.wd = (const u16*)d_in[10];
  a.ws = (unsigned char*)(((uintptr_t)d_ws + 65535) & ~(uintptr_t)65535);
  a.out = (u16*)d_out;
  (void)hipFuncSetAttribute((const void*)k2_main, hipFuncAttributeMaxDynamicSharedMemorySize, LDS_BYTES);
  k1_prep<<<K1_GRID, 256, 0, stream>>>(a);
  k2_main<<<256, 512, LDS_BYTES, stream>>>(a);
}
```

```cpp
#include <hip/hip_runtime.h>
#include <hip/hip_bf16.h>

typedef unsigned u32;
typedef unsigned long long u64;
typedef unsigned short u16;
typedef __attribute__((ext_vector_type(8))) __bf16 bf16x8;
typedef __attribute__((ext_vector_type(2))) __bf16 bf16x2;
typedef __attribute__((ext_vector_type(4))) float f32x4;
typedef __attribute__((ext_vector_type(2))) float f32x2;
typedef __attribute__((ext_vector_type(4))) unsigned u32x4;
typedef __attribute__((ext_vector_type(2))) unsigned u32x2;

constexpr int S_ = 2048, DM = 576, NH = 32, DH = 128, FF = 4608, NSEG = 64, KS = 18;
constexpr int NB = 72, NF = 96;
constexpr u32 HO_OFF = 0;
constexpr u32 PT_OFF = HO_OFF + NSEG * 65536;
constexpr u32 PT_STEP = 4 * 20480;
constexpr u32 FLG_OFF = PT_OFF + NSEG * PT_STEP;
constexpr u32 FLM_OFF = FLG_OFF + NSEG * 128;
constexpr u32 FLQ_OFF = FLM_OFF + NSEG * 3072;
constexpr u32 FLA_OFF = FLQ_OFF + NSEG * 128;
constexpr u32 FLF_OFF = FLA_OFF + NSEG * 128;
constexpr int SPLIT = 56;
constexpr u32 FLP_OFF = FLF_OFF + NF * 128;
constexpr u32 POISON_BYTES = FLP_OFF + 256;
constexpr u32 CTL_OFF = (POISON_BYTES + 4095) & ~4095u;
constexpr u32 M0_OFF = CTL_OFF + 4096;
constexpr u32 XF_OFF = M0_OFF + 12288;
constexpr u32 XF_SEG = 2 * KS * 1024;
constexpr u32 WVO_OFF = XF_OFF + NSEG * XF_SEG;
constexpr u32 WVO_J = 16 * KS * 1024;
constexpr u32 UX_OFF = WVO_OFF + NB * WVO_J;
constexpr u32 UM_OFF = UX_OFF + NSEG * NB * 16384;
constexpr u32 QX_OFF = UM_OFF + NSEG * NB * 4096;
constexpr u32 H_OFF = QX_OFF + NSEG * NH * 16384;
constexpr u32 WDF_OFF = H_OFF + 128 * 144 * 1024;
constexpr u32 FO_OFF = WDF_OFF + 9 * 144 * 4096;
constexpr u32 AO_OFF = FO_OFF + S_ * DM * 2;
constexpr u32 WS_END = AO_OFF + S_ * DM * 2;
constexpr u32 POISON = 0xFFFFFFFFu;
constexpr int SC1 = 16;
constexpr u32 SPIN_CAP = 400000u;
constexpr int LDS_BYTES = 160 * 1024;

__device__ __forceinline__ u32 pk(float lo, float hi) {
  f32x2 v = {lo, hi};
  return __builtin_bit_cast(u32, __builtin_convertvector(v, bf16x2));
}
__device__ __forceinline__ u32 pks(float lo, float hi) { u32 r = pk(lo, hi); return r == POISON ? 0x7FC07FC0u : r; }
__device__ __forceinline__ float bfr(float x) { return (float)(__bf16)x; }
__device__ __forceinline__ float bflo(u32 w) { return __uint_as_float(w << 16); }
__device__ __forceinline__ float bfhi(u32 w) { return __uint_as_float(w & 0xFFFF0000u); }
__device__ __forceinline__ u32 fin(u32 x, u32 p, u32 f) {
  return pk(bfr(bflo(x) + bflo(p)) + bflo(f), bfr(bfhi(x) + bfhi(p)) + bfhi(f));
}
__device__ __forceinline__ f32x4 mfma(u32x4 a, u32x4 b, f32x4 c) {
  return __builtin_amdgcn_mfma_f32_16x16x32_bf16(__builtin_bit_cast(bf16x8, a), __builtin_bit_cast(bf16x8, b), c, 0, 0, 0);
}
__device__ __forceinline__ u32 plo(u32 a, u32 b) { return __builtin_amdgcn_perm(b, a, 0x05040100u); }
__device__ __forceinline__ u32 phi(u32 a, u32 b) { return __builtin_amdgcn_perm(b, a, 0x07060302u); }
__device__ __forceinline__ float vmax(float a, float b) { float r; asm("v_max_f32 %0, %1, %2" : "=v"(r) : "v"(a), "v"(b)); return r; }
__device__ __forceinline__ float vmax3(float a, float b, float c) { float r; asm("v_max3_f32 %0, %1, %2, %3" : "=v"(r) : "v"(a), "v"(b), "v"(c)); return r; }
__device__ __forceinline__ float xmax(float x) {
  auto s = __builtin_amdgcn_permlane16_swap(__float_as_uint(x), __float_as_uint(x), false, false);
  x = vmax(__uint_as_float(s[0]), __uint_as_float(s[1]));
  auto t = __builtin_amdgcn_permlane32_swap(__float_as_uint(x), __float_as_uint(x), false, false);
  return vmax(__uint_as_float(t[0]), __uint_as_float(t[1]));
}
__device__ __forceinline__ float xsum(float x) {
  auto s = __builtin_amdgcn_permlane16_swap(__float_as_uint(x), __float_as_uint(x), false, false);
  x = __uint_as_float(s[0]) + __uint_as_float(s[1]);
  auto t = __builtin_amdgcn_permlane32_swap(__float_as_uint(x), __float_as_uint(x), false, false);
  return __uint_as_float(t[0]) + __uint_as_float(t[1]);
}
__device__ __forceinline__ float ror8(float x) {
  return __builtin_bit_cast(float, __builtin_amdgcn_mov_dpp(__builtin_bit_cast(int, x), 0x128, 0xf, 0xf, true));
}
__device__ __forceinline__ u32 mx(u32 a, u32 b) { return a > b ? a : b; }
__device__ __forceinline__ u32 mx4(u32x4 v) { return mx(mx(v.x, v.y), mx(v.z, v.w)); }
__device__ __forceinline__ u32 mx2(u32x2 v) { return mx(v.x, v.y); }
__device__ __forceinline__ bool allok(u32 m) { return __all(m != POISON); }
__device__ __forceinline__ void glds16(const void* g, u32 lds) {
  u32 keep;
  asm volatile("s_mov_b32 %0, m0\n\ts_mov_b32 m0, %2\n\ts_nop 0\n\tglobal_load_lds_dwordx4 %1, off\n\ts_mov_b32 m0, %0"
               : "=&s"(keep) : "v"(g), "s"(lds) : "memory");
}
__device__ __forceinline__ void wait_vm() { asm volatile("s_waitcnt vmcnt(0)" ::: "memory"); }
__device__ __forceinline__ void bar() {
  asm volatile("s_waitcnt lgkmcnt(0)" ::: "memory");
  __builtin_amdgcn_s_barrier();
  asm volatile("" ::: "memory");
}
__device__ __forceinline__ bool spin_fail(u32& spins, u32* tmo) {
  ++spins;
  if (spins > SPIN_CAP) { __hip_atomic_store(tmo, 1u, __ATOMIC_RELAXED, __HIP_MEMORY_SCOPE_AGENT); return true; }
  if ((spins & 1023u) == 0u && __hip_atomic_load(tmo, __ATOMIC_RELAXED, __HIP_MEMORY_SCOPE_AGENT) != 0u) return true;
  return false;
}
__device__ __forceinline__ u32 homap(u32 lo, u32 flip) { return ((lo & ~8191u) << 1) + (lo & 8191u) + flip; }
constexpr u32 HO_PM = 16384, HO_M = 20480;

#define STAMP(step, k) do {} while (0)
#define STAMPC(step, k) do {} while (0)
#define DUMP() do {} while (0)
struct Args {
  const u16* x; const u16* mem0; const u16* wq; const u16* wk; const u16* wv; const u16* wo;
  const u16* wg; const u16* wu; const u16* wd; unsigned char* ws; u16* out;
};

constexpr int K1_WVO = 2592, K1_LN = 256, K1_LNB = K1_LN / 4, K1_JOBS = K1_WVO + K1_LN, K1_GRID = K1_JOBS / 4 + 8;
__global__ void __launch_bounds__(256) k1_prep(Args a) {
  const int lane = threadIdx.x & 63, wave = __builtin_amdgcn_readfirstlane(threadIdx.x >> 6);
  const int b = blockIdx.x;
  unsigned char* ws = a.ws;
  const int c16 = lane & 15, g = lane >> 4;
  if (b == K1_GRID - 1) {
    for (int i = threadIdx.x; i < 1024; i += 256) ((u32*)(ws + CTL_OFF))[i] = 0u;
    for (int i = threadIdx.x; i < 576; i += 256) {
      u32x4 v = *(const u32x4*)(a.mem0 + (i & 7) * DM + (i >> 3) * 8);
      if (v.x == POISON) v.x = 0x7FC07FC0u; if (v.y == POISON) v.y = 0x7FC07FC0u;
      if (v.z == POISON) v.z = 0x7FC07FC0u; if (v.w == POISON) v.w = 0x7FC07FC0u;
      *(u32x4*)(ws + M0_OFF + i * 16) = v;
    }
    return;
  }
  if (b >= K1_JOBS / 4) return;
  const int job = b * 4 + wave;
  auto poison = [&]() {
    const u32x4 pv = {POISON, POISON, POISON, POISON};
    for (u32 o = (job * 64 + lane) * 16; o < POISON_BYTES; o += K1_JOBS * 64 * 16) *(u32x4*)(ws + o) = pv;
  };
  if (b >= K1_LNB) {
    const int jx = ((b - K1_LNB) >> 3) * 4 + wave, h = (b & 7) * 4 + jx / 81, kp = (jx / 9) % 9, bb = jx % 9;
    u32x2 rw[4][8];
    u32x4 af[2][2][4];
#pragma unroll
    for (int es = 0; es < 4; ++es)
#pragma unroll
      for (int jj = 0; jj < 8; ++jj)
        rw[es][jj] = *(const u32x2*)(a.wo + (size_t)(h * DH + es * 32 + g * 8 + jj) * DM + bb * 64 + c16 * 4);
#pragma unroll
    for (int i = 0; i < 16; ++i) {
      const int k = (kp * 2 + (i >> 3)) * 32 + (c16 >> 2) * 8 + (c16 & 3) + ((i >> 2) & 1) * 4;
      af[i >> 3][(i >> 2) & 1][i & 3] = *(const u32x4*)(a.wv + (size_t)k * (NH * DH) + h * DH + (i & 3) * 32 + g * 8);
    }
    poison();
    __builtin_amdgcn_sched_barrier(0);
    u32x4 bf[4][4];
#pragma unroll
    for (int es = 0; es < 4; ++es)
#pragma unroll
      for (int p = 0; p < 4; ++p) {
        bf[es][0][p] = plo(rw[es][2 * p].x, rw[es][2 * p + 1].x); bf[es][1][p] = phi(rw[es][2 * p].x, rw[es][2 * p + 1].x);
        bf[es][2][p] = plo(rw[es][2 * p].y, rw[es][2 * p + 1].y); bf[es][3][p] = phi(rw[es][2 * p].y, rw[es][2 * p + 1].y);
      }
#pragma unroll
    for (int tt = 0; tt < 2; ++tt) {
      const int t = kp * 2 + tt;
      f32x4 acc[2][4];
#pragma unroll
      for (int rt = 0; rt < 2; ++rt)
#pragma unroll
        for (int i = 0; i < 4; ++i) {
          acc[rt][i] = f32x4{0.f, 0.f, 0.f, 0.f};
#pragma unroll
          for (int es = 0; es < 4; ++es) acc[rt][i] = mfma(af[tt][rt][es], bf[es][i], acc[rt][i]);
        }
      const int j = bb * 8 + (c16 >> 1);
      unsigned char* dst = ws + WVO_OFF + (size_t)j * WVO_J + ((h >> 1) * KS + t) * 1024 + (g * 16 + (h & 1) * 8 + (c16 & 1) * 4) * 16;
#pragma unroll
      for (int i = 0; i < 4; ++i) {
        u32x4 v = {pk(acc[0][i][0], acc[0][i][1]), pk(acc[0][i][2], acc[0][i][3]), pk(acc[1][i][0], acc[1][i][1]), pk(acc[1][i][2], acc[1][i][3])};
        *(u32x4*)(dst + i * 16) = v;
      }
    }
  } else {
    u32x4 vv[2][5];
#pragma unroll
    for (int rr = 0; rr < 2; ++rr)
#pragma unroll
    for (int i = 0; i < 5; ++i) {
      vv[rr][i] = u32x4{0, 0, 0, 0};
      if (i < 4 || c16 < 8) vv[rr][i] = *(const u32x4*)(a.x + (size_t)(job * 8 + rr * 4 + g) * DM + (i * 16 + c16) * 8);
    }
    poison();
#pragma unroll
    for (int rr = 0; rr < 2; ++rr) {
    const int row = job * 8 + rr * 4 + g;
    const u32x4 (&v)[5] = vv[rr];
    float sm = 0.f;
#pragma unroll
    for (int i = 0; i < 20; ++i) sm += bflo(v[i >> 2][i & 3]) + bfhi(v[i >> 2][i & 3]);
#pragma unroll
    for (int o = 1; o < 16; o <<= 1) sm += __shfl_xor(sm, o);
    const float mean = sm * (1.f / DM);
    float q = 0.f;
#pragma unroll
    for (int i = 0; i < 20; ++i) {
      const float d0 = bflo(v[i >> 2][i & 3]) - mean, d1 = bfhi(v[i >> 2][i & 3]) - mean;
      q += (i < 16 || c16 < 8) ? d0 * d0 + d1 * d1 : 0.f;
    }
#pragma unroll
    for (int o = 1; o < 16; o <<= 1) q += __shfl_xor(q, o);
    const float mb = bfr(mean), den = bfr(sqrtf(bfr(bfr(q * (1.f / DM)) + 1e-5f)));
#pragma unroll
    for (int i = 0; i < 5; ++i) {
      u32x4 o;
#pragma unroll
      for (int k = 0; k < 4; ++k) o[k] = pk(bfr(bflo(v[i][k]) - mb) / den, bfr(bfhi(v[i][k]) - mb) / den);
      const int ch = i * 16 + c16;
      if (i < 4 || c16 < 8) *(u32x4*)(ws + XF_OFF + ((row >> 4) * KS + (ch >> 2)) * 1024 + ((ch & 3) * 16 + (row & 15)) * 16) = o;
    }
    }
  }
}

constexpr u32 L_X = 0;
constexpr u32 L_M = 2 * XF_SEG;
constexpr u32 L_QM = L_M + 9216;
constexpr u32 L_QX = L_QM + 8192;

template <bool WA, int ST>
__device__ __forceinline__ void proj2(const u32x4 (&W0)[KS], const u32x4 (&W1)[KS], const char* xb, f32x4& c0, f32x4& c1) {
  u32x4 xf[KS];
#pragma unroll
  for (int t = 0; t < KS; ++t) xf[t] = *(const u32x4*)(xb + t * ST);
  c0 = f32x4{0.f, 0.f, 0.f, 0.f}; c1 = c0;
#pragma unroll
  for (int t = 0; t < KS; ++t) {
    c0 = WA ? mfma(W0[t], xf[t], c0) : mfma(xf[t], W0[t], c0);
    c1 = WA ? mfma(W1[t], xf[t], c1) : mfma(xf[t], W1[t], c1);
  }
  __builtin_amdgcn_sched_group_barrier(0x100, 6, 0);
#pragma unroll
  for (int t = 0; t < KS - 6; ++t) { __builtin_amdgcn_sched_group_barrier(0x008, 2, 0); __builtin_amdgcn_sched_group_barrier(0x100, 1, 0); }
  __builtin_amdgcn_sched_group_barrier(0x008, 12, 0);
}
__device__ __forceinline__ void stage_x(const unsigned char* ws, int seg, int wave, int lane) {
  const unsigned char* src = ws + XF_OFF + seg * XF_SEG + lane * 16;
  const u32 dst = L_X + (seg & 1) * XF_SEG;
#pragma unroll
  for (int i = 0; i < 5; ++i) {
    const int p = i * 8 + wave;
    if (p < 36) glds16(src + p * 1024, dst + p * 1024);
  }
}
__device__ __forceinline__ u32 get_flips(__amdgpu_buffer_rsrc_t r, int lane, u32* tmo) {
  u32 f, spins = 0;
  for (;;) {
    f = __builtin_amdgcn_raw_buffer_load_b32(r, FLP_OFF + lane * 4, 0, SC1);
    if (allok(f)) break;
    if (spin_fail(spins, tmo)) break;
    __builtin_amdgcn_s_sleep(8);
  }
  return f & 8192u;
}
__device__ __forceinline__ void gather_m(__amdgpu_buffer_rsrc_t r, u32 base, u32 flip, bool first, char* smem, int wave, int lane, u32* tmo) {
  const u32 l0 = (wave * 64 + lane) * 16, l1 = (512 + wave * 8 + (lane & 7)) * 16;
  const u32 o0 = first ? M0_OFF + l0 : base + homap(HO_M + l0, flip), o1 = first ? M0_OFF + l1 : base + homap(HO_M + l1, flip);
  u32 spins = 0;
  u32x4 v, w;
  for (;;) {
    v = __builtin_amdgcn_raw_buffer_load_b128(r, o0, 0, SC1);
    w = __builtin_amdgcn_raw_buffer_load_b128(r, o1, 0, SC1);
    if (allok(mx(mx4(v), mx4(w)))) break;
    if (spin_fail(spins, tmo)) break;
  }
  *(u32x4*)(smem + L_M + l0) = v;
  if (lane < 8) *(u32x4*)(smem + L_M + l1) = w;
  wait_vm();
}

__device__ __forceinline__ void load_wqk(const Args& a, int h, u32x4 (&W)[2][KS], char* smem) {
  const int tid = threadIdx.x, lane = tid & 63, wave = __builtin_amdgcn_readfirstlane(tid >> 6);
  const int c16 = lane & 15, g = lane >> 4;
  {
    u32x4 st[18];
    auto ldw = [&](int hf) {
#pragma unroll
      for (int i = 0; i < 18; ++i) {
        const int pc = i * 512 + tid;
        st[i] = *(const u32x4*)(((pc & 16) ? a.wk : a.wq) + (size_t)(hf * 288 + (pc >> 5)) * (NH * DH) + h * DH + (pc & 15) * 8);
      }
    };
    ldw(0);
#pragma unroll
    for (int hf = 0; hf < 2; ++hf) {
#pragma unroll
      for (int i = 0; i < 18; ++i) { const int pc = i * 512 + tid; *(u32x4*)(smem + (pc >> 5) * 528 + (pc & 31) * 16) = st[i]; }
      if (hf == 0) ldw(1);
      bar();
      const char* wl = smem + g * 8 * 528 + wave * 64 + c16 * 4;
#pragma unroll
      for (int t = 0; t < 9; ++t) {
#pragma unroll
        for (int p = 0; p < 4; ++p) {
          const u32 d0 = *(const u32*)(wl + (t * 32 + 2 * p) * 528), d1 = *(const u32*)(wl + (t * 32 + 2 * p + 1) * 528);
          W[0][hf * 9 + t][p] = plo(d0, d1); W[1][hf * 9 + t][p] = phi(d0, d1);
        }
        asm volatile("" : "+v"(W[0][hf * 9 + t]), "+v"(W[1][hf * 9 + t]) :: "memory");
      }
      bar();
    }
  }
}

__device__ __forceinline__ void role_a(const Args& a, int h, char* smem) {
  const int tid = threadIdx.x, lane = tid & 63, wave = __builtin_amdgcn_readfirstlane(tid >> 6);
  const int c16 = lane & 15, g = lane >> 4;
  unsigned char* ws = a.ws;
  u32* tmo = (u32*)(ws + CTL_OFF) + 64;
  __amdgpu_buffer_rsrc_t r = __builtin_amdgcn_make_buffer_rsrc(ws, 0, WS_END, 0x00020000);
  STAMP(64, 0);
  u32x4 W[2][KS];
  load_wqk(a, h, W, smem);
  const u32 flips = get_flips(r, lane, tmo);
  const u32 lx = (((h >> 1) * 4 + g) * 16 + (h & 1) * 8 + (lane & 7)) * 16, lm = HO_PM + h * 128 + g * 64 + (lane & 7) * 8;
  STAMP(64, 1); STAMPC(65, 0); STAMP(65, 1);
  u32x4 kf[8];
  u32 fl = POISON;
  auto q_load = [&](int seg) {
    const u32 fo = FLQ_OFF + seg * 128 + h * 4;
    u32 spins = 0;
    while (!allok(fl)) {
      fl = __builtin_amdgcn_raw_buffer_load_b32(r, fo, 0, SC1);
      if (spin_fail(spins, tmo)) break;
    }
    asm volatile("" ::: "memory");
    const u32 qo = QX_OFF + ((seg * NH + h) * 16 + (wave - 1) * 4) * 1024 + lane * 16;
#pragma unroll
    for (int w = 0; w < 4; ++w) kf[w] = __builtin_amdgcn_raw_buffer_load_b128(r, qo + w * 1024, 0, SC1);
    if (wave == 3) {
#pragma unroll
      for (int w = 4; w < 8; ++w) kf[w] = __builtin_amdgcn_raw_buffer_load_b128(r, qo + w * 1024, 0, SC1);
#pragma unroll
      for (int w = 0; w < 8; ++w) *(u32x4*)(smem + L_QX + (seg & 1) * 16384 + 8192 + w * 1024 + lane * 16) = kf[w];
    }
    fl = seg + 1 < NSEG ? __builtin_amdgcn_raw_buffer_load_b32(r, fo + 128, 0, SC1) : 0u;
  };
  if (wave >= 1 && wave <= 3) q_load(0);
  for (int s = 0; s < NSEG; ++s) {
    const u32 flip = __builtin_amdgcn_readlane(flips, s), base = HO_OFF + s * 65536;
    STAMP(s, 0);
    gather_m(r, base, flip, s == 0, smem, wave, lane, tmo);
    STAMP(s, 1);
    bar();
    STAMP(s, 2);
    {
      f32x4 c0, c1;
      proj2<true, 512>(W[0], W[1], smem + L_M + g * 128 + (lane & 7) * 16, c0, c1);
      u32x4 o = {pk(c0[0], c1[0]), pk(c0[1], c1[1]), pk(c0[2], c1[2]), pk(c0[3], c1[3])};
      *(u32x4*)(smem + L_QM + wave * 1024 + lane * 16) = o;
    }
    if (wave == 0) {
      __builtin_amdgcn_s_setprio(3);
#pragma unroll
      for (int w = 0; w < 8; ++w) kf[w] = *(const u32x4*)(smem + L_QX + (s & 1) * 16384 + 8192 + lane * 16 + w * 1024);
    }
    bar();
    STAMP(s, 3);
    const float C2 = 0.08838834764831845f * 1.4426950408889634f;
    if (wave == 0) {
      if (s < NSEG - 1) {
        const char* qb = smem + L_QM + lane * 16;
        f32x4 s0 = {0.f, 0.f, 0.f, 0.f}, s1 = s0, s2 = s0;
#pragma unroll
        for (int w = 0; w < 4; ++w) {
          const u32x4 qf = *(const u32x4*)(qb + w * 1024);
          s2 = mfma(*(const u32x4*)(qb + 4096 + w * 1024), qf, s2);
          s0 = mfma(kf[w], qf, s0);
          s1 = mfma(kf[4 + w], qf, s1);
        }
        const bool hi = c16 >= 8;
        float v[6];
#pragma unroll
        for (int i = 0; i < 4; ++i) v[i] = hi ? s1[i] : s0[i];
        v[4] = g < 2 ? (hi ? s2[2] : s2[0]) : -INFINITY;
        v[5] = g < 2 ? (hi ? s2[3] : s2[1]) : -INFINITY;
        float m = vmax(vmax3(v[0], v[1], v[2]), vmax3(v[3], v[4], v[5]));
        m = xmax(m);
        m = vmax(m, ror8(m)) * C2;
#pragma unroll
        for (int i = 0; i < 6; ++i) v[i] = __builtin_amdgcn_exp2f(__builtin_fmaf(v[i], C2, -m));
        float sum = ((v[0] + v[1]) + (v[2] + v[3])) + (v[4] + v[5]);
        sum += ror8(sum);
        const float inv = __builtin_amdgcn_rcpf(xsum(sum));
        const u32x2 px = {pk(v[0] * inv, v[1] * inv) & 0x7FFF7FFFu, pk(v[2] * inv, v[3] * inv) & 0x7FFF7FFFu};
        const u32 pm = pk(v[4] * inv, v[5] * inv) & 0x7FFF7FFFu;
        __builtin_amdgcn_raw_buffer_store_b64(px, r, base + homap(lx + (c16 >> 3) * 8, flip), 0, SC1);
        if (g < 2) __builtin_amdgcn_raw_buffer_store_b32(pm, r, base + homap(lm + (c16 >> 3) * 4, flip), 0, SC1);
      }
      __builtin_amdgcn_s_setprio(0);
    } else if (wave < 3) {
      const char* qx = smem + L_QX + (s & 1) * 16384 + lane * 16;
      f32x4 s0 = {0.f, 0.f, 0.f, 0.f}, s1 = s0, s2 = s0;
#pragma unroll
      for (int w = 0; w < 4; ++w) {
        s2 = mfma(*(const u32x4*)(smem + L_QM + 4096 + lane * 16 + w * 1024), kf[w], s2);
        s0 = mfma(*(const u32x4*)(qx + 8192 + w * 1024), kf[w], s0);
        s1 = mfma(*(const u32x4*)(qx + 12288 + w * 1024), kf[w], s1);
      }
      const int qi = (wave - 1) * 16 + c16;
      const float NEG = -1.0e9f * 1.4426950408889634f;
      float v[12];
#pragma unroll
      for (int i = 0; i < 4; ++i) {
        v[i] = (4 * g + i <= qi) ? s0[i] * C2 : NEG;
        v[4 + i] = (16 + 4 * g + i <= qi) ? s1[i] * C2 : NEG;
        v[8 + i] = g < 2 ? s2[i] * C2 : -INFINITY;
      }
      float m = v[0];
#pragma unroll
      for (int i = 1; i < 12; ++i) m = fmaxf(m, v[i]);
      m = xmax(m);
      float sum = 0.f;
#pragma unroll
      for (int i = 0; i < 12; ++i) { v[i] = __builtin_amdgcn_exp2f(v[i] - m); sum += v[i]; }
      const float inv = __builtin_amdgcn_rcpf(xsum(sum));
      u32x4 px; u32x2 pm;
#pragma unroll
      for (int i = 0; i < 4; ++i) px[i] = pk(v[2 * i] * inv, v[2 * i + 1] * inv) & 0x7FFF7FFFu;
#pragma unroll
      for (int i = 0; i < 2; ++i) pm[i] = pk(v[8 + 2 * i] * inv, v[9 + 2 * i] * inv) & 0x7FFF7FFFu;
      const u32 tb = PT_OFF + s * PT_STEP + ((wave - 1) * 2 + (c16 >> 3)) * 20480;
      __builtin_amdgcn_raw_buffer_store_b128(px, r, tb + lx, 0, SC1);
      if (g < 2) __builtin_amdgcn_raw_buffer_store_b64(pm, r, tb + lm, 0, SC1);
    }
    STAMP(s, 4);
    if (wave >= 1 && wave <= 3 && s + 1 < NSEG) q_load(s + 1);
    bar();
  }
  STAMPC(65, 2); STAMP(65, 3);
}

__device__ __forceinline__ void role_b(const Args& a, int j, char* smem) {
  const int tid = threadIdx.x, lane = tid & 63, wave = __builtin_amdgcn_readfirstlane(tid >> 6);
  const int c16 = lane & 15, g = lane >> 4;
  unsigned char* ws = a.ws;
  u32* tmo = (u32*)(ws + CTL_OFF) + 64;
  __amdgpu_buffer_rsrc_t r = __builtin_amdgcn_make_buffer_rsrc(ws, 0, WS_END, 0x00020000);
  STAMP(64, 0);
  u32x4 W[2][KS];
  {
    const unsigned char* wsrc = ws + WVO_OFF + (size_t)j * WVO_J + wave * 2 * KS * 1024 + lane * 16;
#pragma unroll
    for (int tt = 0; tt < 2; ++tt)
#pragma unroll
      for (int t = 0; t < KS; ++t) W[tt][t] = *(const u32x4*)(wsrc + (tt * KS + t) * 1024);
  }
  wait_vm();
  bar();
  const u32 flips = get_flips(r, lane, tmo);
  STAMP(64, 1);
  u32x4 nx0, nx1;
  u32 fl = POISON;
  auto ux_load = [&](int seg) {
    const u32 fo = FLG_OFF + seg * 128 + wave * 16 + (lane & 3) * 4;
    u32 spins = 0;
    while (!allok(fl)) {
      fl = __builtin_amdgcn_raw_buffer_load_b32(r, fo, 0, SC1);
      if (spin_fail(spins, tmo)) break;
    }
    asm volatile("" ::: "memory");
    const u32 uo = UX_OFF + ((seg * NB + j) * 16 + wave * 2) * 1024 + lane * 16;
    nx0 = __builtin_amdgcn_raw_buffer_load_b128(r, uo, 0, SC1);
    nx1 = __builtin_amdgcn_raw_buffer_load_b128(r, uo + 1024, 0, SC1);
    fl = seg + 1 < NSEG ? __builtin_amdgcn_raw_buffer_load_b32(r, fo + 128, 0, SC1) : 0u;
  };
  const u32 lx = wave * 2048 + lane * 16;
  const u32 lm = HO_PM + (wave * 4 + (c16 >> 3)) * 128 + (g & 1) * 64 + (lane & 7) * 8;
  const bool diag = (c16 >> 3) == (g >> 1);
  const u32 redo = ((wave * 2 + (g >> 1)) * 64 + (lane & 7) * 8 + (g & 1) * 4) * 4;
  for (int s = 0; s < NSEG; ++s) {
    const u32 flip = __builtin_amdgcn_readlane(flips, s), base = HO_OFF + s * 65536;
    STAMP(s, 0);
    gather_m(r, base, flip, s == 0, smem, wave, lane, tmo);
    bar();
    STAMP(s, 1);
    u32x4 um;
    {
      f32x4 c0, c1;
      proj2<false, 512>(W[0], W[1], smem + L_M + g * 128 + (lane & 7) * 16, c0, c1);
      um = u32x4{pk(c0[0], c0[1]), pk(c0[2], c0[3]), pk(c1[0], c1[1]), pk(c1[2], c1[3])};
      if (g < 2) __builtin_amdgcn_raw_buffer_store_b128(um, r, UM_OFF + ((s * NB + j) * 8 + wave) * 512 + lane * 16, 0, SC1);
      else um = u32x4{0, 0, 0, 0};
    }
    if (s + 1 < NSEG) {
      if (s == 0) ux_load(0);
      STAMP(s, 2);
      const u32 o0 = base + homap(lx, flip), o1 = o0 + 1024, o2 = base + homap(lm, flip), o3 = o2 + 256;
      u32x4 p0, p1; u32x2 q0, q1;
      u32 spins = 0;
      for (;;) {
        p0 = __builtin_amdgcn_raw_buffer_load_b128(r, o0, 0, SC1);
        p1 = __builtin_amdgcn_raw_buffer_load_b128(r, o1, 0, SC1);
        q0 = __builtin_amdgcn_raw_buffer_load_b64(r, o2, 0, SC1);
        q1 = __builtin_amdgcn_raw_buffer_load_b64(r, o3, 0, SC1);
        if (allok(mx(mx(mx4(p0), mx4(p1)), mx(mx2(q0), mx2(q1))))) break;
        if (spin_fail(spins, tmo)) break;
      }
      STAMP(s, 3);
      f32x4 d = {0.f, 0.f, 0.f, 0.f}, e = d;
      const u32x4 pmv = {q0.x, q0.y, q1.x, q1.y};
      d = mfma(nx0, p0, d); e = mfma(nx1, p1, e); d = mfma(um, pmv, d);
      d += e;
      if (diag) *(f32x4*)(smem + L_QM + redo) = d;
    }
    wait_vm();
    bar();
    if (wave == 0 && s + 1 < NSEG) {
      const char* rp = smem + L_QM + (lane >> 5) * 2048 + (lane & 31) * 8;
      f32x2 acc = *(const f32x2*)rp;
#pragma unroll
      for (int p = 1; p < 8; ++p) acc += *(const f32x2*)(rp + p * 256);
      auto t0 = __builtin_amdgcn_permlane32_swap(__float_as_uint(acc.x), __float_as_uint(acc.x), false, false);
      auto t1 = __builtin_amdgcn_permlane32_swap(__float_as_uint(acc.y), __float_as_uint(acc.y), false, false);
      const u32 o = pks(__uint_as_float(t0[0]) + __uint_as_float(t0[1]), __uint_as_float(t1[0]) + __uint_as_float(t1[1]));
      const u32 nflip = __builtin_amdgcn_readlane(flips, s + 1);
      if (lane < 32) __builtin_amdgcn_raw_buffer_store_b32(o, r, base + 65536 + homap(HO_M + j * 128 + lane * 4, nflip), 0, SC1);
    }
    STAMP(s, 4);
    if (tid == 64) __builtin_amdgcn_raw_buffer_store_b32(1u, r, FLM_OFF + s * 3072 + (j / 6) * 128 + (j % 6) * 4, 0, SC1);
    if (s + 2 < NSEG) ux_load(s + 1);
    STAMP(s, 5);
  }
  STAMP(65, 0);
}

__device__ __forceinline__ void role_o(const Args& a, int o24, char* smem) {
  const int tid = threadIdx.x, lane = tid & 63, wave = __builtin_amdgcn_readfirstlane(tid >> 6);
  const int c16 = lane & 15, g = lane >> 4, o = o24 >> 1;
  unsigned char* ws = a.ws;
  u32* tmo = (u32*)(ws + CTL_OFF) + 64;
  __amdgpu_buffer_rsrc_t r = __builtin_amdgcn_make_buffer_rsrc(ws, 0, WS_END, 0x00020000);
  STAMP(64, 0);
  if (wave < 3 && o24 * 3 + wave < NSEG) {
    const int blk = o24 * 3 + wave;
    u32 best[2] = {~0u, ~0u};
    for (int rep = 0; rep < 4; ++rep)
      for (int f = 0; f < 2; ++f) {
        u32* p = (u32*)(ws + HO_OFF + blk * 65536 + homap(32768 - 128 * (rep + 1), f * 8192));
        __hip_atomic_store(p, POISON, __ATOMIC_RELAXED, __HIP_MEMORY_SCOPE_AGENT);
        wait_vm();
        u64 t0, t1;
        asm volatile("s_memtime %0\n\ts_waitcnt lgkmcnt(0)" : "=s"(t0) :: "memory");
        u32 v = __hip_atomic_load(p, __ATOMIC_RELAXED, __HIP_MEMORY_SCOPE_AGENT);
        asm volatile("s_waitcnt vmcnt(0)" : "+v"(v) :: "memory");
        asm volatile("s_memtime %0\n\ts_waitcnt lgkmcnt(0)" : "=s"(t1) :: "memory");
        const u32 d = (u32)(t1 - t0);
        best[f] = d < best[f] ? d : best[f];
      }
    if (lane == 0) __builtin_amdgcn_raw_buffer_store_b32(best[1] < best[0] ? 8192u : 0u, r, FLP_OFF + blk * 4, 0, SC1);
  }
  STAMP(64, 1);
  const u32 lx = wave * 2048 + lane * 16;
  const u32 lm = HO_PM + (wave * 4 + (c16 >> 3)) * 128 + (g & 1) * 64 + (lane & 7) * 8;
  const bool diag = (c16 >> 3) == (g >> 1);
  const u32 redo = ((wave * 2 + (g >> 1)) * 64 + (lane & 7) * 8 + (g & 1) * 4) * 4;
  u32x4 x0[6], x1[6], um[6];
  auto ux_load = [&](int s) {
    u32 spins = 0;
    while (!allok(__builtin_amdgcn_raw_buffer_load_b32(r, FLG_OFF + s * 128 + wave * 16 + (lane & 3) * 4, 0, SC1))) {
      if (spin_fail(spins, tmo)) break;
      __builtin_amdgcn_s_sleep(16);
    }
    asm volatile("" ::: "memory");
#pragma unroll
    for (int c = 0; c < 6; ++c) {
      const u32 uo = UX_OFF + ((s * NB + o * 6 + c) * 16 + wave * 2) * 1024 + lane * 16;
      x0[c] = __builtin_amdgcn_raw_buffer_load_b128(r, uo, 0, SC1);
      x1[c] = __builtin_amdgcn_raw_buffer_load_b128(r, uo + 1024, 0, SC1);
    }
  };
  const u32 e0 = (((tid & 63) >> 1) * DM + (o * 6 + (tid >> 6)) * 8 + (tid & 1) * 4) * 2;
  u32x2 xv = {0, 0}, fv = {0, 0};
  ux_load(o24 & 1);
  for (int s = o24 & 1; s < NSEG; s += 2) {
    u32 spins = 0;
    if (wave == 0) {
      const u32 fo = FLM_OFF + s * 3072 + o * 128 + (lane % 6) * 4;
      while (!allok(__builtin_amdgcn_raw_buffer_load_b32(r, fo, 0, SC1))) {
        if (spin_fail(spins, tmo)) break;
        __builtin_amdgcn_s_sleep(4);
      }
    }
    if (s - 2 < SPLIT) wait_vm();
    bar();
    if (tid == 0 && s >= 2 && s - 2 < SPLIT) __builtin_amdgcn_raw_buffer_store_b32(1u, r, FLA_OFF + (s - 2) * 128 + o * 4, 0, SC1);
    const u32 eo = e0 + s * 32 * DM * 2;
#pragma unroll
    for (int c = 0; c < 6; ++c)
      um[c] = __builtin_amdgcn_raw_buffer_load_b128(r, UM_OFF + ((s * NB + o * 6 + c) * 8 + wave) * 512 + (lane & 31) * 16, 0, SC1);
    u32x4 p0[4], p1[4]; u32x2 q0[4], q1[4];
    for (;;) {
      u32 m = 0;
#pragma unroll
      for (int u = 0; u < 4; ++u) {
        const u32 tb = PT_OFF + s * PT_STEP + u * 20480;
        p0[u] = __builtin_amdgcn_raw_buffer_load_b128(r, tb + lx, 0, SC1);
        p1[u] = __builtin_amdgcn_raw_buffer_load_b128(r, tb + lx + 1024, 0, SC1);
        q0[u] = __builtin_amdgcn_raw_buffer_load_b64(r, tb + lm, 0, SC1);
        q1[u] = __builtin_amdgcn_raw_buffer_load_b64(r, tb + lm + 256, 0, SC1);
      }
#pragma unroll
      for (int u = 0; u < 4; ++u) m = mx(m, mx(mx(mx4(p0[u]), mx4(p1[u])), mx(mx2(q0[u]), mx2(q1[u]))));
      if (allok(m)) break;
      if (spin_fail(spins, tmo)) break;
    }
#pragma unroll
    for (int c = 0; c < 6; ++c) {
      if (g >= 2) um[c] = u32x4{0, 0, 0, 0};
#pragma unroll
      for (int u = 0; u < 4; ++u) {
        f32x4 d = {0.f, 0.f, 0.f, 0.f};
        const u32x4 pmv = {q0[u].x, q0[u].y, q1[u].x, q1[u].y};
        d = mfma(x0[c], p0[u], d); d = mfma(x1[c], p1[u], d); d = mfma(um[c], pmv, d);
        if (diag) *(f32x4*)(smem + (c * 4 + u) * 4096 + redo) = d;
      }
    }
    bar();
    if (tid < 384) {
      const char* rp = smem + (tid >> 4) * 4096 + (tid & 15) * 16;
      f32x4 acc = *(const f32x4*)rp;
#pragma unroll
      for (int p = 1; p < 16; ++p) acc += *(const f32x4*)(rp + p * 256);
      const u32x2 av = {pk(acc[0], acc[1]), pk(acc[2], acc[3])};
      if (s < SPLIT) __builtin_amdgcn_raw_buffer_store_b64(av, r, AO_OFF + eo, 0, SC1);
      else *(u32x2*)((char*)a.out + eo) = u32x2{fin(xv.x, av.x, fv.x), fin(xv.y, av.y, fv.y)};
      if (s + 2 >= SPLIT && s + 2 < NSEG) {
        while (!allok(__builtin_amdgcn_raw_buffer_load_b32(r, FLF_OFF + (((s + 2) >> 2) * 6 + (o >> 1)) * 128, 0, SC1))) {
          if (spin_fail(spins, tmo)) break;
          __builtin_amdgcn_s_sleep(16);
        }
        asm volatile("" ::: "memory");
        xv = *(const u32x2*)((const char*)a.x + eo + 64 * DM * 2);
        fv = __builtin_amdgcn_raw_buffer_load_b64(r, FO_OFF + eo + 64 * DM * 2, 0, SC1);
      }
    }
    if (s + 2 < NSEG) ux_load(s + 2);
    STAMP(s, 0);
  }
}

__device__ __forceinline__ void role_v(const Args& a, int h, char* smem) {
  const int tid = threadIdx.x, lane = tid & 63, wave = __builtin_amdgcn_readfirstlane(tid >> 6);
  const int c16 = lane & 15, g = lane >> 4;
  unsigned char* ws = a.ws;
  __amdgpu_buffer_rsrc_t r = __builtin_amdgcn_make_buffer_rsrc(ws, 0, WS_END, 0x00020000);
  STAMP(64, 0);
  u32x4 WV[KS], WO[5][4];
  {
    const u32 sel = (c16 & 1) ? 0x07060302u : 0x05040100u;
    u32x4 st[18];
#pragma unroll
    for (int i = 0; i < 18; ++i) { const int pc = i * 512 + tid; st[i] = *(const u32x4*)(a.wv + (size_t)(pc >> 4) * (NH * DH) + h * DH + (pc & 15) * 8); }
#pragma unroll
    for (int i = 0; i < 18; ++i) { const int pc = i * 512 + tid; *(u32x4*)(smem + (pc >> 4) * 272 + (pc & 15) * 16) = st[i]; }
#pragma unroll
    for (int i = 0; i < 18; ++i) st[i] = *(const u32x4*)(a.wo + (size_t)h * DH * DM + (i * 512 + tid) * 8);
    bar();
    {
      const char* wl = smem + g * 8 * 272 + wave * 32 + (c16 >> 1) * 4;
#pragma unroll
      for (int t = 0; t < KS; ++t) {
#pragma unroll
        for (int p = 0; p < 4; ++p)
          WV[t][p] = __builtin_amdgcn_perm(*(const u32*)(wl + (t * 32 + 2 * p + 1) * 272), *(const u32*)(wl + (t * 32 + 2 * p) * 272), sel);
        asm volatile("" : "+v"(WV[t]) :: "memory");
      }
    }
    bar();
#pragma unroll
    for (int i = 0; i < 18; ++i) { const int pc = i * 512 + tid; *(u32x4*)(smem + (pc / 72) * 1168 + (pc % 72) * 16) = st[i]; }
    bar();
#pragma unroll
    for (int i = 0; i < 5; ++i) {
      const int ct = wave + 8 * i < 36 ? wave + 8 * i : 0;
      const char* wl = smem + g * 4 * 1168 + ct * 32 + (c16 >> 1) * 4;
#pragma unroll
      for (int es = 0; es < 4; ++es) {
#pragma unroll
        for (int p = 0; p < 4; ++p) {
          const int e0 = 32 * es + 16 * (p >> 1) + 2 * (p & 1);
          WO[i][es][p] = __builtin_amdgcn_perm(*(const u32*)(wl + (e0 + 1) * 1168), *(const u32*)(wl + e0 * 1168), sel);
        }
        asm volatile("" : "+v"(WO[i][es]) :: "memory");
      }
    }
    bar();
  }
  STAMP(64, 1);
  stage_x(ws, 0, wave, lane);
  for (int sg = 0; sg < NSEG; ++sg) {
    wait_vm();
    bar();
    if (sg > 0 && tid == 0) __builtin_amdgcn_raw_buffer_store_b32(1u, r, FLG_OFF + (sg - 1) * 128 + h * 4, 0, SC1);
    if (sg + 1 < NSEG) stage_x(ws, sg + 1, wave, lane);
    {
      const char* xb = smem + L_X + (sg & 1) * XF_SEG + lane * 16;
      f32x4 c0 = {0.f, 0.f, 0.f, 0.f}, c1 = c0;
#pragma unroll
      for (int t = 0; t < KS; ++t) {
        c0 = mfma(WV[t], *(const u32x4*)(xb + t * 1024), c0);
        c1 = mfma(WV[t], *(const u32x4*)(xb + (KS + t) * 1024), c1);
      }
      __builtin_amdgcn_sched_group_barrier(0x100, 8, 0);
#pragma unroll
      for (int t = 0; t < KS - 4; ++t) { __builtin_amdgcn_sched_group_barrier(0x008, 2, 0); __builtin_amdgcn_sched_group_barrier(0x100, 2, 0); }
      __builtin_amdgcn_sched_group_barrier(0x008, 8, 0);
      char* vt = smem + L_QM + (wave >> 1) * 1024 + lane * 16 + (wave & 1) * 8;
      *(u32x2*)vt = u32x2{pk(c0[0], c0[1]), pk(c0[2], c0[3])};
      *(u32x2*)(vt + 4096) = u32x2{pk(c1[0], c1[1]), pk(c1[2], c1[3])};
    }
    bar();
    u32x4 vf[2][4];
#pragma unroll
    for (int rt = 0; rt < 2; ++rt)
#pragma unroll
      for (int es = 0; es < 4; ++es) vf[rt][es] = *(const u32x4*)(smem + L_QM + rt * 4096 + es * 1024 + lane * 16);
#pragma unroll
    for (int i = 0; i < 5; ++i) {
      const int ct = wave + 8 * i;
      if (ct < 36) {
        f32x4 d0 = {0.f, 0.f, 0.f, 0.f}, d1 = d0;
#pragma unroll
        for (int es = 0; es < 4; ++es) { d0 = mfma(vf[0][es], WO[i][es], d0); d1 = mfma(vf[1][es], WO[i][es], d1); }
        const u32x4 o = {pk(d0[0], d0[1]), pk(d0[2], d0[3]), pk(d1[0], d1[1]), pk(d1[2], d1[3])};
        __builtin_amdgcn_raw_buffer_store_b128(o, r, UX_OFF + ((sg * NB + ct * 2 + (c16 >> 3)) * 16 + (h >> 1)) * 1024 + (g * 16 + (h & 1) * 8 + (lane & 7)) * 16, 0, SC1);
      }
    }
    if (sg == 0) STAMP(64, 2);
  }
  wait_vm();
  bar();
  if (tid == 0) __builtin_amdgcn_raw_buffer_store_b32(1u, r, FLG_OFF + (NSEG - 1) * 128 + h * 4, 0, SC1);
  STAMP(64, 3);
}

__device__ __forceinline__ float gelu_tanh(float x) {
  const float u = 0.7978845608028654f * (x + 0.044715f * x * x * x);
  const float e = __expf(2.f * u);
  return 0.5f * x * (2.f - 2.f / (e + 1.f));
}

__device__ __forceinline__ void role_f(const Args& a, int f, char* smem) {
  const int tid = threadIdx.x, lane = tid & 63, wave = __builtin_amdgcn_readfirstlane(tid >> 6);
  const int c16 = lane & 15, g = lane >> 4;
  unsigned char* ws = a.ws;
  u32* tmo = (u32*)(ws + CTL_OFF) + 64;
  u32* cnt = (u32*)(ws + CTL_OFF) + 128;
  __amdgpu_buffer_rsrc_t r = __builtin_amdgcn_make_buffer_rsrc(ws, 0, WS_END, 0x00020000);
  STAMP(64, 0);
  auto stage = [&](int sg) {
#pragma unroll
    for (int i = 0; i < 5; ++i) {
      const int p = min(i * 8 + wave, 35);
      glds16(ws + XF_OFF + sg * XF_SEG + p * 1024 + lane * 16, (sg & 3) * XF_SEG + p * 1024);
    }
  };
  if (f >= 64) {
    const int h = f - 64;
    u32x4 WW[2][KS];
    load_wqk(a, h, WW, smem);
    wait_vm();
    stage(0); stage(1);
    for (int sg = 0; sg < NSEG; ++sg) {
      if (sg < 4) wait_vm();
      else asm volatile("s_waitcnt vmcnt(5)" ::: "memory");
      bar();
      if (sg >= 1 && tid == 0) __builtin_amdgcn_raw_buffer_store_b32(1u, r, FLQ_OFF + (sg < 4 ? sg - 1 : sg - 2) * 128 + h * 4, 0, SC1);
      stage(min(sg + 2, NSEG - 1));
#pragma unroll
      for (int rt = 0; rt < 2; ++rt) {
        f32x4 c0, c1;
        proj2<true, 1024>(WW[0], WW[1], smem + (sg & 3) * XF_SEG + rt * KS * 1024 + lane * 16, c0, c1);
        const u32x4 o = {pk(c0[0], c1[0]), pk(c0[1], c1[1]), pk(c0[2], c1[2]), pk(c0[3], c1[3])};
        __builtin_amdgcn_raw_buffer_store_b128(o, r, QX_OFF + ((sg * NH + h) * 16 + (wave >> 2) * 8 + rt * 4 + (wave & 3)) * 1024 + lane * 16, 0, SC1);
      }
    }
    wait_vm();
    bar();
    if (tid < 2) __builtin_amdgcn_raw_buffer_store_b32(1u, r, FLQ_OFF + (NSEG - 2 + tid) * 128 + h * 4, 0, SC1);
  }
  for (int it = f * 27 + wave; it < f * 27 + 27; it += 8) {
    const int ks = it / 18, bb = it % 18;
    u32 d[8];
#pragma unroll
    for (int jj = 0; jj < 8; ++jj)
      d[jj] = *(const u32*)(a.wd + (size_t)(ks * 32 + (jj >> 2) * 16 + g * 4 + (jj & 3)) * DM + bb * 32 + c16 * 2);
    const u32x4 lo = {plo(d[0], d[1]), plo(d[2], d[3]), plo(d[4], d[5]), plo(d[6], d[7])};
    const u32x4 hi = {phi(d[0], d[1]), phi(d[2], d[3]), phi(d[4], d[5]), phi(d[6], d[7])};
    __builtin_amdgcn_raw_buffer_store_b128(lo, r, WDF_OFF + (bb * 144 + ks) * 2048 + lane * 16, 0, SC1);
    __builtin_amdgcn_raw_buffer_store_b128(hi, r, WDF_OFF + (bb * 144 + ks) * 2048 + 1024 + lane * 16, 0, SC1);
  }
#pragma unroll 1
  for (int job = 0; job < 3; ++job) {
    int tid = threadIdx.x;
    asm volatile("" : "+v"(tid));
    const int hv = wave & 3, rt = wave >> 2;
    const int u0 = f < 64 ? f * 66 : 4224 + (f - 64) * 12, sl = (u0 >> 6) + job;
    const int q0 = job == 0 ? (u0 & 63) : 0, q1 = min(u0 + (f < 64 ? 66 : 12) - sl * 64, 64);
    if (q1 <= q0) break;
    u32x4 WW[2][KS];
#pragma unroll
    for (int mat = 0; mat < 2; ++mat) {
      __syncthreads();
      u32x4 st[9];
#pragma unroll
      for (int i = 0; i < 9; ++i) {
        const int pc = i * 512 + tid;
        st[i] = *(const u32x4*)((mat ? a.wu : a.wg) + (size_t)(pc >> 3) * FF + sl * 64 + (pc & 7) * 8);
      }
#pragma unroll
      for (int i = 0; i < 9; ++i) { const int pc = i * 512 + tid; *(u32x4*)(smem + (pc >> 3) * 144 + (pc & 7) * 16) = st[i]; }
      __syncthreads();
      const char* wl = smem + g * 8 * 144 + hv * 32 + (c16 >> 1) * 4;
      const u32 sel = (c16 & 1) ? 0x07060302u : 0x05040100u;
#pragma unroll
      for (int t = 0; t < KS; ++t) {
#pragma unroll
        for (int p = 0; p < 4; ++p) {
          const int o = (t * 32 + 2 * p) * 144;
          WW[mat][t][p] = __builtin_amdgcn_perm(*(const u32*)(wl + o + 144), *(const u32*)(wl + o), sel);
        }
        asm volatile("" : "+v"(WW[mat][t]) :: "memory");
      }
    }
    __syncthreads();
    if (job == 0) STAMP(64, 1);
    stage(q0); stage(min(q0 + 1, q1 - 1));
    for (int sg = q0; sg < q1; ++sg) {
      asm volatile("s_waitcnt vmcnt(5)" ::: "memory");
      bar();
      stage(min(sg + 2, q1 - 1));
      f32x4 cg, cu;
      proj2<true, 1024>(WW[0], WW[1], smem + (sg & 3) * XF_SEG + rt * KS * 1024 + lane * 16, cg, cu);
      float hh[4];
#pragma unroll
      for (int i = 0; i < 4; ++i) hh[i] = bfr(gelu_tanh(bfr(cg[i]))) * bfr(cu[i]);
      const u32x2 o = {pk(hh[0], hh[1]), pk(hh[2], hh[3])};
      __builtin_amdgcn_raw_buffer_store_b64(o, r, H_OFF + ((sg * 2 + rt) * 144 + sl * 2 + (hv >> 1)) * 1024 + lane * 16 + (hv & 1) * 8, 0, SC1);
    }
    wait_vm();
  }
  wait_vm();
  __syncthreads();
  STAMP(64, 2);
  if (tid == 0) {
    __hip_atomic_fetch_add(cnt, 1u, __ATOMIC_RELAXED, __HIP_MEMORY_SCOPE_AGENT);
    u32 spins = 0;
    while (__hip_atomic_load(cnt, __ATOMIC_RELAXED, __HIP_MEMORY_SCOPE_AGENT) < (u32)NF) {
      __builtin_amdgcn_s_sleep(8);
      if (spin_fail(spins, tmo)) break;
    }
    __builtin_amdgcn_fence(__ATOMIC_ACQUIRE, "agent");
    wait_vm();
  }
  __syncthreads();
  STAMP(64, 3);
  {
    const int tm = f / 6, b3 = (f % 6) * 3, tg = wave & 3, kh = wave >> 2;
    const unsigned char* hb = ws + H_OFF + ((size_t)(tm * 8 + tg * 2) * 144 + kh * 72) * 1024 + lane * 16;
    const unsigned char* wb = ws + WDF_OFF + (size_t)(b3 * 144 + kh * 72) * 2048 + lane * 16;
    f32x4 acc[2][6];
#pragma unroll
    for (int u = 0; u < 2; ++u)
#pragma unroll
      for (int i = 0; i < 6; ++i) acc[u][i] = f32x4{0.f, 0.f, 0.f, 0.f};
    u32x4 hf[2][2][2], wf[2][2][6];
    auto ld = [&](int st, int t0) {
#pragma unroll
      for (int t = 0; t < 2; ++t) {
        hf[st][t][0] = *(const u32x4*)(hb + (t0 + t) * 1024); hf[st][t][1] = *(const u32x4*)(hb + (144 + t0 + t) * 1024);
#pragma unroll
        for (int i = 0; i < 6; ++i) wf[st][t][i] = *(const u32x4*)(wb + ((i >> 1) * 144 + t0 + t) * 2048 + (i & 1) * 1024);
      }
    };
    auto mm = [&](int st) {
#pragma unroll
      for (int t = 0; t < 2; ++t)
#pragma unroll
        for (int i = 0; i < 6; ++i) { acc[0][i] = mfma(wf[st][t][i], hf[st][t][0], acc[0][i]); acc[1][i] = mfma(wf[st][t][i], hf[st][t][1], acc[1][i]); }
    };
    ld(0, 0);
#pragma unroll 1
    for (int t0 = 0; t0 < 72; t0 += 4) {
      ld(1, t0 + 2);
      __builtin_amdgcn_sched_barrier(0);
      mm(0);
      __builtin_amdgcn_sched_barrier(0);
      ld(0, min(t0 + 4, 70));
      __builtin_amdgcn_sched_barrier(0);
      mm(1);
      __builtin_amdgcn_sched_barrier(0);
    }
    STAMP(64, 4);
    if (kh == 1) {
#pragma unroll
      for (int u = 0; u < 2; ++u)
#pragma unroll
        for (int i = 0; i < 6; ++i) *(f32x4*)(smem + ((tg * 12 + u * 6 + i) * 64 + lane) * 16) = acc[u][i];
    }
    const bool mine = tm * 4 < SPLIT;
    if (mine && wave == 0) {
      u32 spins = 0;
      while (!allok(__builtin_amdgcn_raw_buffer_load_b32(r, FLA_OFF + (tm * 4 + ((lane >> 1) & 3)) * 128 + ((f % 6) * 2 + (lane & 1)) * 4, 0, SC1))) {
        if (spin_fail(spins, tmo)) break;
        __builtin_amdgcn_s_sleep(16);
      }
    }
    __syncthreads();
    if (kh == 0) {
#pragma unroll
      for (int u = 0; u < 2; ++u)
#pragma unroll
        for (int b = 0; b < 3; ++b) {
          const f32x4 e0 = acc[u][2 * b] + *(const f32x4*)(smem + ((tg * 12 + u * 6 + 2 * b) * 64 + lane) * 16);
          const f32x4 e1 = acc[u][2 * b + 1] + *(const f32x4*)(smem + ((tg * 12 + u * 6 + 2 * b + 1) * 64 + lane) * 16);
          const u32x4 o = {pk(e0[0], e1[0]), pk(e0[1], e1[1]), pk(e0[2], e1[2]), pk(e0[3], e1[3])};
          const u32 eo = ((tm * 128 + tg * 32 + u * 16 + c16) * DM + (b3 + b) * 32 + g * 8) * 2;
          if (mine) {
            const u32x4 xv = *(const u32x4*)((const char*)a.x + eo), av = __builtin_amdgcn_raw_buffer_load_b128(r, AO_OFF + eo, 0, SC1);
            *(u32x4*)((char*)a.out + eo) = u32x4{fin(xv.x, av.x, o.x), fin(xv.y, av.y, o.y), fin(xv.z, av.z, o.z), fin(xv.w, av.w, o.w)};
          } else {
            __builtin_amdgcn_raw_buffer_store_b128(o, r, FO_OFF + eo, 0, SC1);
          }
        }
    }
    wait_vm();
    __syncthreads();
    if (!mine && tid == 0) __builtin_amdgcn_raw_buffer_store_b32(1u, r, FLF_OFF + f * 128, 0, SC1);
  }
}

__global__ void __launch_bounds__(512, 2) k2_main(Args a) {
  extern __shared__ __attribute__((aligned(16))) char smem[];
  if (threadIdx.x == 0) {
    u32* ctl = (u32*)(a.ws + CTL_OFF);
    const u32 x = __builtin_amdgcn_s_getreg((4 - 1) << 11 | 0 << 6 | 20) & 7u;
    const u32 rank = __hip_atomic_fetch_add(ctl + 256 + x * 32, 1u, __ATOMIC_RELAXED, __HIP_MEMORY_SCOPE_AGENT);
    u32 slot = rank * 8 + x;
    if (rank >= 32) {
      u32 k = __hip_atomic_fetch_add(ctl + 192, 1u, __ATOMIC_RELAXED, __HIP_MEMORY_SCOPE_AGENT), spins = 0;
      for (;;) {
        u32 tot = 0;
        for (int i = 0; i < 8; ++i) tot += __hip_atomic_load(ctl + 256 + i * 32, __ATOMIC_RELAXED, __HIP_MEMORY_SCOPE_AGENT);
        if (tot >= 256u || spin_fail(spins, ctl + 64)) break;
      }
      slot = 256;
      for (int i = 0; i < 8 && slot == 256; ++i) {
        const u32 c = __hip_atomic_load(ctl + 256 + i * 32, __ATOMIC_RELAXED, __HIP_MEMORY_SCOPE_AGENT), fr = c < 32u ? 32u - c : 0u;
        if (k < fr) slot = (c + k) * 8 + i; else k -= fr;
      }
    }
    *(u32*)smem = slot;
  }
  __syncthreads();
  const int b = __builtin_amdgcn_readfirstlane(*(const u32*)smem), rr = b & 7, q = b >> 3;
  __syncthreads();
  if (b >= 256) return;
  if (rr == 0) role_a(a, q, smem);
  else if (rr < 4 && q < 24) role_b(a, (rr - 1) * 24 + q, smem);
  else if (rr == 4) role_v(a, q, smem);
  else if (rr > 4) role_f(a, (rr - 5) * 32 + q, smem);
  else role_o(a, (rr - 1) * 8 + (q - 24), smem);
  DUMP();
}

extern "C" void kernel_launch(void* const* d_in, const int* in_sizes, int n_in, void* d_out, int out_size, void* d_ws,
                              size_t ws_size, hipStream_t stream) {
  Args a;
  a.x = (const u16*)d_in[0]; a.mem0 = (const u16*)d_in[1]; a.wq = (const u16*)d_in[2]; a.wk = (const u16*)d_in[3];
  a.wv = (const u16*)d_in[4]; a.wo = (const u16*)d_in[5]; a.wg = (const u16*)d_in[8]; a.wu = (const u16*)d_in[9];
  a.wd = (const u16*)d_in[10];
  a.ws = (unsigned char*)(((uintptr_t)d_ws + 65535) & ~(uintptr_t)65535);
  a.out = (u16*)d_out;
  (void)hipFuncSetAttribute((const void*)k2_main, hipFuncAttributeMaxDynamicSharedMemorySize, LDS_BYTES);
  k1_prep<<<K1_GRID, 256, 0, stream>>>(a);
  k2_main<<<256, 512, LDS_BYTES, stream>>>(a);
}
```

```cpp
#include <hip/hip_runtime.h>
#include <hip/hip_bf16.h>

typedef unsigned u32;
typedef unsigned long long u64;
typedef unsigned short u16;
typedef __attribute__((ext_vector_type(8))) __bf16 bf16x8;
typedef __attribute__((ext_vector_type(2))) __bf16 bf16x2;
typedef __attribute__((ext_vector_type(4))) float f32x4;
typedef __attribute__((ext_vector_type(2))) float f32x2;
typedef __attribute__((ext_vector_type(4))) unsigned u32x4;
typedef __attribute__((ext_vector_type(2))) unsigned u32x2;

constexpr int S_ = 2048, DM = 576, NH = 32, DH = 128, FF = 4608, NSEG = 64, KS = 18;
constexpr int NB = 72, NF = 96;
constexpr u32 HO_OFF = 0;
constexpr u32 PT_OFF = HO_OFF + NSEG * 65536;
constexpr u32 PT_STEP = 4 * 20480;
constexpr u32 FLG_OFF = PT_OFF + NSEG * PT_STEP;
constexpr u32 FLM_OFF = FLG_OFF + NSEG * 128;
constexpr u32 FLQ_OFF = FLM_OFF + NSEG * 3072;
constexpr u32 FLA_OFF = FLQ_OFF + NSEG * 128;
constexpr u32 FLF_OFF = FLA_OFF + NSEG * 128;
constexpr int SPLIT = 56;
constexpr u32 FLP_OFF = FLF_OFF + NF * 128;
constexpr u32 POISON_BYTES = FLP_OFF + 256;
constexpr u32 CTL_OFF = (POISON_BYTES + 4095) & ~4095u;
constexpr u32 M0_OFF = CTL_OFF + 4096;
constexpr u32 XF_OFF = M0_OFF + 12288;
constexpr u32 XF_SEG = 2 * KS * 1024;
constexpr u32 WVO_OFF = XF_OFF + NSEG * XF_SEG;
constexpr u32 WVO_J = 16 * KS * 1024;
constexpr u32 UX_OFF = WVO_OFF + NB * WVO_J;
constexpr u32 UM_OFF = UX_OFF + NSEG * NB * 16384;
constexpr u32 QX_OFF = UM_OFF + NSEG * NB * 4096;
constexpr u32 H_OFF = QX_OFF + NSEG * NH * 16384;
constexpr u32 WDF_OFF = H_OFF + 128 * 144 * 1024;
constexpr u32 FO_OFF = WDF_OFF + 9 * 144 * 4096;
constexpr u32 AO_OFF = FO_OFF + S_ * DM * 2;
constexpr u32 WS_END = AO_OFF + S_ * DM * 2;
constexpr u32 POISON = 0xFFFFFFFFu;
constexpr int SC1 = 16;
constexpr u32 SPIN_CAP = 400000u;
constexpr int LDS_BYTES = 160 * 1024;

__device__ __forceinline__ u32 pk(float lo, float hi) {
  f32x2 v = {lo, hi};
  return __builtin_bit_cast(u32, __builtin_convertvector(v, bf16x2));
}
__device__ __forceinline__ u32 pks(float lo, float hi) { u32 r = pk(lo, hi); return r == POISON ? 0x7FC07FC0u : r; }
__device__ __forceinline__ float bfr(float x) { return (float)(__bf16)x; }
__device__ __forceinline__ float bflo(u32 w) { return __uint_as_float(w << 16); }
__device__ __forceinline__ float bfhi(u32 w) { return __uint_as_float(w & 0xFFFF0000u); }
__device__ __forceinline__ u32 fin(u32 x, u32 p, u32 f) {
  return pk(bfr(bflo(x) + bflo(p)) + bflo(f), bfr(bfhi(x) + bfhi(p)) + bfhi(f));
}
__device__ __forceinline__ f32x4 mfma(u32x4 a, u32x4 b, f32x4 c) {
  return __builtin_amdgcn_mfma_f32_16x16x32_bf16(__builtin_bit_cast(bf16x8, a), __builtin_bit_cast(bf16x8, b), c, 0, 0, 0);
}
__device__ __forceinline__ u32 plo(u32 a, u32 b) { return __builtin_amdgcn_perm(b, a, 0x05040100u); }
__device__ __forceinline__ u32 phi(u32 a, u32 b) { return __builtin_amdgcn_perm(b, a, 0x07060302u); }
__device__ __forceinline__ float vmax(float a, float b) { float r; asm("v_max_f32 %0, %1, %2" : "=v"(r) : "v"(a), "v"(b)); return r; }
__device__ __forceinline__ float vmax3(float a, float b, float c) { float r; asm("v_max3_f32 %0, %1, %2, %3" : "=v"(r) : "v"(a), "v"(b), "v"(c)); return r; }
__device__ __forceinline__ float xmax(float x) {
  auto s = __builtin_amdgcn_permlane16_swap(__float_as_uint(x), __float_as_uint(x), false, false);
  x = vmax(__uint_as_float(s[0]), __uint_as_float(s[1]));
  auto t = __builtin_amdgcn_permlane32_swap(__float_as_uint(x), __float_as_uint(x), false, false);
  return vmax(__uint_as_float(t[0]), __uint_as_float(t[1]));
}
__device__ __forceinline__ float xsum(float x) {
  auto s = __builtin_amdgcn_permlane16_swap(__float_as_uint(x), __float_as_uint(x), false, false);
  x = __uint_as_float(s[0]) + __uint_as_float(s[1]);
  auto t = __builtin_amdgcn_permlane32_swap(__float_as_uint(x), __float_as_uint(x), false, false);
  return __uint_as_float(t[0]) + __uint_as_float(t[1]);
}
__device__ __forceinline__ float ror8(float x) {
  return __builtin_bit_cast(float, __builtin_amdgcn_mov_dpp(__builtin_bit_cast(int, x), 0x128, 0xf, 0xf, true));
}
__device__ __forceinline__ u32 mx(u32 a, u32 b) { return a > b ? a : b; }
__device__ __forceinline__ u32 mx4(u32x4 v) { return mx(mx(v.x, v.y), mx(v.z, v.w)); }
__device__ __forceinline__ u32 mx2(u32x2 v) { return mx(v.x, v.y); }
__device__ __forceinline__ bool allok(u32 m) { return __all(m != POISON); }
__device__ __forceinline__ void glds16(const void* g, u32 lds) {
  u32 keep;
  asm volatile("s_mov_b32 %0, m0\n\ts_mov_b32 m0, %2\n\ts_nop 0\n\tglobal_load_lds_dwordx4 %1, off\n\ts_mov_b32 m0, %0"
               : "=&s"(keep) : "v"(g), "s"(lds) : "memory");
}
__device__ __forceinline__ void wait_vm() { asm volatile("s_waitcnt vmcnt(0)" ::: "memory"); }
__device__ __forceinline__ void bar() {
  asm volatile("s_waitcnt lgkmcnt(0)" ::: "memory");
  __builtin_amdgcn_s_barrier();
  asm volatile("" ::: "memory");
}
__device__ __forceinline__ bool spin_fail(u32& spins, u32* tmo) {
  ++spins;
  if (spins > SPIN_CAP) { __hip_atomic_store(tmo, 1u, __ATOMIC_RELAXED, __HIP_MEMORY_SCOPE_AGENT); return true; }
  if ((spins & 1023u) == 0u && __hip_atomic_load(tmo, __ATOMIC_RELAXED, __HIP_MEMORY_SCOPE_AGENT) != 0u) return true;
  return false;
}
__device__ __forceinline__ u32 homap(u32 lo, u32 flip) { return ((lo & ~8191u) << 1) + (lo & 8191u) + flip; }
constexpr u32 HO_PM = 16384, HO_M = 20480;

#define STAMP(step, k) do {} while (0)
#define STAMPC(step, k) do {} while (0)
#define DUMP() do {} while (0)
struct Args {
  const u16* x; const u16* mem0; const u16* wq; const u16* wk; const u16* wv; const u16* wo;
  const u16* wg; const u16* wu; const u16* wd; unsigned char* ws; u16* out;
};

constexpr int K1_WVO = 2592, K1_LN = 256, K1_LNB = K1_LN / 4, K1_JOBS = K1_WVO + K1_LN, K1_GRID = K1_JOBS / 4 + 8;
__global__ void __launch_bounds__(256) k1_prep(Args a) {
  const int lane = threadIdx.x & 63, wave = __builtin_amdgcn_readfirstlane(threadIdx.x >> 6);
  const int b = blockIdx.x;
  unsigned char* ws = a.ws;
  const int c16 = lane & 15, g = lane >> 4;
  if (b == K1_GRID - 1) {
    for (int i = threadIdx.x; i < 1024; i += 256) ((u32*)(ws + CTL_OFF))[i] = 0u;
    for (int i = threadIdx.x; i < 576; i += 256) {
      u32x4 v = *(const u32x4*)(a.mem0 + (i & 7) * DM + (i >> 3) * 8);
      if (v.x == POISON) v.x = 0x7FC07FC0u; if (v.y == POISON) v.y = 0x7FC07FC0u;
      if (v.z == POISON) v.z = 0x7FC07FC0u; if (v.w == POISON) v.w = 0x7FC07FC0u;
      *(u32x4*)(ws + M0_OFF + i * 16) = v;
    }
    return;
  }
  if (b >= K1_JOBS / 4) return;
  const int job = b * 4 + wave;
  auto poison = [&]() {
    const u32x4 pv = {POISON, POISON, POISON, POISON};
    for (u32 o = (job * 64 + lane) * 16; o < POISON_BYTES; o += K1_JOBS * 64 * 16) *(u32x4*)(ws + o) = pv;
  };
  if (b >= K1_LNB) {
    const int jx = ((b - K1_LNB) >> 3) * 4 + wave, h = (b & 7) * 4 + jx / 81, kp = (jx / 9) % 9, bb = jx % 9;
    u32x2 rw[4][8];
    u32x4 af[2][2][4];
#pragma unroll
    for (int es = 0; es < 4; ++es)
#pragma unroll
      for (int jj = 0; jj < 8; ++jj)
        rw[es][jj] = *(const u32x2*)(a.wo + (size_t)(h * DH + es * 32 + g * 8 + jj) * DM + bb * 64 + c16 * 4);
#pragma unroll
    for (int i = 0; i < 16; ++i) {
      const int k = (kp * 2 + (i >> 3)) * 32 + (c16 >> 2) * 8 + (c16 & 3) + ((i >> 2) & 1) * 4;
      af[i >> 3][(i >> 2) & 1][i & 3] = *(const u32x4*)(a.wv + (size_t)k * (NH * DH) + h * DH + (i & 3) * 32 + g * 8);
    }
    poison();
    __builtin_amdgcn_sched_barrier(0);
    u32x4 bf[4][4];
#pragma unroll
    for (int es = 0; es < 4; ++es)
#pragma unroll
      for (int p = 0; p < 4; ++p) {
        bf[es][0][p] = plo(rw[es][2 * p].x, rw[es][2 * p + 1].x); bf[es][1][p] = phi(rw[es][2 * p].x, rw[es][2 * p + 1].x);
        bf[es][2][p] = plo(rw[es][2 * p].y, rw[es][2 * p + 1].y); bf[es][3][p] = phi(rw[es][2 * p].y, rw[es][2 * p + 1].y);
      }
#pragma unroll
    for (int tt = 0; tt < 2; ++tt) {
      const int t = kp * 2 + tt;
      f32x4 acc[2][4];
#pragma unroll
      for (int rt = 0; rt < 2; ++rt)
#pragma unroll
        for (int i = 0; i < 4; ++i) {
          acc[rt][i] = f32x4{0.f, 0.f, 0.f, 0.f};
#pragma unroll
          for (int es = 0; es < 4; ++es) acc[rt][i] = mfma(af[tt][rt][es], bf[es][i], acc[rt][i]);
        }
      const int j = bb * 8 + (c16 >> 1);
      unsigned char* dst = ws + WVO_OFF + (size_t)j * WVO_J + ((h >> 1) * KS + t) * 1024 + (g * 16 + (h & 1) * 8 + (c16 & 1) * 4) * 16;
#pragma unroll
      for (int i = 0; i < 4; ++i) {
        u32x4 v = {pk(acc[0][i][0], acc[0][i][1]), pk(acc[0][i][2], acc[0][i][3]), pk(acc[1][i][0], acc[1][i][1]), pk(acc[1][i][2], acc[1][i][3])};
        *(u32x4*)(dst + i * 16) = v;
      }
    }
  } else {
    u32x4 vv[2][5];
#pragma unroll
    for (int rr = 0; rr < 2; ++rr)
#pragma unroll
    for (int i = 0; i < 5; ++i) {
      vv[rr][i] = u32x4{0, 0, 0, 0};
      if (i < 4 || c16 < 8) vv[rr][i] = *(const u32x4*)(a.x + (size_t)(job * 8 + rr * 4 + g) * DM + (i * 16 + c16) * 8);
    }
    poison();
#pragma unroll
    for (int rr = 0; rr < 2; ++rr) {
    const int row = job * 8 + rr * 4 + g;
    const u32x4 (&v)[5] = vv[rr];
    float sm = 0.f;
#pragma unroll
    for (int i = 0; i < 20; ++i) sm += bflo(v[i >> 2][i & 3]) + bfhi(v[i >> 2][i & 3]);
#pragma unroll
    for (int o = 1; o < 16; o <<= 1) sm += __shfl_xor(sm, o);
    const float mean = sm * (1.f / DM);
    float q = 0.f;
#pragma unroll
    for (int i = 0; i < 20; ++i) {
      const float d0 = bflo(v[i >> 2][i & 3]) - mean, d1 = bfhi(v[i >> 2][i & 3]) - mean;
      q += (i < 16 || c16 < 8) ? d0 * d0 + d1 * d1 : 0.f;
    }
#pragma unroll
    for (int o = 1; o < 16; o <<= 1) q += __shfl_xor(q, o);
    const float mb = bfr(mean), den = bfr(sqrtf(bfr(bfr(q * (1.f / DM)) + 1e-5f)));
#pragma unroll
    for (int i = 0; i < 5; ++i) {
      u32x4 o;
#pragma unroll
      for (int k = 0; k < 4; ++k) o[k] = pk(bfr(bflo(v[i][k]) - mb) / den, bfr(bfhi(v[i][k]) - mb) / den);
      const int ch = i * 16 + c16;
      if (i < 4 || c16 < 8) *(u32x4*)(ws + XF_OFF + ((row >> 4) * KS + (ch >> 2)) * 1024 + ((ch & 3) * 16 + (row & 15)) * 16) = o;
    }
    }
  }
}

constexpr u32 L_X = 0;
constexpr u32 L_M = 2 * XF_SEG;
constexpr u32 L_QM = L_M + 9216;
constexpr u32 L_QX = L_QM + 8192;

template <bool WA, int ST>
__device__ __forceinline__ void proj2(const u32x4 (&W0)[KS], const u32x4 (&W1)[KS], const char* xb, f32x4& c0, f32x4& c1) {
  u32x4 xf[KS];
#pragma unroll
  for (int t = 0; t < KS; ++t) xf[t] = *(const u32x4*)(xb + t * ST);
  c0 = f32x4{0.f, 0.f, 0.f, 0.f}; c1 = c0;
#pragma unroll
  for (int t = 0; t < KS; ++t) {
    c0 = WA ? mfma(W0[t], xf[t], c0) : mfma(xf[t], W0[t], c0);
    c1 = WA ? mfma(W1[t], xf[t], c1) : mfma(xf[t], W1[t], c1);
  }
  __builtin_amdgcn_sched_group_barrier(0x100, 6, 0);
#pragma unroll
  for (int t = 0; t < KS - 6; ++t) { __builtin_amdgcn_sched_group_barrier(0x008, 2, 0); __builtin_amdgcn_sched_group_barrier(0x100, 1, 0); }
  __builtin_amdgcn_sched_group_barrier(0x008, 12, 0);
}
__device__ __forceinline__ void stage_x(const unsigned char* ws, int seg, int wave, int lane) {
  const unsigned char* src = ws + XF_OFF + seg * XF_SEG + lane * 16;
  const u32 dst = L_X + (seg & 1) * XF_SEG;
#pragma unroll
  for (int i = 0; i < 5; ++i) {
    const int p = i * 8 + wave;
    if (p < 36) glds16(src + p * 1024, dst + p * 1024);
  }
}
__device__ __forceinline__ u32 get_flips(__amdgpu_buffer_rsrc_t r, int lane, u32* tmo) {
  u32 f, spins = 0;
  for (;;) {
    f = __builtin_amdgcn_raw_buffer_load_b32(r, FLP_OFF + lane * 4, 0, SC1);
    if (allok(f)) break;
    if (spin_fail(spins, tmo)) break;
    __builtin_amdgcn_s_sleep(8);
  }
  return f & 8192u;
}
__device__ __forceinline__ void gather_m(__amdgpu_buffer_rsrc_t r, u32 base, u32 flip, bool first, char* smem, int wave, int lane, u32* tmo) {
  const u32 l0 = (wave * 64 + lane) * 16, l1 = (512 + wave * 8 + (lane & 7)) * 16;
  const u32 o0 = first ? M0_OFF + l0 : base + homap(HO_M + l0, flip), o1 = first ? M0_OFF + l1 : base + homap(HO_M + l1, flip);
  u32 spins = 0;
  u32x4 v, w;
  for (;;) {
    v = __builtin_amdgcn_raw_buffer_load_b128(r, o0, 0, SC1);
    w = __builtin_amdgcn_raw_buffer_load_b128(r, o1, 0, SC1);
    if (allok(mx(mx4(v), mx4(w)))) break;
    if (spin_fail(spins, tmo)) break;
  }
  *(u32x4*)(smem + L_M + l0) = v;
  if (lane < 8) *(u32x4*)(smem + L_M + l1) = w;
  wait_vm();
}

__device__ __forceinline__ void load_wqk(const Args& a, int h, u32x4 (&W)[2][KS], char* smem) {
  const int tid = threadIdx.x, lane = tid & 63, wave = __builtin_amdgcn_readfirstlane(tid >> 6);
  const int c16 = lane & 15, g = lane >> 4;
  {
    u32x4 st[18];
    auto ldw = [&](int hf) {
#pragma unroll
      for (int i = 0; i < 18; ++i) {
        const int pc = i * 512 + tid;
        st[i] = *(const u32x4*)(((pc & 16) ? a.wk : a.wq) + (size_t)(hf * 288 + (pc >> 5)) * (NH * DH) + h * DH + (pc & 15) * 8);
      }
    };
    ldw(0);
#pragma unroll
    for (int hf = 0; hf < 2; ++hf) {
#pragma unroll
      for (int i = 0; i < 18; ++i) { const int pc = i * 512 + tid; *(u32x4*)(smem + (pc >> 5) * 528 + (pc & 31) * 16) = st[i]; }
      if (hf == 0) ldw(1);
      bar();
      const char* wl = smem + g * 8 * 528 + wave * 64 + c16 * 4;
#pragma unroll
      for (int t = 0; t < 9; ++t) {
#pragma unroll
        for (int p = 0; p < 4; ++p) {
          const u32 d0 = *(const u32*)(wl + (t * 32 + 2 * p) * 528), d1 = *(const u32*)(wl + (t * 32 + 2 * p + 1) * 528);
          W[0][hf * 9 + t][p] = plo(d0, d1); W[1][hf * 9 + t][p] = phi(d0, d1);
        }
        asm volatile("" : "+v"(W[0][hf * 9 + t]), "+v"(W[1][hf * 9 + t]) :: "memory");
      }
      bar();
    }
  }
}

__device__ __forceinline__ void role_a(const Args& a, int h, char* smem) {
  const int tid = threadIdx.x, lane = tid & 63, wave = __builtin_amdgcn_readfirstlane(tid >> 6);
  const int c16 = lane & 15, g = lane >> 4;
  unsigned char* ws = a.ws;
  u32* tmo = (u32*)(ws + CTL_OFF) + 64;
  __amdgpu_buffer_rsrc_t r = __builtin_amdgcn_make_buffer_rsrc(ws, 0, WS_END, 0x00020000);
  STAMP(64, 0);
  u32x4 W[2][KS];
  load_wqk(a, h, W, smem);
  const u32 flips = get_flips(r, lane, tmo);
  const u32 lx = (((h >> 1) * 4 + g) * 16 + (h & 1) * 8 + (lane & 7)) * 16, lm = HO_PM + h * 128 + g * 64 + (lane & 7) * 8;
  STAMP(64, 1); STAMPC(65, 0); STAMP(65, 1);
  u32x4 kf[8];
  u32 fl = POISON;
  auto q_load = [&](int seg) {
    const u32 fo = FLQ_OFF + seg * 128 + h * 4;
    u32 spins = 0;
    while (!allok(fl)) {
      fl = __builtin_amdgcn_raw_buffer_load_b32(r, fo, 0, SC1);
      if (spin_fail(spins, tmo)) break;
    }
    asm volatile("" ::: "memory");
    const u32 qo = QX_OFF + ((seg * NH + h) * 16 + (wave - 1) * 4) * 1024 + lane * 16;
#pragma unroll
    for (int w = 0; w < 4; ++w) kf[w] = __builtin_amdgcn_raw_buffer_load_b128(r, qo + w * 1024, 0, SC1);
    if (wave == 3) {
#pragma unroll
      for (int w = 4; w < 8; ++w) kf[w] = __builtin_amdgcn_raw_buffer_load_b128(r, qo + w * 1024, 0, SC1);
#pragma unroll
      for (int w = 0; w < 8; ++w) *(u32x4*)(smem + L_QX + (seg & 1) * 16384 + 8192 + w * 1024 + lane * 16) = kf[w];
    }
    fl = seg + 1 < NSEG ? __builtin_amdgcn_raw_buffer_load_b32(r, fo + 128, 0, SC1) : 0u;
  };
  if (wave >= 1 && wave <= 3) q_load(0);
  for (int s = 0; s < NSEG; ++s) {
    const u32 flip = __builtin_amdgcn_readlane(flips, s), base = HO_OFF + s * 65536;
    STAMP(s, 0);
    gather_m(r, base, flip, s == 0, smem, wave, lane, tmo);
    STAMP(s, 1);
    bar();
    STAMP(s, 2);
    {
      f32x4 c0, c1;
      proj2<true, 512>(W[0], W[1], smem + L_M + g * 128 + (lane & 7) * 16, c0, c1);
      u32x4 o = {pk(c0[0], c1[0]), pk(c0[1], c1[1]), pk(c0[2], c1[2]), pk(c0[3], c1[3])};
      *(u32x4*)(smem + L_QM + wave * 1024 + lane * 16) = o;
    }
    if (wave == 0) {
      __builtin_amdgcn_s_setprio(3);
#pragma unroll
      for (int w = 0; w < 8; ++w) kf[w] = *(const u32x4*)(smem + L_QX + (s & 1) * 16384 + 8192 + lane * 16 + w * 1024);
    }
    bar();
    STAMP(s, 3);
    const float C2 = 0.08838834764831845f * 1.4426950408889634f;
    if (wave == 0) {
      if (s < NSEG - 1) {
        const char* qb = smem + L_QM + lane * 16;
        f32x4 s0 = {0.f, 0.f, 0.f, 0.f}, s1 = s0, s2 = s0;
#pragma unroll
        for (int w = 0; w < 4; ++w) {
          const u32x4 qf = *(const u32x4*)(qb + w * 1024);
          s2 = mfma(*(const u32x4*)(qb + 4096 + w * 1024), qf, s2);
          s0 = mfma(kf[w], qf, s0);
          s1 = mfma(kf[4 + w], qf, s1);
        }
        const bool hi = c16 >= 8;
        float v[6];
#pragma unroll
        for (int i = 0; i < 4; ++i) v[i] = hi ? s1[i] : s0[i];
        v[4] = g < 2 ? (hi ? s2[2] : s2[0]) : -INFINITY;
        v[5] = g < 2 ? (hi ? s2[3] : s2[1]) : -INFINITY;
        float m = vmax(vmax3(v[0], v[1], v[2]), vmax3(v[3], v[4], v[5]));
        m = xmax(m);
        m = vmax(m, ror8(m)) * C2;
#pragma unroll
        for (int i = 0; i < 6; ++i) v[i] = __builtin_amdgcn_exp2f(__builtin_fmaf(v[i], C2, -m));
        float sum = ((v[0] + v[1]) + (v[2] + v[3])) + (v[4] + v[5]);
        sum += ror8(sum);
        const float inv = __builtin_amdgcn_rcpf(xsum(sum));
        const u32x2 px = {pk(v[0] * inv, v[1] * inv) & 0x7FFF7FFFu, pk(v[2] * inv, v[3] * inv) & 0x7FFF7FFFu};
        const u32 pm = pk(v[4] * inv, v[5] * inv) & 0x7FFF7FFFu;
        __builtin_amdgcn_raw_buffer_store_b64(px, r, base + homap(lx + (c16 >> 3) * 8, flip), 0, SC1);
        if (g < 2) __builtin_amdgcn_raw_buffer_store_b32(pm, r, base + homap(lm + (c16 >> 3) * 4, flip), 0, SC1);
      }
      __builtin_amdgcn_s_setprio(0);
    } else if (wave < 3) {
      const char* qx = smem + L_QX + (s & 1) * 16384 + lane * 16;
      f32x4 s0 = {0.f, 0.f, 0.f, 0.f}, s1 = s0, s2 = s0;
#pragma unroll
      for (int w = 0; w < 4; ++w) {
        s2 = mfma(*(const u32x4*)(smem + L_QM + 4096 + lane * 16 + w * 1024), kf[w], s2);
        s0 = mfma(*(const u32x4*)(qx + 8192 + w * 1024), kf[w], s0);
        s1 = mfma(*(const u32x4*)(qx + 12288 + w * 1024), kf[w], s1);
      }
      const int qi = (wave - 1) * 16 + c16;
      const float NEG = -1.0e9f * 1.4426950408889634f;
      float v[12];
#pragma unroll
      for (int i = 0; i < 4; ++i) {
        v[i] = (4 * g + i <= qi) ? s0[i] * C2 : NEG;
        v[4 + i] = (16 + 4 * g + i <= qi) ? s1[i] * C2 : NEG;
        v[8 + i] = g < 2 ? s2[i] * C2 : -INFINITY;
      }
      float m = v[0];
#pragma unroll
      for (int i = 1; i < 12; ++i) m = fmaxf(m, v[i]);
      m = xmax(m);
      float sum = 0.f;
#pragma unroll
      for (int i = 0; i < 12; ++i) { v[i] = __builtin_amdgcn_exp2f(v[i] - m); sum += v[i]; }
      const float inv = __builtin_amdgcn_rcpf(xsum(sum));
      u32x4 px; u32x2 pm;
#pragma unroll
      for (int i = 0; i < 4; ++i) px[i] = pk(v[2 * i] * inv, v[2 * i + 1] * inv) & 0x7FFF7FFFu;
#pragma unroll
      for (int i = 0; i < 2; ++i) pm[i] = pk(v[8 + 2 * i] * inv, v[9 + 2 * i] * inv) & 0x7FFF7FFFu;
      const u32 tb = PT_OFF + s * PT_STEP + ((wave - 1) * 2 + (c16 >> 3)) * 20480;
      __builtin_amdgcn_raw_buffer_store_b128(px, r, tb + lx, 0, SC1);
      if (g < 2) __builtin_amdgcn_raw_buffer_store_b64(pm, r, tb + lm, 0, SC1);
    }
    STAMP(s, 4);
    if (wave >= 1 && wave <= 3 && s + 1 < NSEG) q_load(s + 1);
    bar();
  }
  STAMPC(65, 2); STAMP(65, 3);
}

__device__ __forceinline__ void role_b(const Args& a, int j, char* smem) {
  const int tid = threadIdx.x, lane = tid & 63, wave = __builtin_amdgcn_readfirstlane(tid >> 6);
  const int c16 = lane & 15, g = lane >> 4;
  unsigned char* ws = a.ws;
  u32* tmo = (u32*)(ws + CTL_OFF) + 64;
  __amdgpu_buffer_rsrc_t r = __builtin_amdgcn_make_buffer_rsrc(ws, 0, WS_END, 0x00020000);
  STAMP(64, 0);
  u32x4 W[2][KS];
  {
    const unsigned char* wsrc = ws + WVO_OFF + (size_t)j * WVO_J + wave * 2 * KS * 1024 + lane * 16;
#pragma unroll
    for (int tt = 0; tt < 2; ++tt)
#pragma unroll
      for (int t = 0; t < KS; ++t) W[tt][t] = *(const u32x4*)(wsrc + (tt * KS + t) * 1024);
  }
  wait_vm();
  bar();
  const u32 flips = get_flips(r, lane, tmo);
  STAMP(64, 1);
  u32x4 nx0, nx1;
  u32 fl = POISON;
  auto ux_load = [&](int seg) {
    const u32 fo = FLG_OFF + seg * 128 + wave * 16 + (lane & 3) * 4;
    u32 spins = 0;
    while (!allok(fl)) {
      fl = __builtin_amdgcn_raw_buffer_load_b32(r, fo, 0, SC1);
      if (spin_fail(spins, tmo)) break;
    }
    asm volatile("" ::: "memory");
    const u32 uo = UX_OFF + ((seg * NB + j) * 16 + wave * 2) * 1024 + lane * 16;
    nx0 = __builtin_amdgcn_raw_buffer_load_b128(r, uo, 0, SC1);
    nx1 = __builtin_amdgcn_raw_buffer_load_b128(r, uo + 1024, 0, SC1);
    fl = seg + 1 < NSEG ? __builtin_amdgcn_raw_buffer_load_b32(r, fo + 128, 0, SC1) : 0u;
  };
  const u32 lx = wave * 2048 + lane * 16;
  const u32 lm = HO_PM + (wave * 4 + (c16 >> 3)) * 128 + (g & 1) * 64 + (lane & 7) * 8;
  const bool diag = (c16 >> 3) == (g >> 1);
  const u32 redo = ((wave * 2 + (g >> 1)) * 64 + (lane & 7) * 8 + (g & 1) * 4) * 4;
  for (int s = 0; s < NSEG; ++s) {
    const u32 flip = __builtin_amdgcn_readlane(flips, s), base = HO_OFF + s * 65536;
    STAMP(s, 0);
    gather_m(r, base, flip, s == 0, smem, wave, lane, tmo);
    bar();
    STAMP(s, 1);
    u32x4 um;
    {
      f32x4 c0, c1;
      proj2<false, 512>(W[0], W[1], smem + L_M + g * 128 + (lane & 7) * 16, c0, c1);
      um = u32x4{pk(c0[0], c0[1]), pk(c0[2], c0[3]), pk(c1[0], c1[1]), pk(c1[2], c1[3])};
      if (g < 2) __builtin_amdgcn_raw_buffer_store_b128(um, r, UM_OFF + ((s * NB + j) * 8 + wave) * 512 + lane * 16, 0, SC1);
      else um = u32x4{0, 0, 0, 0};
    }
    if (s + 1 < NSEG) {
      if (s == 0) ux_load(0);
      STAMP(s, 2);
      const u32 o0 = base + homap(lx, flip), o1 = o0 + 1024, o2 = base + homap(lm, flip), o3 = o2 + 256;
      u32x4 p0, p1; u32x2 q0, q1;
      u32 spins = 0;
      for (;;) {
        p0 = __builtin_amdgcn_raw_buffer_load_b128(r, o0, 0, SC1);
        p1 = __builtin_amdgcn_raw_buffer_load_b128(r, o1, 0, SC1);
        q0 = __builtin_amdgcn_raw_buffer_load_b64(r, o2, 0, SC1);
        q1 = __builtin_amdgcn_raw_buffer_load_b64(r, o3, 0, SC1);
        if (allok(mx(mx(mx4(p0), mx4(p1)), mx(mx2(q0), mx2(q1))))) break;
        if (spin_fail(spins, tmo)) break;
      }
      STAMP(s, 3);
      f32x4 d = {0.f, 0.f, 0.f, 0.f}, e = d;
      const u32x4 pmv = {q0.x, q0.y, q1.x, q1.y};
      d = mfma(nx0, p0, d); e = mfma(nx1, p1, e); d = mfma(um, pmv, d);
      d += e;
      if (diag) *(f32x4*)(smem + L_QM + redo) = d;
    }
    wait_vm();
    bar();
    if (wave == 0 && s + 1 < NSEG) {
      const char* rp = smem + L_QM + (lane >> 5) * 2048 + (lane & 31) * 8;
      f32x2 acc = *(const f32x2*)rp;
#pragma unroll
      for (int p = 1; p < 8; ++p) acc += *(const f32x2*)(rp + p * 256);
      auto t0 = __builtin_amdgcn_permlane32_swap(__float_as_uint(acc.x), __float_as_uint(acc.x), false, false);
      auto t1 = __builtin_amdgcn_permlane32_swap(__float_as_uint(acc.y), __float_as_uint(acc.y), false, false);
      const u32 o = pks(__uint_as_float(t0[0]) + __uint_as_float(t0[1]), __uint_as_float(t1[0]) + __uint_as_float(t1[1]));
      const u32 nflip = __builtin_amdgcn_readlane(flips, s + 1);
      if (lane < 32) __builtin_amdgcn_raw_buffer_store_b32(o, r, base + 65536 + homap(HO_M + j * 128 + lane * 4, nflip), 0, SC1);
    }
    STAMP(s, 4);
    if (tid == 64) __builtin_amdgcn_raw_buffer_store_b32(1u, r, FLM_OFF + s * 3072 + (j / 6) * 128 + (j % 6) * 4, 0, SC1);
    if (s + 2 < NSEG) ux_load(s + 1);
    STAMP(s, 5);
  }
  STAMP(65, 0);
}

__device__ __forceinline__ void role_o(const Args& a, int o24, char* smem) {
  const int tid = threadIdx.x, lane = tid & 63, wave = __builtin_amdgcn_readfirstlane(tid >> 6);
  const int c16 = lane & 15, g = lane >> 4, o = o24 >> 1;
  unsigned char* ws = a.ws;
  u32* tmo = (u32*)(ws + CTL_OFF) + 64;
  __amdgpu_buffer_rsrc_t r = __builtin_amdgcn_make_buffer_rsrc(ws, 0, WS_END, 0x00020000);
  STAMP(64, 0);
  if (wave < 3 && o24 * 3 + wave < NSEG) {
    const int blk = o24 * 3 + wave;
    u32 best[2] = {~0u, ~0u};
    for (int rep = 0; rep < 4; ++rep)
      for (int f = 0; f < 2; ++f) {
        u32* p = (u32*)(ws + HO_OFF + blk * 65536 + homap(32768 - 128 * (rep + 1), f * 8192));
        __hip_atomic_store(p, POISON, __ATOMIC_RELAXED, __HIP_MEMORY_SCOPE_AGENT);
        wait_vm();
        u64 t0, t1;
        asm volatile("s_memtime %0\n\ts_waitcnt lgkmcnt(0)" : "=s"(t0) :: "memory");
        u32 v = __hip_atomic_load(p, __ATOMIC_RELAXED, __HIP_MEMORY_SCOPE_AGENT);
        asm volatile("s_waitcnt vmcnt(0)" : "+v"(v) :: "memory");
        asm volatile("s_memtime %0\n\ts_waitcnt lgkmcnt(0)" : "=s"(t1) :: "memory");
        const u32 d = (u32)(t1 - t0);
        best[f] = d < best[f] ? d : best[f];
      }
    if (lane == 0) __builtin_amdgcn_raw_buffer_store_b32(best[1] < best[0] ? 8192u : 0u, r, FLP_OFF + blk * 4, 0, SC1);
  }
  STAMP(64, 1);
  const u32 lx = wave * 2048 + lane * 16;
  const u32 lm = HO_PM + (wave * 4 + (c16 >> 3)) * 128 + (g & 1) * 64 + (lane & 7) * 8;
  const bool diag = (c16 >> 3) == (g >> 1);
  const u32 redo = ((wave * 2 + (g >> 1)) * 64 + (lane & 7) * 8 + (g & 1) * 4) * 4;
  u32x4 x0[6], x1[6], um[6];
  auto ux_load = [&](int s) {
    u32 spins = 0;
    while (!allok(__builtin_amdgcn_raw_buffer_load_b32(r, FLG_OFF + s * 128 + wave * 16 + (lane & 3) * 4, 0, SC1))) {
      if (spin_fail(spins, tmo)) break;
      __builtin_amdgcn_s_sleep(16);
    }
    asm volatile("" ::: "memory");
#pragma unroll
    for (int c = 0; c < 6; ++c) {
      const u32 uo = UX_OFF + ((s * NB + o * 6 + c) * 16 + wave * 2) * 1024 + lane * 16;
      x0[c] = __builtin_amdgcn_raw_buffer_load_b128(r, uo, 0, SC1);
      x1[c] = __builtin_amdgcn_raw_buffer_load_b128(r, uo + 1024, 0, SC1);
    }
  };
  const u32 e0 = (((tid & 63) >> 1) * DM + (o * 6 + (tid >> 6)) * 8 + (tid & 1) * 4) * 2;
  u32x2 xv = {0, 0}, fv = {0, 0};
  ux_load(o24 & 1);
  for (int s = o24 & 1; s < NSEG; s += 2) {
    u32 spins = 0;
    if (wave == 0) {
      const u32 fo = FLM_OFF + s * 3072 + o * 128 + (lane % 6) * 4;
      while (!allok(__builtin_amdgcn_raw_buffer_load_b32(r, fo, 0, SC1))) {
        if (spin_fail(spins, tmo)) break;
        __builtin_amdgcn_s_sleep(4);
      }
    }
    if (s - 2 < SPLIT) wait_vm();
    bar();
    if (tid == 0 && s >= 2 && s - 2 < SPLIT) __builtin_amdgcn_raw_buffer_store_b32(1u, r, FLA_OFF + (s - 2) * 128 + o * 4, 0, SC1);
    const u32 eo = e0 + s * 32 * DM * 2;
#pragma unroll
    for (int c = 0; c < 6; ++c)
      um[c] = __builtin_amdgcn_raw_buffer_load_b128(r, UM_OFF + ((s * NB + o * 6 + c) * 8 + wave) * 512 + (lane & 31) * 16, 0, SC1);
    u32x4 p0[4], p1[4]; u32x2 q0[4], q1[4];
    for (;;) {
      u32 m = 0;
#pragma unroll
      for (int u = 0; u < 4; ++u) {
        const u32 tb = PT_OFF + s * PT_STEP + u * 20480;
        p0[u] = __builtin_amdgcn_raw_buffer_load_b128(r, tb + lx, 0, SC1);
        p1[u] = __builtin_amdgcn_raw_buffer_load_b128(r, tb + lx + 1024, 0, SC1);
        q0[u] = __builtin_amdgcn_raw_buffer_load_b64(r, tb + lm, 0, SC1);
        q1[u] = __builtin_amdgcn_raw_buffer_load_b64(r, tb + lm + 256, 0, SC1);
      }
#pragma unroll
      for (int u = 0; u < 4; ++u) m = mx(m, mx(mx(mx4(p0[u]), mx4(p1[u])), mx(mx2(q0[u]), mx2(q1[u]))));
      if (allok(m)) break;
      if (spin_fail(spins, tmo)) break;
    }
#pragma unroll
    for (int c = 0; c < 6; ++c) {
      if (g >= 2) um[c] = u32x4{0, 0, 0, 0};
#pragma unroll
      for (int u = 0; u < 4; ++u) {
        f32x4 d = {0.f, 0.f, 0.f, 0.f};
        const u32x4 pmv = {q0[u].x, q0[u].y, q1[u].x, q1[u].y};
        d = mfma(x0[c], p0[u], d); d = mfma(x1[c], p1[u], d); d = mfma(um[c], pmv, d);
        if (diag) *(f32x4*)(smem + (c * 4 + u) * 4096 + redo) = d;
      }
    }
    bar();
    if (tid < 384) {
      const char* rp = smem + (tid >> 4) * 4096 + (tid & 15) * 16;
      f32x4 acc = *(const f32x4*)rp;
#pragma unroll
      for (int p = 1; p < 16; ++p) acc += *(const f32x4*)(rp + p * 256);
      const u32x2 av = {pk(acc[0], acc[1]), pk(acc[2], acc[3])};
      if (s < SPLIT) __builtin_amdgcn_raw_buffer_store_b64(av, r, AO_OFF + eo, 0, SC1);
      else *(u32x2*)((char*)a.out + eo) = u32x2{fin(xv.x, av.x, fv.x), fin(xv.y, av.y, fv.y)};
      if (s + 2 >= SPLIT && s + 2 < NSEG) {
        while (!allok(__builtin_amdgcn_raw_buffer_load_b32(r, FLF_OFF + (((s + 2) >> 2) * 6 + (o >> 1)) * 128, 0, SC1))) {
          if (spin_fail(spins, tmo)) break;
          __builtin_amdgcn_s_sleep(16);
        }
        asm volatile("" ::: "memory");
        xv = *(const u32x2*)((const char*)a.x + eo + 64 * DM * 2);
        fv = __builtin_amdgcn_raw_buffer_load_b64(r, FO_OFF + eo + 64 * DM * 2, 0, SC1);
      }
    }
    if (s + 2 < NSEG) ux_load(s + 2);
    STAMP(s, 0);
  }
}

__device__ __forceinline__ void role_v(const Args& a, int h, char* smem) {
  const int tid = threadIdx.x, lane = tid & 63, wave = __builtin_amdgcn_readfirstlane(tid >> 6);
  const int c16 = lane & 15, g = lane >> 4;
  unsigned char* ws = a.ws;
  __amdgpu_buffer_rsrc_t r = __builtin_amdgcn_make_buffer_rsrc(ws, 0, WS_END, 0x00020000);
  STAMP(64, 0);
  u32x4 WV[KS], WO[5][4];
  {
    const u32 sel = (c16 & 1) ? 0x07060302u : 0x05040100u;
    u32x4 st[18];
#pragma unroll
    for (int i = 0; i < 18; ++i) { const int pc = i * 512 + tid; st[i] = *(const u32x4*)(a.wv + (size_t)(pc >> 4) * (NH * DH) + h * DH + (pc & 15) * 8); }
#pragma unroll
    for (int i = 0; i < 18; ++i) { const int pc = i * 512 + tid; *(u32x4*)(smem + (pc >> 4) * 272 + (pc & 15) * 16) = st[i]; }
#pragma unroll
    for (int i = 0; i < 18; ++i) st[i] = *(const u32x4*)(a.wo + (size_t)h * DH * DM + (i * 512 + tid) * 8);
    bar();
    {
      const char* wl = smem + g * 8 * 272 + wave * 32 + (c16 >> 1) * 4;
#pragma unroll
      for (int t = 0; t < KS; ++t) {
#pragma unroll
        for (int p = 0; p < 4; ++p)
          WV[t][p] = __builtin_amdgcn_perm(*(const u32*)(wl + (t * 32 + 2 * p + 1) * 272), *(const u32*)(wl + (t * 32 + 2 * p) * 272), sel);
        asm volatile("" : "+v"(WV[t]) :: "memory");
      }
    }
    bar();
#pragma unroll
    for (int i = 0; i < 18; ++i) { const int pc = i * 512 + tid; *(u32x4*)(smem + (pc / 72) * 1168 + (pc % 72) * 16) = st[i]; }
    bar();
#pragma unroll
    for (int i = 0; i < 5; ++i) {
      const int ct = wave + 8 * i < 36 ? wave + 8 * i : 0;
      const char* wl = smem + g * 4 * 1168 + ct * 32 + (c16 >> 1) * 4;
#pragma unroll
      for (int es = 0; es < 4; ++es) {
#pragma unroll
        for (int p = 0; p < 4; ++p) {
          const int e0 = 32 * es + 16 * (p >> 1) + 2 * (p & 1);
          WO[i][es][p] = __builtin_amdgcn_perm(*(const u32*)(wl + (e0 + 1) * 1168), *(const u32*)(wl + e0 * 1168), sel);
        }
        asm volatile("" : "+v"(WO[i][es]) :: "memory");
      }
    }
    bar();
  }
  STAMP(64, 1);
  stage_x(ws, 0, wave, lane);
  for (int sg = 0; sg < NSEG; ++sg) {
    wait_vm();
    bar();
    if (sg > 0 && tid == 0) __builtin_amdgcn_raw_buffer_store_b32(1u, r, FLG_OFF + (sg - 1) * 128 + h * 4, 0, SC1);
    {
      const char* xb = smem + L_X + (sg & 1) * XF_SEG + lane * 16;
      f32x4 c0 = {0.f, 0.f, 0.f, 0.f}, c1 = c0;
#pragma unroll
      for (int t = 0; t < KS; ++t) {
        c0 = mfma(WV[t], *(const u32x4*)(xb + t * 1024), c0);
        c1 = mfma(WV[t], *(const u32x4*)(xb + (KS + t) * 1024), c1);
      }
      __builtin_amdgcn_sched_group_barrier(0x100, 8, 0);
#pragma unroll
      for (int t = 0; t < KS - 4; ++t) { __builtin_amdgcn_sched_group_barrier(0x008, 2, 0); __builtin_amdgcn_sched_group_barrier(0x100, 2, 0); }
      __builtin_amdgcn_sched_group_barrier(0x008, 8, 0);
      if (sg + 1 < NSEG) stage_x(ws, sg + 1, wave, lane);
      char* vt = smem + L_QM + (wave >> 1) * 1024 + lane * 16 + (wave & 1) * 8;
      *(u32x2*)vt = u32x2{pk(c0[0], c0[1]), pk(c0[2], c0[3])};
      *(u32x2*)(vt + 4096) = u32x2{pk(c1[0], c1[1]), pk(c1[2], c1[3])};
    }
    bar();
    u32x4 vf[2][4];
#pragma unroll
    for (int rt = 0; rt < 2; ++rt)
#pragma unroll
      for (int es = 0; es < 4; ++es) vf[rt][es] = *(const u32x4*)(smem + L_QM + rt * 4096 + es * 1024 + lane * 16);
#pragma unroll
    for (int i = 0; i < 5; ++i) {
      const int ct = wave + 8 * i;
      if (ct < 36) {
        f32x4 d0 = {0.f, 0.f, 0.f, 0.f}, d1 = d0;
#pragma unroll
        for (int es = 0; es < 4; ++es) { d0 = mfma(vf[0][es], WO[i][es], d0); d1 = mfma(vf[1][es], WO[i][es], d1); }
        const u32x4 o = {pk(d0[0], d0[1]), pk(d0[2], d0[3]), pk(d1[0], d1[1]), pk(d1[2], d1[3])};
        __builtin_amdgcn_raw_buffer_store_b128(o, r, UX_OFF + ((sg * NB + ct * 2 + (c16 >> 3)) * 16 + (h >> 1)) * 1024 + (g * 16 + (h & 1) * 8 + (lane & 7)) * 16, 0, SC1);
      }
    }
    if (sg == 0) STAMP(64, 2);
  }
  wait_vm();
  bar();
  if (tid == 0) __builtin_amdgcn_raw_buffer_store_b32(1u, r, FLG_OFF + (NSEG - 1) * 128 + h * 4, 0, SC1);
  STAMP(64, 3);
}

__device__ __forceinline__ float gelu_tanh(float x) {
  const float u = 0.7978845608028654f * (x + 0.044715f * x * x * x);
  const float e = __expf(2.f * u);
  return 0.5f * x * (2.f - 2.f / (e + 1.f));
}

__device__ __forceinline__ void role_f(const Args& a, int f, char* smem) {
  const int tid = threadIdx.x, lane = tid & 63, wave = __builtin_amdgcn_readfirstlane(tid >> 6);
  const int c16 = lane & 15, g = lane >> 4;
  unsigned char* ws = a.ws;
  u32* tmo = (u32*)(ws + CTL_OFF) + 64;
  u32* cnt = (u32*)(ws + CTL_OFF) + 128;
  __amdgpu_buffer_rsrc_t r = __builtin_amdgcn_make_buffer_rsrc(ws, 0, WS_END, 0x00020000);
  STAMP(64, 0);
  auto stage = [&](int sg) {
#pragma unroll
    for (int i = 0; i < 5; ++i) {
      const int p = min(i * 8 + wave, 35);
      glds16(ws + XF_OFF + sg * XF_SEG + p * 1024 + lane * 16, (sg & 3) * XF_SEG + p * 1024);
    }
  };
  if (f >= 64) {
    const int h = f - 64;
    u32x4 WW[2][KS];
    load_wqk(a, h, WW, smem);
    wait_vm();
    stage(0); stage(1);
    for (int sg = 0; sg < NSEG; ++sg) {
      if (sg < 4) wait_vm();
      else asm volatile("s_waitcnt vmcnt(5)" ::: "memory");
      bar();
      if (sg >= 1 && tid == 0) __builtin_amdgcn_raw_buffer_store_b32(1u, r, FLQ_OFF + (sg < 4 ? sg - 1 : sg - 2) * 128 + h * 4, 0, SC1);
#pragma unroll
      for (int rt = 0; rt < 2; ++rt) {
        f32x4 c0, c1;
        proj2<true, 1024>(WW[0], WW[1], smem + (sg & 3) * XF_SEG + rt * KS * 1024 + lane * 16, c0, c1);
        if (rt == 0) stage(min(sg + 2, NSEG - 1));
        const u32x4 o = {pk(c0[0], c1[0]), pk(c0[1], c1[1]), pk(c0[2], c1[2]), pk(c0[3], c1[3])};
        __builtin_amdgcn_raw_buffer_store_b128(o, r, QX_OFF + ((sg * NH + h) * 16 + (wave >> 2) * 8 + rt * 4 + (wave & 3)) * 1024 + lane * 16, 0, SC1);
      }
    }
    wait_vm();
    bar();
    if (tid < 2) __builtin_amdgcn_raw_buffer_store_b32(1u, r, FLQ_OFF + (NSEG - 2 + tid) * 128 + h * 4, 0, SC1);
  }
  for (int it = f * 41 + wave; it < min(f * 41 + 41, 2592) && f < 64; it += 8) {
    const int ks = it / 18, bb = it % 18;
    u32 d[8];
#pragma unroll
    for (int jj = 0; jj < 8; ++jj)
      d[jj] = *(const u32*)(a.wd + (size_t)(ks * 32 + (jj >> 2) * 16 + g * 4 + (jj & 3)) * DM + bb * 32 + c16 * 2);
    const u32x4 lo = {plo(d[0], d[1]), plo(d[2], d[3]), plo(d[4], d[5]), plo(d[6], d[7])};
    const u32x4 hi = {phi(d[0], d[1]), phi(d[2], d[3]), phi(d[4], d[5]), phi(d[6], d[7])};
    __builtin_amdgcn_raw_buffer_store_b128(lo, r, WDF_OFF + (bb * 144 + ks) * 2048 + lane * 16, 0, SC1);
    __builtin_amdgcn_raw_buffer_store_b128(hi, r, WDF_OFF + (bb * 144 + ks) * 2048 + 1024 + lane * 16, 0, SC1);
  }
#pragma unroll 1
  for (int job = 0; job < 3 && f < 64; ++job) {
    int tid = threadIdx.x;
    asm volatile("" : "+v"(tid));
    const int hv = wave & 3, rt = wave >> 2;
    const int u0 = f * 72, sl = (u0 >> 6) + job;
    const int q0 = job == 0 ? (u0 & 63) : 0, q1 = min(u0 + 72 - sl * 64, 64);
    if (q1 <= q0) break;
    u32x4 WW[2][KS];
#pragma unroll
    for (int mat = 0; mat < 2; ++mat) {
      __syncthreads();
      u32x4 st[9];
#pragma unroll
      for (int i = 0; i < 9; ++i) {
        const int pc = i * 512 + tid;
        st[i] = *(const u32x4*)((mat ? a.wu : a.wg) + (size_t)(pc >> 3) * FF + sl * 64 + (pc & 7) * 8);
      }
#pragma unroll
      for (int i = 0; i < 9; ++i) { const int pc = i * 512 + tid; *(u32x4*)(smem + (pc >> 3) * 144 + (pc & 7) * 16) = st[i]; }
      __syncthreads();
      const char* wl = smem + g * 8 * 144 + hv * 32 + (c16 >> 1) * 4;
      const u32 sel = (c16 & 1) ? 0x07060302u : 0x05040100u;
#pragma unroll
      for (int t = 0; t < KS; ++t) {
#pragma unroll
        for (int p = 0; p < 4; ++p) {
          const int o = (t * 32 + 2 * p) * 144;
          WW[mat][t][p] = __builtin_amdgcn_perm(*(const u32*)(wl + o + 144), *(const u32*)(wl + o), sel);
        }
        asm volatile("" : "+v"(WW[mat][t]) :: "memory");
      }
    }
    __syncthreads();
    if (job == 0) STAMP(64, 1);
    stage(q0); stage(min(q0 + 1, q1 - 1));
    for (int sg = q0; sg < q1; ++sg) {
      asm volatile("s_waitcnt vmcnt(5)" ::: "memory");
      bar();
      f32x4 cg, cu;
      proj2<true, 1024>(WW[0], WW[1], smem + (sg & 3) * XF_SEG + rt * KS * 1024 + lane * 16, cg, cu);
      stage(min(sg + 2, q1 - 1));
      float hh[4];
#pragma unroll
      for (int i = 0; i < 4; ++i) hh[i] = bfr(gelu_tanh(bfr(cg[i]))) * bfr(cu[i]);
      const u32x2 o = {pk(hh[0], hh[1]), pk(hh[2], hh[3])};
      __builtin_amdgcn_raw_buffer_store_b64(o, r, H_OFF + ((sg * 2 + rt) * 144 + sl * 2 + (hv >> 1)) * 1024 + lane * 16 + (hv & 1) * 8, 0, 0);
    }
    wait_vm();
  }
  wait_vm();
  __syncthreads();
  STAMP(64, 2);
  if (tid == 0) {
    if (f < 64) {
      __builtin_amdgcn_fence(__ATOMIC_RELEASE, "agent");
      wait_vm();
      __hip_atomic_fetch_add(cnt, 1u, __ATOMIC_RELAXED, __HIP_MEMORY_SCOPE_AGENT);
    }
    u32 spins = 0;
    while (__hip_atomic_load(cnt, __ATOMIC_RELAXED, __HIP_MEMORY_SCOPE_AGENT) < 64u) {
      __builtin_amdgcn_s_sleep(8);
      if (spin_fail(spins, tmo)) break;
    }
    __builtin_amdgcn_fence(__ATOMIC_ACQUIRE, "agent");
    wait_vm();
  }
  __syncthreads();
  STAMP(64, 3);
  {
    const int ft = (f + 32) % NF;
    const int tm = ft / 6, b3 = (ft % 6) * 3, tg = wave & 3, kh = wave >> 2;
    const unsigned char* hb = ws + H_OFF + ((size_t)(tm * 8 + tg * 2) * 144 + kh * 72) * 1024 + lane * 16;
    const unsigned char* wb = ws + WDF_OFF + (size_t)(b3 * 144 + kh * 72) * 2048 + lane * 16;
    f32x4 acc[2][6];
#pragma unroll
    for (int u = 0; u < 2; ++u)
#pragma unroll
      for (int i = 0; i < 6; ++i) acc[u][i] = f32x4{0.f, 0.f, 0.f, 0.f};
    u32x4 hf[2][2][2], wf[2][2][6];
    auto ld = [&](int st, int t0) {
#pragma unroll
      for (int t = 0; t < 2; ++t) {
        hf[st][t][0] = *(const u32x4*)(hb + (t0 + t) * 1024); hf[st][t][1] = *(const u32x4*)(hb + (144 + t0 + t) * 1024);
#pragma unroll
        for (int i = 0; i < 6; ++i) wf[st][t][i] = *(const u32x4*)(wb + ((i >> 1) * 144 + t0 + t) * 2048 + (i & 1) * 1024);
      }
    };
    auto mm = [&](int st) {
#pragma unroll
      for (int t = 0; t < 2; ++t)
#pragma unroll
        for (int i = 0; i < 6; ++i) { acc[0][i] = mfma(wf[st][t][i], hf[st][t][0], acc[0][i]); acc[1][i] = mfma(wf[st][t][i], hf[st][t][1], acc[1][i]); }
    };
    ld(0, 0);
#pragma unroll 1
    for (int t0 = 0; t0 < 72; t0 += 4) {
      ld(1, t0 + 2);
      __builtin_amdgcn_sched_barrier(0);
      mm(0);
      __builtin_amdgcn_sched_barrier(0);
      ld(0, min(t0 + 4, 70));
      __builtin_amdgcn_sched_barrier(0);
      mm(1);
      __builtin_amdgcn_sched_barrier(0);
    }
    STAMP(64, 4);
    if (kh == 1) {
#pragma unroll
      for (int u = 0; u < 2; ++u)
#pragma unroll
        for (int i = 0; i < 6; ++i) *(f32x4*)(smem + ((tg * 12 + u * 6 + i) * 64 + lane) * 16) = acc[u][i];
    }
    const bool mine = tm * 4 < SPLIT;
    if (mine && wave == 0) {
      u32 spins = 0;
      while (!allok(__builtin_amdgcn_raw_buffer_load_b32(r, FLA_OFF + (tm * 4 + ((lane >> 1) & 3)) * 128 + ((ft % 6) * 2 + (lane & 1)) * 4, 0, SC1))) {
        if (spin_fail(spins, tmo)) break;
        __builtin_amdgcn_s_sleep(16);
      }
    }
    __syncthreads();
    if (kh == 0) {
#pragma unroll
      for (int u = 0; u < 2; ++u)
#pragma unroll
        for (int b = 0; b < 3; ++b) {
          const f32x4 e0 = acc[u][2 * b] + *(const f32x4*)(smem + ((tg * 12 + u * 6 + 2 * b) * 64 + lane) * 16);
          const f32x4 e1 = acc[u][2 * b + 1] + *(const f32x4*)(smem + ((tg * 12 + u * 6 + 2 * b + 1) * 64 + lane) * 16);
          const u32x4 o = {pk(e0[0], e1[0]), pk(e0[1], e1[1]), pk(e0[2], e1[2]), pk(e0[3], e1[3])};
          const u32 eo = ((tm * 128 + tg * 32 + u * 16 + c16) * DM + (b3 + b) * 32 + g * 8) * 2;
          if (mine) {
            const u32x4 xv = *(const u32x4*)((const char*)a.x + eo), av = __builtin_amdgcn_raw_buffer_load_b128(r, AO_OFF + eo, 0, SC1);
            *(u32x4*)((char*)a.out + eo) = u32x4{fin(xv.x, av.x, o.x), fin(xv.y, av.y, o.y), fin(xv.z, av.z, o.z), fin(xv.w, av.w, o.w)};
          } else {
            __builtin_amdgcn_raw_buffer_store_b128(o, r, FO_OFF + eo, 0, SC1);
          }
        }
    }
    wait_vm();
    __syncthreads();
    if (!mine && tid == 0) __builtin_amdgcn_raw_buffer_store_b32(1u, r, FLF_OFF + ft * 128, 0, SC1);
  }
}

__global__ void __launch_bounds__(512, 2) k2_main(Args a) {
  extern __shared__ __attribute__((aligned(16))) char smem[];
  if (threadIdx.x == 0) {
    u32* ctl = (u32*)(a.ws + CTL_OFF);
    const u32 x = __builtin_amdgcn_s_getreg((4 - 1) << 11 | 0 << 6 | 20) & 7u;
    const u32 rank = __hip_atomic_fetch_add(ctl + 256 + x * 32, 1u, __ATOMIC_RELAXED, __HIP_MEMORY_SCOPE_AGENT);
    u32 slot = rank * 8 + x;
    if (rank >= 32) {
      u32 k = __hip_atomic_fetch_add(ctl + 192, 1u, __ATOMIC_RELAXED, __HIP_MEMORY_SCOPE_AGENT), spins = 0;
      for (;;) {
        u32 tot = 0;
        for (int i = 0; i < 8; ++i) tot += __hip_atomic_load(ctl + 256 + i * 32, __ATOMIC_RELAXED, __HIP_MEMORY_SCOPE_AGENT);
        if (tot >= 256u || spin_fail(spins, ctl + 64)) break;
      }
      slot = 256;
      for (int i = 0; i < 8 && slot == 256; ++i) {
        const u32 c = __hip_atomic_load(ctl + 256 + i * 32, __ATOMIC_RELAXED, __HIP_MEMORY_SCOPE_AGENT), fr = c < 32u ? 32u - c : 0u;
        if (k < fr) slot = (c + k) * 8 + i; else k -= fr;
      }
    }
    *(u32*)smem = slot;
  }
  __syncthreads();
  const int b = __builtin_amdgcn_readfirstlane(*(const u32*)smem), rr = b & 7, q = b >> 3;
  __syncthreads();
  if (b >= 256) return;
  if (rr == 0) role_a(a, q, smem);
  else if (rr < 4 && q < 24) role_b(a, (rr - 1) * 24 + q, smem);
  else if (rr == 4) role_v(a, q, smem);
  else if (rr > 4) role_f(a, (rr - 5) * 32 + q, smem);
  else role_o(a, (rr - 1) * 8 + (q - 24), smem);
  DUMP();
}

extern "C" void kernel_launch(void* const* d_in, const int* in_sizes, int n_in, void* d_out, int out_size, void* d_ws,
                              size_t ws_size, hipStream_t stream) {
  Args a;
  a.x = (const u16*)d_in[0]; a.mem0 = (const u16*)d_in[1]; a.wq = (const u16*)d_in[2]; a.wk = (const u16*)d_in[3];
  a.wv = (const u16*)d_in[4]; a.wo = (const u16*)d_in[5]; a.wg = (const u16*)d_in[8]; a.wu = (const u16*)d_in[9];
  a.wd = (const u16*)d_in[10];
  a.ws = (unsigned char*)(((uintptr_t)d_ws + 65535) & ~(uintptr_t)65535);
  a.out = (u16*)d_out;
  (void)hipFuncSetAttribute((const void*)k2_main, hipFuncAttributeMaxDynamicSharedMemorySize, LDS_BYTES);
  k1_prep<<<K1_GRID, 256, 0, stream>>>(a);
  k2_main<<<256, 512, LDS_BYTES, stream>>>(a);
}
```

```cpp
#include <hip/hip_runtime.h>
#include <hip/hip_bf16.h>

typedef unsigned u32;
typedef unsigned long long u64;
typedef unsigned short u16;
typedef __attribute__((ext_vector_type(8))) __bf16 bf16x8;
typedef __attribute__((ext_vector_type(2))) __bf16 bf16x2;
typedef __attribute__((ext_vector_type(4))) float f32x4;
typedef __attribute__((ext_vector_type(2))) float f32x2;
typedef __attribute__((ext_vector_type(4))) unsigned u32x4;
typedef __attribute__((ext_vector_type(2))) unsigned u32x2;

constexpr int S_ = 2048, DM = 576, NH = 32, DH = 128, FF = 4608, NSEG = 64, KS = 18;
constexpr int NB = 72, NF = 96;
constexpr u32 HO_OFF = 0;
constexpr u32 PT_OFF = HO_OFF + NSEG * 65536;
constexpr u32 PT_STEP = 4 * 20480;
constexpr u32 FLG_OFF = PT_OFF + NSEG * PT_STEP;
constexpr u32 FLM_OFF = FLG_OFF + NSEG * 128;
constexpr u32 FLQ_OFF = FLM_OFF + NSEG * 3072;
constexpr u32 FLA_OFF = FLQ_OFF + NSEG * 128;
constexpr u32 FLF_OFF = FLA_OFF + NSEG * 128;
constexpr int SPLIT = 56;
constexpr u32 FLP_OFF = FLF_OFF + NF * 128;
constexpr u32 POISON_BYTES = FLP_OFF + 256;
constexpr u32 CTL_OFF = (POISON_BYTES + 4095) & ~4095u;
constexpr u32 M0_OFF = CTL_OFF + 4096;
constexpr u32 XF_OFF = M0_OFF + 12288;
constexpr u32 XF_SEG = 2 * KS * 1024;
constexpr u32 WVO_OFF = XF_OFF + NSEG * XF_SEG;
constexpr u32 WVO_J = 16 * KS * 1024;
constexpr u32 UX_OFF = WVO_OFF + NB * WVO_J;
constexpr u32 UM_OFF = UX_OFF + NSEG * NB * 16384;
constexpr u32 QX_OFF = UM_OFF + NSEG * NB * 4096;
constexpr u32 H_OFF = QX_OFF + NSEG * NH * 16384;
constexpr u32 WDF_OFF = H_OFF + 128 * 144 * 1024;
constexpr u32 FO_OFF = WDF_OFF + 9 * 144 * 4096;
constexpr u32 AO_OFF = FO_OFF + S_ * DM * 2;
constexpr u32 WS_END = AO_OFF + S_ * DM * 2;
constexpr u32 POISON = 0xFFFFFFFFu;
constexpr int SC1 = 16;
constexpr u32 SPIN_CAP = 400000u;
constexpr int LDS_BYTES = 160 * 1024;

__device__ __forceinline__ u32 pk(float lo, float hi) {
  f32x2 v = {lo, hi};
  return __builtin_bit_cast(u32, __builtin_convertvector(v, bf16x2));
}
__device__ __forceinline__ u32 pks(float lo, float hi) { u32 r = pk(lo, hi); return r == POISON ? 0x7FC07FC0u : r; }
__device__ __forceinline__ float bfr(float x) { return (float)(__bf16)x; }
__device__ __forceinline__ float bflo(u32 w) { return __uint_as_float(w << 16); }
__device__ __forceinline__ float bfhi(u32 w) { return __uint_as_float(w & 0xFFFF0000u); }
__device__ __forceinline__ u32 fin(u32 x, u32 p, u32 f) {
  return pk(bfr(bflo(x) + bflo(p)) + bflo(f), bfr(bfhi(x) + bfhi(p)) + bfhi(f));
}
__device__ __forceinline__ f32x4 mfma(u32x4 a, u32x4 b, f32x4 c) {
  return __builtin_amdgcn_mfma_f32_16x16x32_bf16(__builtin_bit_cast(bf16x8, a), __builtin_bit_cast(bf16x8, b), c, 0, 0, 0);
}
__device__ __forceinline__ u32 plo(u32 a, u32 b) { return __builtin_amdgcn_perm(b, a, 0x05040100u); }
__device__ __forceinline__ u32 phi(u32 a, u32 b) { return __builtin_amdgcn_perm(b, a, 0x07060302u); }
__device__ __forceinline__ float vmax(float a, float b) { float r; asm("v_max_f32 %0, %1, %2" : "=v"(r) : "v"(a), "v"(b)); return r; }
__device__ __forceinline__ float vmax3(float a, float b, float c) { float r; asm("v_max3_f32 %0, %1, %2, %3" : "=v"(r) : "v"(a), "v"(b), "v"(c)); return r; }
__device__ __forceinline__ float xmax(float x) {
  auto s = __builtin_amdgcn_permlane16_swap(__float_as_uint(x), __float_as_uint(x), false, false);
  x = vmax(__uint_as_float(s[0]), __uint_as_float(s[1]));
  auto t = __builtin_amdgcn_permlane32_swap(__float_as_uint(x), __float_as_uint(x), false, false);
  return vmax(__uint_as_float(t[0]), __uint_as_float(t[1]));
}
__device__ __forceinline__ float xsum(float x) {
  auto s = __builtin_amdgcn_permlane16_swap(__float_as_uint(x), __float_as_uint(x), false, false);
  x = __uint_as_float(s[0]) + __uint_as_float(s[1]);
  auto t = __builtin_amdgcn_permlane32_swap(__float_as_uint(x), __float_as_uint(x), false, false);
  return __uint_as_float(t[0]) + __uint_as_float(t[1]);
}
__device__ __forceinline__ float ror8(float x) {
  return __builtin_bit_cast(float, __builtin_amdgcn_mov_dpp(__builtin_bit_cast(int, x), 0x128, 0xf, 0xf, true));
}
__device__ __forceinline__ u32 mx(u32 a, u32 b) { return a > b ? a : b; }
__device__ __forceinline__ u32 mx4(u32x4 v) { return mx(mx(v.x, v.y), mx(v.z, v.w)); }
__device__ __forceinline__ u32 mx2(u32x2 v) { return mx(v.x, v.y); }
__device__ __forceinline__ bool allok(u32 m) { return __all(m != POISON); }
__device__ __forceinline__ void glds16(const void* g, u32 lds) {
  u32 keep;
  asm volatile("s_mov_b32 %0, m0\n\ts_mov_b32 m0, %2\n\ts_nop 0\n\tglobal_load_lds_dwordx4 %1, off\n\ts_mov_b32 m0, %0"
               : "=&s"(keep) : "v"(g), "s"(lds) : "memory");
}
__device__ __forceinline__ void wait_vm() { asm volatile("s_waitcnt vmcnt(0)" ::: "memory"); }
__device__ __forceinline__ void bar() {
  asm volatile("s_waitcnt lgkmcnt(0)" ::: "memory");
  __builtin_amdgcn_s_barrier();
  asm volatile("" ::: "memory");
}
__device__ __forceinline__ bool spin_fail(u32& spins, u32* tmo) {
  ++spins;
  if (spins > SPIN_CAP) { __hip_atomic_store(tmo, 1u, __ATOMIC_RELAXED, __HIP_MEMORY_SCOPE_AGENT); return true; }
  if ((spins & 1023u) == 0u && __hip_atomic_load(tmo, __ATOMIC_RELAXED, __HIP_MEMORY_SCOPE_AGENT) != 0u) return true;
  return false;
}
__device__ __forceinline__ u32 homap(u32 lo, u32 flip) { return ((lo & ~8191u) << 1) + (lo & 8191u) + flip; }
constexpr u32 HO_PM = 16384, HO_M = 20480;

#define STAMP(step, k) do {} while (0)
#define STAMPC(step, k) do {} while (0)
#define DUMP() do {} while (0)
struct Args {
  const u16* x; const u16* mem0; const u16* wq; const u16* wk; const u16* wv; const u16* wo;
  const u16* wg; const u16* wu; const u16* wd; unsigned char* ws; u16* out;
};

constexpr int K1_WVO = 2592, K1_LN = 256, K1_LNB = K1_LN / 4, K1_JOBS = K1_WVO + K1_LN, K1_GRID = K1_JOBS / 4 + 8;
__global__ void __launch_bounds__(256) k1_prep(Args a) {
  __shared__ __attribute__((aligned(16))) char tbuf[16384];
  const int lane = threadIdx.x & 63, wave = __builtin_amdgcn_readfirstlane(threadIdx.x >> 6);
  const int b = blockIdx.x;
  unsigned char* ws = a.ws;
  const int c16 = lane & 15, g = lane >> 4;
  if (b == K1_GRID - 1) {
    for (int i = threadIdx.x; i < 1024; i += 256) ((u32*)(ws + CTL_OFF))[i] = 0u;
    for (int i = threadIdx.x; i < 576; i += 256) {
      u32x4 v = *(const u32x4*)(a.mem0 + (i & 7) * DM + (i >> 3) * 8);
      if (v.x == POISON) v.x = 0x7FC07FC0u; if (v.y == POISON) v.y = 0x7FC07FC0u;
      if (v.z == POISON) v.z = 0x7FC07FC0u; if (v.w == POISON) v.w = 0x7FC07FC0u;
      *(u32x4*)(ws + M0_OFF + i * 16) = v;
    }
    return;
  }
  if (b >= K1_JOBS / 4) return;
  const int job = b * 4 + wave;
  auto poison = [&]() {
    const u32x4 pv = {POISON, POISON, POISON, POISON};
    for (u32 o = (job * 64 + lane) * 16; o < POISON_BYTES; o += K1_JOBS * 64 * 16) *(u32x4*)(ws + o) = pv;
  };
  if (b >= K1_LNB) {
    const int jx = ((b - K1_LNB) >> 3) * 4 + wave, h = (b & 7) * 4 + jx / 81, kp = (jx / 9) % 9, bb = jx % 9;
    u32x2 rw[4][8];
    u32x4 af[2][2][4];
#pragma unroll
    for (int es = 0; es < 4; ++es)
#pragma unroll
      for (int jj = 0; jj < 8; ++jj)
        rw[es][jj] = *(const u32x2*)(a.wo + (size_t)(h * DH + es * 32 + g * 8 + jj) * DM + bb * 64 + c16 * 4);
#pragma unroll
    for (int i = 0; i < 16; ++i) {
      const int k = (kp * 2 + (i >> 3)) * 32 + (c16 >> 2) * 8 + (c16 & 3) + ((i >> 2) & 1) * 4;
      af[i >> 3][(i >> 2) & 1][i & 3] = *(const u32x4*)(a.wv + (size_t)k * (NH * DH) + h * DH + (i & 3) * 32 + g * 8);
    }
    poison();
    __builtin_amdgcn_sched_barrier(0);
    u32x4 bf[4][4];
#pragma unroll
    for (int es = 0; es < 4; ++es)
#pragma unroll
      for (int p = 0; p < 4; ++p) {
        bf[es][0][p] = plo(rw[es][2 * p].x, rw[es][2 * p + 1].x); bf[es][1][p] = phi(rw[es][2 * p].x, rw[es][2 * p + 1].x);
        bf[es][2][p] = plo(rw[es][2 * p].y, rw[es][2 * p + 1].y); bf[es][3][p] = phi(rw[es][2 * p].y, rw[es][2 * p + 1].y);
      }
#pragma unroll
    for (int tt = 0; tt < 2; ++tt) {
      const int t = kp * 2 + tt;
      f32x4 acc[2][4];
#pragma unroll
      for (int rt = 0; rt < 2; ++rt)
#pragma unroll
        for (int i = 0; i < 4; ++i) {
          acc[rt][i] = f32x4{0.f, 0.f, 0.f, 0.f};
#pragma unroll
          for (int es = 0; es < 4; ++es) acc[rt][i] = mfma(af[tt][rt][es], bf[es][i], acc[rt][i]);
        }
      char* tl = tbuf + wave * 4096;
#pragma unroll
      for (int i = 0; i < 4; ++i) {
        u32x4 v = {pk(acc[0][i][0], acc[0][i][1]), pk(acc[0][i][2], acc[0][i][3]), pk(acc[1][i][0], acc[1][i][1]), pk(acc[1][i][2], acc[1][i][3])};
        *(u32x4*)(tl + ((c16 >> 1) * 4 + g) * 128 + ((c16 & 1) * 4 + i) * 16) = v;
      }
      __builtin_amdgcn_wave_barrier();
#pragma unroll
      for (int k = 0; k < 4; ++k) {
        const int ln = k * 8 + (lane >> 3);
        const u32x4 v = *(const u32x4*)(tl + k * 1024 + lane * 16);
        *(u32x4*)(ws + WVO_OFF + (size_t)(bb * 8 + (ln >> 2)) * WVO_J + ((h >> 1) * KS + t) * 1024 + ((ln & 3) * 16 + (h & 1) * 8 + (lane & 7)) * 16) = v;
      }
      __builtin_amdgcn_wave_barrier();
    }
  } else {
    u32x4 vv[2][5];
#pragma unroll
    for (int rr = 0; rr < 2; ++rr)
#pragma unroll
    for (int i = 0; i < 5; ++i) {
      vv[rr][i] = u32x4{0, 0, 0, 0};
      if (i < 4 || c16 < 8) vv[rr][i] = *(const u32x4*)(a.x + (size_t)(job * 8 + rr * 4 + g) * DM + (i * 16 + c16) * 8);
    }
    poison();
#pragma unroll
    for (int rr = 0; rr < 2; ++rr) {
    const int row = job * 8 + rr * 4 + g;
    const u32x4 (&v)[5] = vv[rr];
    float sm = 0.f;
#pragma unroll
    for (int i = 0; i < 20; ++i) sm += bflo(v[i >> 2][i & 3]) + bfhi(v[i >> 2][i & 3]);
#pragma unroll
    for (int o = 1; o < 16; o <<= 1) sm += __shfl_xor(sm, o);
    const float mean = sm * (1.f / DM);
    float q = 0.f;
#pragma unroll
    for (int i = 0; i < 20; ++i) {
      const float d0 = bflo(v[i >> 2][i & 3]) - mean, d1 = bfhi(v[i >> 2][i & 3]) - mean;
      q += (i < 16 || c16 < 8) ? d0 * d0 + d1 * d1 : 0.f;
    }
#pragma unroll
    for (int o = 1; o < 16; o <<= 1) q += __shfl_xor(q, o);
    const float mb = bfr(mean), den = bfr(sqrtf(bfr(bfr(q * (1.f / DM)) + 1e-5f)));
#pragma unroll
    for (int i = 0; i < 5; ++i) {
      u32x4 o;
#pragma unroll
      for (int k = 0; k < 4; ++k) o[k] = pk(bfr(bflo(v[i][k]) - mb) / den, bfr(bfhi(v[i][k]) - mb) / den);
      const int ch = i * 16 + c16;
      if (i < 4 || c16 < 8) *(u32x4*)(ws + XF_OFF + ((row >> 4) * KS + (ch >> 2)) * 1024 + ((ch & 3) * 16 + (row & 15)) * 16) = o;
    }
    }
  }
}

constexpr u32 L_X = 0;
constexpr u32 L_M = 2 * XF_SEG;
constexpr u32 L_QM = L_M + 9216;
constexpr u32 L_QX = L_QM + 8192;

template <bool WA, int ST>
__device__ __forceinline__ void proj2(const u32x4 (&W0)[KS], const u32x4 (&W1)[KS], const char* xb, f32x4& c0, f32x4& c1) {
  u32x4 xf[KS];
#pragma unroll
  for (int t = 0; t < KS; ++t) xf[t] = *(const u32x4*)(xb + t * ST);
  c0 = f32x4{0.f, 0.f, 0.f, 0.f}; c1 = c0;
#pragma unroll
  for (int t = 0; t < KS; ++t) {
    c0 = WA ? mfma(W0[t], xf[t], c0) : mfma(xf[t], W0[t], c0);
    c1 = WA ? mfma(W1[t], xf[t], c1) : mfma(xf[t], W1[t], c1);
  }
  __builtin_amdgcn_sched_group_barrier(0x100, 6, 0);
#pragma unroll
  for (int t = 0; t < KS - 6; ++t) { __builtin_amdgcn_sched_group_barrier(0x008, 2, 0); __builtin_amdgcn_sched_group_barrier(0x100, 1, 0); }
  __builtin_amdgcn_sched_group_barrier(0x008, 12, 0);
}
__device__ __forceinline__ void stage_x(const unsigned char* ws, int seg, int wave, int lane) {
  const unsigned char* src = ws + XF_OFF + seg * XF_SEG + lane * 16;
  const u32 dst = L_X + (seg & 1) * XF_SEG;
#pragma unroll
  for (int i = 0; i < 5; ++i) {
    const int p = i * 8 + wave;
    if (p < 36) glds16(src + p * 1024, dst + p * 1024);
  }
}
__device__ __forceinline__ u32 get_flips(__amdgpu_buffer_rsrc_t r, int lane, u32* tmo) {
  u32 f, spins = 0;
  for (;;) {
    f = __builtin_amdgcn_raw_buffer_load_b32(r, FLP_OFF + lane * 4, 0, SC1);
    if (allok(f)) break;
    if (spin_fail(spins, tmo)) break;
    __builtin_amdgcn_s_sleep(8);
  }
  return f & 8192u;
}
__device__ __forceinline__ void gather_m(__amdgpu_buffer_rsrc_t r, u32 base, u32 flip, bool first, char* smem, int wave, int lane, u32* tmo) {
  const u32 l0 = (wave * 64 + lane) * 16, l1 = (512 + wave * 8 + (lane & 7)) * 16;
  const u32 o0 = first ? M0_OFF + l0 : base + homap(HO_M + l0, flip), o1 = first ? M0_OFF + l1 : base + homap(HO_M + l1, flip);
  u32 spins = 0;
  u32x4 v, w;
  for (;;) {
    v = __builtin_amdgcn_raw_buffer_load_b128(r, o0, 0, SC1);
    w = __builtin_amdgcn_raw_buffer_load_b128(r, o1, 0, SC1);
    if (allok(mx(mx4(v), mx4(w)))) break;
    if (spin_fail(spins, tmo)) break;
  }
  *(u32x4*)(smem + L_M + l0) = v;
  if (lane < 8) *(u32x4*)(smem + L_M + l1) = w;
  wait_vm();
}

__device__ __forceinline__ void load_wqk(const Args& a, int h, u32x4 (&W)[2][KS], char* smem) {
  const int tid = threadIdx.x, lane = tid & 63, wave = __builtin_amdgcn_readfirstlane(tid >> 6);
  const int c16 = lane & 15, g = lane >> 4;
  {
    u32x4 st[18];
    auto ldw = [&](int hf) {
#pragma unroll
      for (int i = 0; i < 18; ++i) {
        const int pc = i * 512 + tid;
        st[i] = *(const u32x4*)(((pc & 16) ? a.wk : a.wq) + (size_t)(hf * 288 + (pc >> 5)) * (NH * DH) + h * DH + (pc & 15) * 8);
      }
    };
    ldw(0);
#pragma unroll
    for (int hf = 0; hf < 2; ++hf) {
#pragma unroll
      for (int i = 0; i < 18; ++i) { const int pc = i * 512 + tid; *(u32x4*)(smem + (pc >> 5) * 528 + (pc & 31) * 16) = st[i]; }
      if (hf == 0) ldw(1);
      bar();
      const char* wl = smem + g * 8 * 528 + wave * 64 + c16 * 4;
#pragma unroll
      for (int t = 0; t < 9; ++t) {
#pragma unroll
        for (int p = 0; p < 4; ++p) {
          const u32 d0 = *(const u32*)(wl + (t * 32 + 2 * p) * 528), d1 = *(const u32*)(wl + (t * 32 + 2 * p + 1) * 528);
          W[0][hf * 9 + t][p] = plo(d0, d1); W[1][hf * 9 + t][p] = phi(d0, d1);
        }
        asm volatile("" : "+v"(W[0][hf * 9 + t]), "+v"(W[1][hf * 9 + t]) :: "memory");
      }
      bar();
    }
  }
}

__device__ __forceinline__ void role_a(const Args& a, int h, char* smem) {
  const int tid = threadIdx.x, lane = tid & 63, wave = __builtin_amdgcn_readfirstlane(tid >> 6);
  const int c16 = lane & 15, g = lane >> 4;
  unsigned char* ws = a.ws;
  u32* tmo = (u32*)(ws + CTL_OFF) + 64;
  __amdgpu_buffer_rsrc_t r = __builtin_amdgcn_make_buffer_rsrc(ws, 0, WS_END, 0x00020000);
  STAMP(64, 0);
  u32x4 W[2][KS];
  load_wqk(a, h, W, smem);
  const u32 flips = get_flips(r, lane, tmo);
  const u32 lx = (((h >> 1) * 4 + g) * 16 + (h & 1) * 8 + (lane & 7)) * 16, lm = HO_PM + h * 128 + g * 64 + (lane & 7) * 8;
  STAMP(64, 1); STAMPC(65, 0); STAMP(65, 1);
  u32x4 kf[8];
  u32 fl = POISON;
  auto q_load = [&](int seg) {
    const u32 fo = FLQ_OFF + seg * 128 + h * 4;
    u32 spins = 0;
    while (!allok(fl)) {
      fl = __builtin_amdgcn_raw_buffer_load_b32(r, fo, 0, SC1);
      if (spin_fail(spins, tmo)) break;
    }
    asm volatile("" ::: "memory");
    const u32 qo = QX_OFF + ((seg * NH + h) * 16 + (wave - 1) * 4) * 1024 + lane * 16;
#pragma unroll
    for (int w = 0; w < 4; ++w) kf[w] = __builtin_amdgcn_raw_buffer_load_b128(r, qo + w * 1024, 0, SC1);
    if (wave == 3) {
#pragma unroll
      for (int w = 4; w < 8; ++w) kf[w] = __builtin_amdgcn_raw_buffer_load_b128(r, qo + w * 1024, 0, SC1);
#pragma unroll
      for (int w = 0; w < 8; ++w) *(u32x4*)(smem + L_QX + (seg & 1) * 16384 + 8192 + w * 1024 + lane * 16) = kf[w];
    }
    fl = seg + 1 < NSEG ? __builtin_amdgcn_raw_buffer_load_b32(r, fo + 128, 0, SC1) : 0u;
  };
  stage_x(ws, 0, wave, lane);
  wait_vm();
  bar();
  if (wave >= 4) {
#pragma unroll
    for (int rt = 0; rt < 2; ++rt) {
      f32x4 c0, c1;
      proj2<true, 1024>(W[0], W[1], smem + L_X + rt * KS * 1024 + lane * 16, c0, c1);
      *(u32x4*)(smem + L_QX + 8192 + rt * 4096 + (wave & 3) * 1024 + lane * 16) = u32x4{pk(c0[0], c1[0]), pk(c0[1], c1[1]), pk(c0[2], c1[2]), pk(c0[3], c1[3])};
    }
  }
  for (int s = 0; s < NSEG; ++s) {
    const u32 flip = __builtin_amdgcn_readlane(flips, s), base = HO_OFF + s * 65536;
    STAMP(s, 0);
    gather_m(r, base, flip, s == 0, smem, wave, lane, tmo);
    STAMP(s, 1);
    bar();
    STAMP(s, 2);
    {
      f32x4 c0, c1;
      proj2<true, 512>(W[0], W[1], smem + L_M + g * 128 + (lane & 7) * 16, c0, c1);
      u32x4 o = {pk(c0[0], c1[0]), pk(c0[1], c1[1]), pk(c0[2], c1[2]), pk(c0[3], c1[3])};
      *(u32x4*)(smem + L_QM + wave * 1024 + lane * 16) = o;
    }
    if (wave == 0) {
      __builtin_amdgcn_s_setprio(3);
#pragma unroll
      for (int w = 0; w < 8; ++w) kf[w] = *(const u32x4*)(smem + L_QX + (s & 1) * 16384 + 8192 + lane * 16 + w * 1024);
    }
    bar();
    STAMP(s, 3);
    const float C2 = 0.08838834764831845f * 1.4426950408889634f;
    if (wave == 0) {
      if (s < NSEG - 1) {
        const char* qb = smem + L_QM + lane * 16;
        f32x4 s0 = {0.f, 0.f, 0.f, 0.f}, s1 = s0, s2 = s0;
#pragma unroll
        for (int w = 0; w < 4; ++w) {
          const u32x4 qf = *(const u32x4*)(qb + w * 1024);
          s2 = mfma(*(const u32x4*)(qb + 4096 + w * 1024), qf, s2);
          s0 = mfma(kf[w], qf, s0);
          s1 = mfma(kf[4 + w], qf, s1);
        }
        const bool hi = c16 >= 8;
        float v[6];
#pragma unroll
        for (int i = 0; i < 4; ++i) v[i] = hi ? s1[i] : s0[i];
        v[4] = g < 2 ? (hi ? s2[2] : s2[0]) : -INFINITY;
        v[5] = g < 2 ? (hi ? s2[3] : s2[1]) : -INFINITY;
        float m = vmax(vmax3(v[0], v[1], v[2]), vmax3(v[3], v[4], v[5]));
        m = xmax(m);
        m = vmax(m, ror8(m)) * C2;
#pragma unroll
        for (int i = 0; i < 6; ++i) v[i] = __builtin_amdgcn_exp2f(__builtin_fmaf(v[i], C2, -m));
        float sum = ((v[0] + v[1]) + (v[2] + v[3])) + (v[4] + v[5]);
        sum += ror8(sum);
        const float inv = __builtin_amdgcn_rcpf(xsum(sum));
        const u32x2 px = {pk(v[0] * inv, v[1] * inv) & 0x7FFF7FFFu, pk(v[2] * inv, v[3] * inv) & 0x7FFF7FFFu};
        const u32 pm = pk(v[4] * inv, v[5] * inv) & 0x7FFF7FFFu;
        __builtin_amdgcn_raw_buffer_store_b64(px, r, base + homap(lx + (c16 >> 3) * 8, flip), 0, SC1);
        if (g < 2) __builtin_amdgcn_raw_buffer_store_b32(pm, r, base + homap(lm + (c16 >> 3) * 4, flip), 0, SC1);
      }
      __builtin_amdgcn_s_setprio(0);
    } else if (wave < 3) {
      const char* qx = smem + L_QX + (s & 1) * 16384 + lane * 16;
      if (s == 0) q_load(0);
      f32x4 s0 = {0.f, 0.f, 0.f, 0.f}, s1 = s0, s2 = s0;
#pragma unroll
      for (int w = 0; w < 4; ++w) {
        s2 = mfma(*(const u32x4*)(smem + L_QM + 4096 + lane * 16 + w * 1024), kf[w], s2);
        s0 = mfma(*(const u32x4*)(qx + 8192 + w * 1024), kf[w], s0);
        s1 = mfma(*(const u32x4*)(qx + 12288 + w * 1024), kf[w], s1);
      }
      const int qi = (wave - 1) * 16 + c16;
      const float NEG = -1.0e9f * 1.4426950408889634f;
      float v[12];
#pragma unroll
      for (int i = 0; i < 4; ++i) {
        v[i] = (4 * g + i <= qi) ? s0[i] * C2 : NEG;
        v[4 + i] = (16 + 4 * g + i <= qi) ? s1[i] * C2 : NEG;
        v[8 + i] = g < 2 ? s2[i] * C2 : -INFINITY;
      }
      float m = v[0];
#pragma unroll
      for (int i = 1; i < 12; ++i) m = fmaxf(m, v[i]);
      m = xmax(m);
      float sum = 0.f;
#pragma unroll
      for (int i = 0; i < 12; ++i) { v[i] = __builtin_amdgcn_exp2f(v[i] - m); sum += v[i]; }
      const float inv = __builtin_amdgcn_rcpf(xsum(sum));
      u32x4 px; u32x2 pm;
#pragma unroll
      for (int i = 0; i < 4; ++i) px[i] = pk(v[2 * i] * inv, v[2 * i + 1] * inv) & 0x7FFF7FFFu;
#pragma unroll
      for (int i = 0; i < 2; ++i) pm[i] = pk(v[8 + 2 * i] * inv, v[9 + 2 * i] * inv) & 0x7FFF7FFFu;
      const u32 tb = PT_OFF + s * PT_STEP + ((wave - 1) * 2 + (c16 >> 3)) * 20480;
      __builtin_amdgcn_raw_buffer_store_b128(px, r, tb + lx, 0, SC1);
      if (g < 2) __builtin_amdgcn_raw_buffer_store_b64(pm, r, tb + lm, 0, SC1);
    }
    STAMP(s, 4);
    if (wave >= 1 && wave <= 3 && s + 1 < NSEG) q_load(s + 1);
    bar();
    __builtin_amdgcn_s_sleep(10);
  }
  STAMPC(65, 2); STAMP(65, 3);
}

__device__ __forceinline__ void role_b(const Args& a, int j, char* smem) {
  const int tid = threadIdx.x, lane = tid & 63, wave = __builtin_amdgcn_readfirstlane(tid >> 6);
  const int c16 = lane & 15, g = lane >> 4;
  unsigned char* ws = a.ws;
  u32* tmo = (u32*)(ws + CTL_OFF) + 64;
  __amdgpu_buffer_rsrc_t r = __builtin_amdgcn_make_buffer_rsrc(ws, 0, WS_END, 0x00020000);
  STAMP(64, 0);
  u32x4 W[2][KS];
  {
    const unsigned char* wsrc = ws + WVO_OFF + (size_t)j * WVO_J + wave * 2 * KS * 1024 + lane * 16;
#pragma unroll
    for (int tt = 0; tt < 2; ++tt)
#pragma unroll
      for (int t = 0; t < KS; ++t) W[tt][t] = *(const u32x4*)(wsrc + (tt * KS + t) * 1024);
  }
  wait_vm();
  bar();
  const u32 flips = get_flips(r, lane, tmo);
  STAMP(64, 1);
  u32x4 nx0, nx1;
  u32 fl = POISON;
  auto ux_load = [&](int seg) {
    const u32 fo = FLG_OFF + seg * 128 + wave * 16 + (lane & 3) * 4;
    u32 spins = 0;
    while (!allok(fl)) {
      fl = __builtin_amdgcn_raw_buffer_load_b32(r, fo, 0, SC1);
      if (spin_fail(spins, tmo)) break;
    }
    asm volatile("" ::: "memory");
    const u32 uo = UX_OFF + ((seg * NB + j) * 16 + wave * 2) * 1024 + lane * 16;
    nx0 = __builtin_amdgcn_raw_buffer_load_b128(r, uo, 0, SC1);
    nx1 = __builtin_amdgcn_raw_buffer_load_b128(r, uo + 1024, 0, SC1);
    fl = seg + 1 < NSEG ? __builtin_amdgcn_raw_buffer_load_b32(r, fo + 128, 0, SC1) : 0u;
  };
  const u32 lx = wave * 2048 + lane * 16;
  const u32 lm = HO_PM + (wave * 4 + (c16 >> 3)) * 128 + (g & 1) * 64 + (lane & 7) * 8;
  const bool diag = (c16 >> 3) == (g >> 1);
  const u32 redo = ((wave * 2 + (g >> 1)) * 64 + (lane & 7) * 8 + (g & 1) * 4) * 4;
  for (int s = 0; s < NSEG; ++s) {
    const u32 flip = __builtin_amdgcn_readlane(flips, s), base = HO_OFF + s * 65536;
    STAMP(s, 0);
    gather_m(r, base, flip, s == 0, smem, wave, lane, tmo);
    bar();
    STAMP(s, 1);
    u32x4 um;
    {
      f32x4 c0, c1;
      proj2<false, 512>(W[0], W[1], smem + L_M + g * 128 + (lane & 7) * 16, c0, c1);
      um = u32x4{pk(c0[0], c0[1]), pk(c0[2], c0[3]), pk(c1[0], c1[1]), pk(c1[2], c1[3])};
      if (g < 2) __builtin_amdgcn_raw_buffer_store_b128(um, r, UM_OFF + ((s * NB + j) * 8 + wave) * 512 + lane * 16, 0, SC1);
      else um = u32x4{0, 0, 0, 0};
    }
    if (s + 1 < NSEG) {
      if (s == 0) ux_load(0);
      if (s > 0) __builtin_amdgcn_s_sleep(16);
      const u32 o0 = base + homap(lx, flip), o1 = o0 + 1024, o2 = base + homap(lm, flip), o3 = o2 + 256;
      u32x4 p0, p1; u32x2 q0, q1;
      u32 spins = 0;
      for (;;) {
        p0 = __builtin_amdgcn_raw_buffer_load_b128(r, o0, 0, SC1);
        p1 = __builtin_amdgcn_raw_buffer_load_b128(r, o1, 0, SC1);
        q0 = __builtin_amdgcn_raw_buffer_load_b64(r, o2, 0, SC1);
        q1 = __builtin_amdgcn_raw_buffer_load_b64(r, o3, 0, SC1);
        if (allok(mx(mx(mx4(p0), mx4(p1)), mx(mx2(q0), mx2(q1))))) break;
        if (spin_fail(spins, tmo)) break;
      }
      STAMP(s, 3);
      f32x4 d = {0.f, 0.f, 0.f, 0.f}, e = d;
      const u32x4 pmv = {q0.x, q0.y, q1.x, q1.y};
      d = mfma(nx0, p0, d); e = mfma(nx1, p1, e); d = mfma(um, pmv, d);
      d += e;
      if (diag) *(f32x4*)(smem + L_QM + redo) = d;
    }
    wait_vm();
    bar();
    if (wave == 0 && s + 1 < NSEG) {
      const char* rp = smem + L_QM + (lane >> 5) * 2048 + (lane & 31) * 8;
      f32x2 acc = *(const f32x2*)rp;
#pragma unroll
      for (int p = 1; p < 8; ++p) acc += *(const f32x2*)(rp + p * 256);
      auto t0 = __builtin_amdgcn_permlane32_swap(__float_as_uint(acc.x), __float_as_uint(acc.x), false, false);
      auto t1 = __builtin_amdgcn_permlane32_swap(__float_as_uint(acc.y), __float_as_uint(acc.y), false, false);
      const u32 o = pks(__uint_as_float(t0[0]) + __uint_as_float(t0[1]), __uint_as_float(t1[0]) + __uint_as_float(t1[1]));
      const u32 nflip = __builtin_amdgcn_readlane(flips, s + 1);
      if (lane < 32) __builtin_amdgcn_raw_buffer_store_b32(o, r, base + 65536 + homap(HO_M + j * 128 + lane * 4, nflip), 0, SC1);
    }
    STAMP(s, 4);
    if (tid == 64) __builtin_amdgcn_raw_buffer_store_b32(1u, r, FLM_OFF + s * 3072 + (j / 6) * 128 + (j % 6) * 4, 0, SC1);
    if (s + 2 < NSEG) ux_load(s + 1);
    STAMP(s, 5);
  }
  STAMP(65, 0);
}

__device__ __forceinline__ void role_o(const Args& a, int o24, char* smem) {
  const int tid = threadIdx.x, lane = tid & 63, wave = __builtin_amdgcn_readfirstlane(tid >> 6);
  const int c16 = lane & 15, g = lane >> 4, o = o24 >> 1;
  unsigned char* ws = a.ws;
  u32* tmo = (u32*)(ws + CTL_OFF) + 64;
  __amdgpu_buffer_rsrc_t r = __builtin_amdgcn_make_buffer_rsrc(ws, 0, WS_END, 0x00020000);
  STAMP(64, 0);
  if (wave < 3 && o24 * 3 + wave < NSEG) {
    const int blk = o24 * 3 + wave;
    u32 best[2] = {~0u, ~0u};
    for (int rep = 0; rep < 4; ++rep)
      for (int f = 0; f < 2; ++f) {
        u32* p = (u32*)(ws + HO_OFF + blk * 65536 + homap(32768 - 128 * (rep + 1), f * 8192));
        __hip_atomic_store(p, POISON, __ATOMIC_RELAXED, __HIP_MEMORY_SCOPE_AGENT);
        wait_vm();
        u64 t0, t1;
        asm volatile("s_memtime %0\n\ts_waitcnt lgkmcnt(0)" : "=s"(t0) :: "memory");
        u32 v = __hip_atomic_load(p, __ATOMIC_RELAXED, __HIP_MEMORY_SCOPE_AGENT);
        asm volatile("s_waitcnt vmcnt(0)" : "+v"(v) :: "memory");
        asm volatile("s_memtime %0\n\ts_waitcnt lgkmcnt(0)" : "=s"(t1) :: "memory");
        const u32 d = (u32)(t1 - t0);
        best[f] = d < best[f] ? d : best[f];
      }
    if (lane == 0) __builtin_amdgcn_raw_buffer_store_b32(best[1] < best[0] ? 8192u : 0u, r, FLP_OFF + blk * 4, 0, SC1);
  }
  STAMP(64, 1);
  const u32 lx = wave * 2048 + lane * 16;
  const u32 lm = HO_PM + (wave * 4 + (c16 >> 3)) * 128 + (g & 1) * 64 + (lane & 7) * 8;
  const bool diag = (c16 >> 3) == (g >> 1);
  const u32 redo = ((wave * 2 + (g >> 1)) * 64 + (lane & 7) * 8 + (g & 1) * 4) * 4;
  u32x4 x0[6], x1[6], um[6];
  auto ux_load = [&](int s) {
    u32 spins = 0;
    while (!allok(__builtin_amdgcn_raw_buffer_load_b32(r, FLG_OFF + s * 128 + wave * 16 + (lane & 3) * 4, 0, SC1))) {
      if (spin_fail(spins, tmo)) break;
      __builtin_amdgcn_s_sleep(16);
    }
    asm volatile("" ::: "memory");
#pragma unroll
    for (int c = 0; c < 6; ++c) {
      const u32 uo = UX_OFF + ((s * NB + o * 6 + c) * 16 + wave * 2) * 1024 + lane * 16;
      x0[c] = __builtin_amdgcn_raw_buffer_load_b128(r, uo, 0, SC1);
      x1[c] = __builtin_amdgcn_raw_buffer_load_b128(r, uo + 1024, 0, SC1);
    }
  };
  const u32 e0 = (((tid & 63) >> 1) * DM + (o * 6 + (tid >> 6)) * 8 + (tid & 1) * 4) * 2;
  u32x2 xv = {0, 0}, fv = {0, 0};
  ux_load(o24 & 1);
  for (int s = o24 & 1; s < NSEG; s += 2) {
    u32 spins = 0;
    if (wave == 0) {
      const u32 fo = FLM_OFF + s * 3072 + o * 128 + (lane % 6) * 4;
      while (!allok(__builtin_amdgcn_raw_buffer_load_b32(r, fo, 0, SC1))) {
        if (spin_fail(spins, tmo)) break;
        __builtin_amdgcn_s_sleep(4);
      }
    }
    if (s - 2 < SPLIT) wait_vm();
    bar();
    if (tid == 0 && s >= 2 && s - 2 < SPLIT) __builtin_amdgcn_raw_buffer_store_b32(1u, r, FLA_OFF + (s - 2) * 128 + o * 4, 0, SC1);
    const u32 eo = e0 + s * 32 * DM * 2;
#pragma unroll
    for (int c = 0; c < 6; ++c)
      um[c] = __builtin_amdgcn_raw_buffer_load_b128(r, UM_OFF + ((s * NB + o * 6 + c) * 8 + wave) * 512 + (lane & 31) * 16, 0, SC1);
    u32x4 p0[4], p1[4]; u32x2 q0[4], q1[4];
    for (;;) {
      u32 m = 0;
#pragma unroll
      for (int u = 0; u < 4; ++u) {
        const u32 tb = PT_OFF + s * PT_STEP + u * 20480;
        p0[u] = __builtin_amdgcn_raw_buffer_load_b128(r, tb + lx, 0, SC1);
        p1[u] = __builtin_amdgcn_raw_buffer_load_b128(r, tb + lx + 1024, 0, SC1);
        q0[u] = __builtin_amdgcn_raw_buffer_load_b64(r, tb + lm, 0, SC1);
        q1[u] = __builtin_amdgcn_raw_buffer_load_b64(r, tb + lm + 256, 0, SC1);
      }
#pragma unroll
      for (int u = 0; u < 4; ++u) m = mx(m, mx(mx(mx4(p0[u]), mx4(p1[u])), mx(mx2(q0[u]), mx2(q1[u]))));
      if (allok(m)) break;
      if (spin_fail(spins, tmo)) break;
    }
#pragma unroll
    for (int c = 0; c < 6; ++c) {
      if (g >= 2) um[c] = u32x4{0, 0, 0, 0};
#pragma unroll
      for (int u = 0; u < 4; ++u) {
        f32x4 d = {0.f, 0.f, 0.f, 0.f};
        const u32x4 pmv = {q0[u].x, q0[u].y, q1[u].x, q1[u].y};
        d = mfma(x0[c], p0[u], d); d = mfma(x1[c], p1[u], d); d = mfma(um[c], pmv, d);
        if (diag) *(f32x4*)(smem + (c * 4 + u) * 4096 + redo) = d;
      }
    }
    bar();
    if (tid < 384) {
      const char* rp = smem + (tid >> 4) * 4096 + (tid & 15) * 16;
      f32x4 acc = *(const f32x4*)rp;
#pragma unroll
      for (int p = 1; p < 16; ++p) acc += *(const f32x4*)(rp + p * 256);
      const u32x2 av = {pk(acc[0], acc[1]), pk(acc[2], acc[3])};
      if (s < SPLIT) __builtin_amdgcn_raw_buffer_store_b64(av, r, AO_OFF + eo, 0, SC1);
      else *(u32x2*)((char*)a.out + eo) = u32x2{fin(xv.x, av.x, fv.x), fin(xv.y, av.y, fv.y)};
      if (s + 2 >= SPLIT && s + 2 < NSEG) {
        while (!allok(__builtin_amdgcn_raw_buffer_load_b32(r, FLF_OFF + (((s + 2) >> 2) * 6 + (o >> 1)) * 128, 0, SC1))) {
          if (spin_fail(spins, tmo)) break;
          __builtin_amdgcn_s_sleep(16);
        }
        asm volatile("" ::: "memory");
        xv = *(const u32x2*)((const char*)a.x + eo + 64 * DM * 2);
        fv = __builtin_amdgcn_raw_buffer_load_b64(r, FO_OFF + eo + 64 * DM * 2, 0, SC1);
      }
    }
    if (s + 2 < NSEG) ux_load(s + 2);
    STAMP(s, 0);
  }
}

__device__ __forceinline__ void role_v(const Args& a, int h, char* smem) {
  const int tid = threadIdx.x, lane = tid & 63, wave = __builtin_amdgcn_readfirstlane(tid >> 6);
  const int c16 = lane & 15, g = lane >> 4;
  unsigned char* ws = a.ws;
  __amdgpu_buffer_rsrc_t r = __builtin_amdgcn_make_buffer_rsrc(ws, 0, WS_END, 0x00020000);
  STAMP(64, 0);
  u32x4 WV[KS], WO[5][4];
  {
    const u32 sel = (c16 & 1) ? 0x07060302u : 0x05040100u;
    u32x4 st[18];
#pragma unroll
    for (int i = 0; i < 18; ++i) { const int pc = i * 512 + tid; st[i] = *(const u32x4*)(a.wv + (size_t)(pc >> 4) * (NH * DH) + h * DH + (pc & 15) * 8); }
#pragma unroll
    for (int i = 0; i < 18; ++i) { const int pc = i * 512 + tid; *(u32x4*)(smem + (pc >> 4) * 272 + (pc & 15) * 16) = st[i]; }
#pragma unroll
    for (int i = 0; i < 18; ++i) st[i] = *(const u32x4*)(a.wo + (size_t)h * DH * DM + (i * 512 + tid) * 8);
    bar();
    {
      const char* wl = smem + g * 8 * 272 + wave * 32 + (c16 >> 1) * 4;
#pragma unroll
      for (int t = 0; t < KS; ++t) {
#pragma unroll
        for (int p = 0; p < 4; ++p)
          WV[t][p] = __builtin_amdgcn_perm(*(const u32*)(wl + (t * 32 + 2 * p + 1) * 272), *(const u32*)(wl + (t * 32 + 2 * p) * 272), sel);
        asm volatile("" : "+v"(WV[t]) :: "memory");
      }
    }
    bar();
#pragma unroll
    for (int i = 0; i < 18; ++i) { const int pc = i * 512 + tid; *(u32x4*)(smem + (pc / 72) * 1168 + (pc % 72) * 16) = st[i]; }
    bar();
#pragma unroll
    for (int i = 0; i < 5; ++i) {
      const int ct = wave + 8 * i < 36 ? wave + 8 * i : 0;
      const char* wl = smem + g * 4 * 1168 + ct * 32 + (c16 >> 1) * 4;
#pragma unroll
      for (int es = 0; es < 4; ++es) {
#pragma unroll
        for (int p = 0; p < 4; ++p) {
          const int e0 = 32 * es + 16 * (p >> 1) + 2 * (p & 1);
          WO[i][es][p] = __builtin_amdgcn_perm(*(const u32*)(wl + (e0 + 1) * 1168), *(const u32*)(wl + e0 * 1168), sel);
        }
        asm volatile("" : "+v"(WO[i][es]) :: "memory");
      }
    }
    bar();
  }
  STAMP(64, 1);
  stage_x(ws, 0, wave, lane);
  for (int sg = 0; sg < NSEG; ++sg) {
    wait_vm();
    bar();
    if (sg > 0 && tid == 0) __builtin_amdgcn_raw_buffer_store_b32(1u, r, FLG_OFF + (sg - 1) * 128 + h * 4, 0, SC1);
    {
      const char* xb = smem + L_X + (sg & 1) * XF_SEG + lane * 16;
      f32x4 c0 = {0.f, 0.f, 0.f, 0.f}, c1 = c0;
#pragma unroll
      for (int t = 0; t < KS; ++t) {
        c0 = mfma(WV[t], *(const u32x4*)(xb + t * 1024), c0);
        c1 = mfma(WV[t], *(const u32x4*)(xb + (KS + t) * 1024), c1);
      }
      __builtin_amdgcn_sched_group_barrier(0x100, 8, 0);
#pragma unroll
      for (int t = 0; t < KS - 4; ++t) { __builtin_amdgcn_sched_group_barrier(0x008, 2, 0); __builtin_amdgcn_sched_group_barrier(0x100, 2, 0); }
      __builtin_amdgcn_sched_group_barrier(0x008, 8, 0);
      if (sg + 1 < NSEG) stage_x(ws, sg + 1, wave, lane);
      char* vt = smem + L_QM + (wave >> 1) * 1024 + lane * 16 + (wave & 1) * 8;
      *(u32x2*)vt = u32x2{pk(c0[0], c0[1]), pk(c0[2], c0[3])};
      *(u32x2*)(vt + 4096) = u32x2{pk(c1[0], c1[1]), pk(c1[2], c1[3])};
    }
    bar();
    u32x4 vf[2][4];
#pragma unroll
    for (int rt = 0; rt < 2; ++rt)
#pragma unroll
      for (int es = 0; es < 4; ++es) vf[rt][es] = *(const u32x4*)(smem + L_QM + rt * 4096 + es * 1024 + lane * 16);
#pragma unroll
    for (int i = 0; i < 5; ++i) {
      const int ct = wave + 8 * i;
      if (ct < 36) {
        f32x4 d0 = {0.f, 0.f, 0.f, 0.f}, d1 = d0;
#pragma unroll
        for (int es = 0; es < 4; ++es) { d0 = mfma(vf[0][es], WO[i][es], d0); d1 = mfma(vf[1][es], WO[i][es], d1); }
        const u32x4 o = {pk(d0[0], d0[1]), pk(d0[2], d0[3]), pk(d1[0], d1[1]), pk(d1[2], d1[3])};
        __builtin_amdgcn_raw_buffer_store_b128(o, r, UX_OFF + ((sg * NB + ct * 2 + (c16 >> 3)) * 16 + (h >> 1)) * 1024 + (g * 16 + (h & 1) * 8 + (lane & 7)) * 16, 0, SC1);
      }
    }
    if (sg == 0) STAMP(64, 2);
  }
  wait_vm();
  bar();
  if (tid == 0) __builtin_amdgcn_raw_buffer_store_b32(1u, r, FLG_OFF + (NSEG - 1) * 128 + h * 4, 0, SC1);
  STAMP(64, 3);
}

__device__ __forceinline__ float gelu_tanh(float x) {
  const float u = 0.7978845608028654f * (x + 0.044715f * x * x * x);
  const float e = __expf(2.f * u);
  return 0.5f * x * (2.f - 2.f / (e + 1.f));
}

__device__ __forceinline__ void role_f(const Args& a, int f, char* smem) {
  const int tid = threadIdx.x, lane = tid & 63, wave = __builtin_amdgcn_readfirstlane(tid >> 6);
  const int c16 = lane & 15, g = lane >> 4;
  unsigned char* ws = a.ws;
  u32* tmo = (u32*)(ws + CTL_OFF) + 64;
  u32* cnt = (u32*)(ws + CTL_OFF) + 128;
  __amdgpu_buffer_rsrc_t r = __builtin_amdgcn_make_buffer_rsrc(ws, 0, WS_END, 0x00020000);
  STAMP(64, 0);
  auto stage = [&](int sg) {
#pragma unroll
    for (int i = 0; i < 5; ++i) {
      const int p = min(i * 8 + wave, 35);
      glds16(ws + XF_OFF + sg * XF_SEG + p * 1024 + lane * 16, (sg & 3) * XF_SEG + p * 1024);
    }
  };
  if (f >= 64) {
    const int h = f - 64;
    u32x4 WW[2][KS];
    load_wqk(a, h, WW, smem);
    wait_vm();
    stage(0); stage(1);
    for (int sg = 0; sg < NSEG; ++sg) {
      if (sg < 4) wait_vm();
      else asm volatile("s_waitcnt vmcnt(5)" ::: "memory");
      bar();
      if (sg >= 1 && tid == 0) __builtin_amdgcn_raw_buffer_store_b32(1u, r, FLQ_OFF + (sg < 4 ? sg - 1 : sg - 2) * 128 + h * 4, 0, SC1);
#pragma unroll
      for (int rt = 0; rt < 2; ++rt) {
        f32x4 c0, c1;
        proj2<true, 1024>(WW[0], WW[1], smem + (sg & 3) * XF_SEG + rt * KS * 1024 + lane * 16, c0, c1);
        if (rt == 0) stage(min(sg + 2, NSEG - 1));
        const u32x4 o = {pk(c0[0], c1[0]), pk(c0[1], c1[1]), pk(c0[2], c1[2]), pk(c0[3], c1[3])};
        __builtin_amdgcn_raw_buffer_store_b128(o, r, QX_OFF + ((sg * NH + h) * 16 + (wave >> 2) * 8 + rt * 4 + (wave & 3)) * 1024 + lane * 16, 0, SC1);
      }
    }
    wait_vm();
    bar();
    if (tid < 2) __builtin_amdgcn_raw_buffer_store_b32(1u, r, FLQ_OFF + (NSEG - 2 + tid) * 128 + h * 4, 0, SC1);
  }
  for (int it = f * 41 + wave; it < min(f * 41 + 41, 2592) && f < 64; it += 8) {
    const int ks = it / 18, bb = it % 18;
    u32 d[8];
#pragma unroll
    for (int jj = 0; jj < 8; ++jj)
      d[jj] = *(const u32*)(a.wd + (size_t)(ks * 32 + (jj >> 2) * 16 + g * 4 + (jj & 3)) * DM + bb * 32 + c16 * 2);
    const u32x4 lo = {plo(d[0], d[1]), plo(d[2], d[3]), plo(d[4], d[5]), plo(d[6], d[7])};
    const u32x4 hi = {phi(d[0], d[1]), phi(d[2], d[3]), phi(d[4], d[5]), phi(d[6], d[7])};
    __builtin_amdgcn_raw_buffer_store_b128(lo, r, WDF_OFF + (bb * 144 + ks) * 2048 + lane * 16, 0, SC1);
    __builtin_amdgcn_raw_buffer_store_b128(hi, r, WDF_OFF + (bb * 144 + ks) * 2048 + 1024 + lane * 16, 0, SC1);
  }
#pragma unroll 1
  for (int job = 0; job < 3 && f < 64; ++job) {
    int tid = threadIdx.x;
    asm volatile("" : "+v"(tid));
    const int hv = wave & 3, rt = wave >> 2;
    const int u0 = f * 72, sl = (u0 >> 6) + job;
    const int q0 = job == 0 ? (u0 & 63) : 0, q1 = min(u0 + 72 - sl * 64, 64);
    if (q1 <= q0) break;
    u32x4 WW[2][KS];
#pragma unroll
    for (int mat = 0; mat < 2; ++mat) {
      __syncthreads();
      u32x4 st[9];
#pragma unroll
      for (int i = 0; i < 9; ++i) {
        const int pc = i * 512 + tid;
        st[i] = *(const u32x4*)((mat ? a.wu : a.wg) + (size_t)(pc >> 3) * FF + sl * 64 + (pc & 7) * 8);
      }
#pragma unroll
      for (int i = 0; i < 9; ++i) { const int pc = i * 512 + tid; *(u32x4*)(smem + (pc >> 3) * 144 + (pc & 7) * 16) = st[i]; }
      __syncthreads();
      const char* wl = smem + g * 8 * 144 + hv * 32 + (c16 >> 1) * 4;
      const u32 sel = (c16 & 1) ? 0x07060302u : 0x05040100u;
#pragma unroll
      for (int t = 0; t < KS; ++t) {
#pragma unroll
        for (int p = 0; p < 4; ++p) {
          const int o = (t * 32 + 2 * p) * 144;
          WW[mat][t][p] = __builtin_amdgcn_perm(*(const u32*)(wl + o + 144), *(const u32*)(wl + o), sel);
        }
        asm volatile("" : "+v"(WW[mat][t]) :: "memory");
      }
    }
    __syncthreads();
    if (job == 0) STAMP(64, 1);
    stage(q0); stage(min(q0 + 1, q1 - 1));
    for (int sg = q0; sg < q1; ++sg) {
      asm volatile("s_waitcnt vmcnt(5)" ::: "memory");
      bar();
      f32x4 cg, cu;
      proj2<true, 1024>(WW[0], WW[1], smem + (sg & 3) * XF_SEG + rt * KS * 1024 + lane * 16, cg, cu);
      stage(min(sg + 2, q1 - 1));
      float hh[4];
#pragma unroll
      for (int i = 0; i < 4; ++i) hh[i] = bfr(gelu_tanh(bfr(cg[i]))) * bfr(cu[i]);
      const u32x2 o = {pk(hh[0], hh[1]), pk(hh[2], hh[3])};
      __builtin_amdgcn_raw_buffer_store_b64(o, r, H_OFF + ((sg * 2 + rt) * 144 + sl * 2 + (hv >> 1)) * 1024 + lane * 16 + (hv & 1) * 8, 0, 0);
    }
    wait_vm();
  }
  wait_vm();
  __syncthreads();
  STAMP(64, 2);
  if (tid == 0) {
    if (f < 64) {
      __builtin_amdgcn_fence(__ATOMIC_RELEASE, "agent");
      wait_vm();
      __hip_atomic_fetch_add(cnt, 1u, __ATOMIC_RELAXED, __HIP_MEMORY_SCOPE_AGENT);
    }
    u32 spins = 0;
    while (__hip_atomic_load(cnt, __ATOMIC_RELAXED, __HIP_MEMORY_SCOPE_AGENT) < 64u) {
      __builtin_amdgcn_s_sleep(8);
      if (spin_fail(spins, tmo)) break;
    }
    __builtin_amdgcn_fence(__ATOMIC_ACQUIRE, "agent");
    wait_vm();
  }
  __syncthreads();
  STAMP(64, 3);
  {
    const int ft = (f + 32) % NF;
    const int tm = ft / 6, b3 = (ft % 6) * 3, tg = wave & 3, kh = wave >> 2;
    const unsigned char* hb = ws + H_OFF + ((size_t)(tm * 8 + tg * 2) * 144 + kh * 72) * 1024 + lane * 16;
    const unsigned char* wb = ws + WDF_OFF + (size_t)(b3 * 144 + kh * 72) * 2048 + lane * 16;
    f32x4 acc[2][6];
#pragma unroll
    for (int u = 0; u < 2; ++u)
#pragma unroll
      for (int i = 0; i < 6; ++i) acc[u][i] = f32x4{0.f, 0.f, 0.f, 0.f};
    u32x4 hf[2][2][2], wf[2][2][6];
    auto ld = [&](int st, int t0) {
#pragma unroll
      for (int t = 0; t < 2; ++t) {
        hf[st][t][0] = *(const u32x4*)(hb + (t0 + t) * 1024); hf[st][t][1] = *(const u32x4*)(hb + (144 + t0 + t) * 1024);
#pragma unroll
        for (int i = 0; i < 6; ++i) wf[st][t][i] = *(const u32x4*)(wb + ((i >> 1) * 144 + t0 + t) * 2048 + (i & 1) * 1024);
      }
    };
    auto mm = [&](int st) {
#pragma unroll
      for (int t = 0; t < 2; ++t)
#pragma unroll
        for (int i = 0; i < 6; ++i) { acc[0][i] = mfma(wf[st][t][i], hf[st][t][0], acc[0][i]); acc[1][i] = mfma(wf[st][t][i], hf[st][t][1], acc[1][i]); }
    };
    ld(0, 0);
#pragma unroll 1
    for (int t0 = 0; t0 < 72; t0 += 4) {
      ld(1, t0 + 2);
      __builtin_amdgcn_sched_barrier(0);
      mm(0);
      __builtin_amdgcn_sched_barrier(0);
      ld(0, min(t0 + 4, 70));
      __builtin_amdgcn_sched_barrier(0);
      mm(1);
      __builtin_amdgcn_sched_barrier(0);
    }
    STAMP(64, 4);
    if (kh == 1) {
#pragma unroll
      for (int u = 0; u < 2; ++u)
#pragma unroll
        for (int i = 0; i < 6; ++i) *(f32x4*)(smem + ((tg * 12 + u * 6 + i) * 64 + lane) * 16) = acc[u][i];
    }
    const bool mine = tm * 4 < SPLIT;
    if (mine && wave == 0) {
      u32 spins = 0;
      while (!allok(__builtin_amdgcn_raw_buffer_load_b32(r, FLA_OFF + (tm * 4 + ((lane >> 1) & 3)) * 128 + ((ft % 6) * 2 + (lane & 1)) * 4, 0, SC1))) {
        if (spin_fail(spins, tmo)) break;
        __builtin_amdgcn_s_sleep(16);
      }
    }
    __syncthreads();
    if (kh == 0) {
#pragma unroll
      for (int u = 0; u < 2; ++u)
#pragma unroll
        for (int b = 0; b < 3; ++b) {
          const f32x4 e0 = acc[u][2 * b] + *(const f32x4*)(smem + ((tg * 12 + u * 6 + 2 * b) * 64 + lane) * 16);
          const f32x4 e1 = acc[u][2 * b + 1] + *(const f32x4*)(smem + ((tg * 12 + u * 6 + 2 * b + 1) * 64 + lane) * 16);
          const u32x4 o = {pk(e0[0], e1[0]), pk(e0[1], e1[1]), pk(e0[2], e1[2]), pk(e0[3], e1[3])};
          const u32 eo = ((tm * 128 + tg * 32 + u * 16 + c16) * DM + (b3 + b) * 32 + g * 8) * 2;
          if (mine) {
            const u32x4 xv = *(const u32x4*)((const char*)a.x + eo), av = __builtin_amdgcn_raw_buffer_load_b128(r, AO_OFF + eo, 0, SC1);
            *(u32x4*)((char*)a.out + eo) = u32x4{fin(xv.x, av.x, o.x), fin(xv.y, av.y, o.y), fin(xv.z, av.z, o.z), fin(xv.w, av.w, o.w)};
          } else {
            __builtin_amdgcn_raw_buffer_store_b128(o, r, FO_OFF + eo, 0, SC1);
          }
        }
    }
    wait_vm();
    __syncthreads();
    if (!mine && tid == 0) __builtin_amdgcn_raw_buffer_store_b32(1u, r, FLF_OFF + ft * 128, 0, SC1);
  }
}

__global__ void __launch_bounds__(512, 2) k2_main(Args a) {
  extern __shared__ __attribute__((aligned(16))) char smem[];
  if (threadIdx.x == 0) {
    u32* ctl = (u32*)(a.ws + CTL_OFF);
    const u32 x = __builtin_amdgcn_s_getreg((4 - 1) << 11 | 0 << 6 | 20) & 7u;
    const u32 rank = __hip_atomic_fetch_add(ctl + 256 + x * 32, 1u, __ATOMIC_RELAXED, __HIP_MEMORY_SCOPE_AGENT);
    u32 slot = rank * 8 + x;
    if (rank >= 32) {
      u32 k = __hip_atomic_fetch_add(ctl + 192, 1u, __ATOMIC_RELAXED, __HIP_MEMORY_SCOPE_AGENT), spins = 0;
      for (;;) {
        u32 tot = 0;
        for (int i = 0; i < 8; ++i) tot += __hip_atomic_load(ctl + 256 + i * 32, __ATOMIC_RELAXED, __HIP_MEMORY_SCOPE_AGENT);
        if (tot >= 256u || spin_fail(spins, ctl + 64)) break;
      }
      slot = 256;
      for (int i = 0; i < 8 && slot == 256; ++i) {
        const u32 c = __hip_atomic_load(ctl + 256 + i * 32, __ATOMIC_RELAXED, __HIP_MEMORY_SCOPE_AGENT), fr = c < 32u ? 32u - c : 0u;
        if (k < fr) slot = (c + k) * 8 + i; else k -= fr;
      }
    }
    *(u32*)smem = slot;
  }
  __syncthreads();
  const int b = __builtin_amdgcn_readfirstlane(*(const u32*)smem), rr = b & 7, q = b >> 3;
  __syncthreads();
  if (b >= 256) return;
  if (rr == 0) role_a(a, q, smem);
  else if (rr < 4 && q < 24) role_b(a, (rr - 1) * 24 + q, smem);
  else if (rr == 4) role_v(a, q, smem);
  else if (rr > 4) role_f(a, (rr - 5) * 32 + q, smem);
  else role_o(a, (rr - 1) * 8 + (q - 24), smem);
  DUMP();
}

extern "C" void kernel_launch(void* const* d_in, const int* in_sizes, int n_in, void* d_out, int out_size, void* d_ws,
                              size_t ws_size, hipStream_t stream) {
  Args a;
  a.x = (const u16*)d_in[0]; a.mem0 = (const u16*)d_in[1]; a.wq = (const u16*)d_in[2]; a.wk = (const u16*)d_in[3];
  a.wv = (const u16*)d_in[4]; a.wo = (const u16*)d_in[5]; a.wg = (const u16*)d_in[8]; a.wu = (const u16*)d_in[9];
  a.wd = (const u16*)d_in[10];
  a.ws = (unsigned char*)(((uintptr_t)d_ws + 65535) & ~(uintptr_t)65535);
  a.out = (u16*)d_out;
  (void)hipFuncSetAttribute((const void*)k2_main, hipFuncAttributeMaxDynamicSharedMemorySize, LDS_BYTES);
  k1_prep<<<K1_GRID, 256, 0, stream>>>(a);
  k2_main<<<256, 512, LDS_BYTES, stream>>>(a);
}
```

```cpp
#include <hip/hip_runtime.h>
#include <hip/hip_bf16.h>

typedef unsigned u32;
typedef unsigned long long u64;
typedef unsigned short u16;
typedef __attribute__((ext_vector_type(8))) __bf16 bf16x8;
typedef __attribute__((ext_vector_type(2))) __bf16 bf16x2;
typedef __attribute__((ext_vector_type(4))) float f32x4;
typedef __attribute__((ext_vector_type(2))) float f32x2;
typedef __attribute__((ext_vector_type(4))) unsigned u32x4;
typedef __attribute__((ext_vector_type(2))) unsigned u32x2;

constexpr int S_ = 2048, DM = 576, NH = 32, DH = 128, FF = 4608, NSEG = 64, KS = 18;
constexpr int NB = 72, NF = 96;
constexpr u32 HO_OFF = 0;
constexpr u32 PT_OFF = HO_OFF + NSEG * 65536;
constexpr u32 PT_STEP = 4 * 20480;
constexpr u32 FLG_OFF = PT_OFF + NSEG * PT_STEP;
constexpr u32 FLM_OFF = FLG_OFF + NSEG * 128;
constexpr u32 FLQ_OFF = FLM_OFF + NSEG * 3072;
constexpr u32 FLA_OFF = FLQ_OFF + NSEG * 128;
constexpr u32 FLF_OFF = FLA_OFF + NSEG * 128;
constexpr int SPLIT = 56;
constexpr u32 FLP_OFF = FLF_OFF + NF * 128;
constexpr u32 POISON_BYTES = FLP_OFF + 256;
constexpr u32 CTL_OFF = (POISON_BYTES + 4095) & ~4095u;
constexpr u32 M0_OFF = CTL_OFF + 4096;
constexpr u32 XF_OFF = M0_OFF + 12288;
constexpr u32 XF_SEG = 2 * KS * 1024;
constexpr u32 WVO_OFF = XF_OFF + NSEG * XF_SEG;
constexpr u32 WVO_J = 16 * KS * 1024;
constexpr u32 UX_OFF = WVO_OFF + NB * WVO_J;
constexpr u32 UM_OFF = UX_OFF + NSEG * NB * 16384;
constexpr u32 QX_OFF = UM_OFF + NSEG * NB * 4096;
constexpr u32 H_OFF = QX_OFF + NSEG * NH * 16384;
constexpr u32 WDF_OFF = H_OFF + 128 * 144 * 1024;
constexpr u32 FO_OFF = WDF_OFF + 9 * 144 * 4096;
constexpr u32 AO_OFF = FO_OFF + S_ * DM * 2;
constexpr u32 WS_END = AO_OFF + S_ * DM * 2;
constexpr u32 POISON = 0xFFFFFFFFu;
constexpr int SC1 = 16;
constexpr u32 SPIN_CAP = 400000u;
constexpr int LDS_BYTES = 160 * 1024;

__device__ __forceinline__ u32 pk(float lo, float hi) {
  f32x2 v = {lo, hi};
  return __builtin_bit_cast(u32, __builtin_convertvector(v, bf16x2));
}
__device__ __forceinline__ u32 pks(float lo, float hi) { u32 r = pk(lo, hi); return r == POISON ? 0x7FC07FC0u : r; }
__device__ __forceinline__ float bfr(float x) { return (float)(__bf16)x; }
__device__ __forceinline__ float bflo(u32 w) { return __uint_as_float(w << 16); }
__device__ __forceinline__ float bfhi(u32 w) { return __uint_as_float(w & 0xFFFF0000u); }
__device__ __forceinline__ u32 fin(u32 x, u32 p, u32 f) {
  return pk(bfr(bflo(x) + bflo(p)) + bflo(f), bfr(bfhi(x) + bfhi(p)) + bfhi(f));
}
__device__ __forceinline__ f32x4 mfma(u32x4 a, u32x4 b, f32x4 c) {
  return __builtin_amdgcn_mfma_f32_16x16x32_bf16(__builtin_bit_cast(bf16x8, a), __builtin_bit_cast(bf16x8, b), c, 0, 0, 0);
}
__device__ __forceinline__ u32 plo(u32 a, u32 b) { return __builtin_amdgcn_perm(b, a, 0x05040100u); }
__device__ __forceinline__ u32 phi(u32 a, u32 b) { return __builtin_amdgcn_perm(b, a, 0x07060302u); }
__device__ __forceinline__ float vmax(float a, float b) { float r; asm("v_max_f32 %0, %1, %2" : "=v"(r) : "v"(a), "v"(b)); return r; }
__device__ __forceinline__ float vmax3(float a, float b, float c) { float r; asm("v_max3_f32 %0, %1, %2, %3" : "=v"(r) : "v"(a), "v"(b), "v"(c)); return r; }
__device__ __forceinline__ float xmax(float x) {
  auto s = __builtin_amdgcn_permlane16_swap(__float_as_uint(x), __float_as_uint(x), false, false);
  x = vmax(__uint_as_float(s[0]), __uint_as_float(s[1]));
  auto t = __builtin_amdgcn_permlane32_swap(__float_as_uint(x), __float_as_uint(x), false, false);
  return vmax(__uint_as_float(t[0]), __uint_as_float(t[1]));
}
__device__ __forceinline__ float xsum(float x) {
  auto s = __builtin_amdgcn_permlane16_swap(__float_as_uint(x), __float_as_uint(x), false, false);
  x = __uint_as_float(s[0]) + __uint_as_float(s[1]);
  auto t = __builtin_amdgcn_permlane32_swap(__float_as_uint(x), __float_as_uint(x), false, false);
  return __uint_as_float(t[0]) + __uint_as_float(t[1]);
}
__device__ __forceinline__ float ror8(float x) {
  return __builtin_bit_cast(float, __builtin_amdgcn_mov_dpp(__builtin_bit_cast(int, x), 0x128, 0xf, 0xf, true));
}
__device__ __forceinline__ u32 mx(u32 a, u32 b) { return a > b ? a : b; }
__device__ __forceinline__ u32 mx4(u32x4 v) { return mx(mx(v.x, v.y), mx(v.z, v.w)); }
__device__ __forceinline__ u32 mx2(u32x2 v) { return mx(v.x, v.y); }
__device__ __forceinline__ bool allok(u32 m) { return __all(m != POISON); }
__device__ __forceinline__ void glds16(const void* g, u32 lds) {
  u32 keep;
  asm volatile("s_mov_b32 %0, m0\n\ts_mov_b32 m0, %2\n\ts_nop 0\n\tglobal_load_lds_dwordx4 %1, off\n\ts_mov_b32 m0, %0"
               : "=&s"(keep) : "v"(g), "s"(lds) : "memory");
}
__device__ __forceinline__ void wait_vm() { asm volatile("s_waitcnt vmcnt(0)" ::: "memory"); }
__device__ __forceinline__ void bar() {
  asm volatile("s_waitcnt lgkmcnt(0)" ::: "memory");
  __builtin_amdgcn_s_barrier();
  asm volatile("" ::: "memory");
}
__device__ __forceinline__ bool spin_fail(u32& spins, u32* tmo) {
  ++spins;
  if (spins > SPIN_CAP) { __hip_atomic_store(tmo, 1u, __ATOMIC_RELAXED, __HIP_MEMORY_SCOPE_AGENT); return true; }
  if ((spins & 1023u) == 0u && __hip_atomic_load(tmo, __ATOMIC_RELAXED, __HIP_MEMORY_SCOPE_AGENT) != 0u) return true;
  return false;
}
__device__ __forceinline__ u32 homap(u32 lo, u32 flip) { return ((lo & ~8191u) << 1) + (lo & 8191u) + flip; }
constexpr u32 HO_PM = 16384, HO_M = 20480;

struct Args {
  const u16* x; const u16* mem0; const u16* wq; const u16* wk; const u16* wv; const u16* wo;
  const u16* wg; const u16* wu; const u16* wd; unsigned char* ws; u16* out;
};

constexpr int K1_WVO = 2592, K1_LN = 256, K1_LNB = K1_LN / 4, K1_JOBS = K1_WVO + K1_LN, K1_GRID = K1_JOBS / 4 + 8;
__global__ void __launch_bounds__(256) k1_prep(Args a) {
  __shared__ __attribute__((aligned(16))) char tbuf[16384];
  const int lane = threadIdx.x & 63, wave = __builtin_amdgcn_readfirstlane(threadIdx.x >> 6);
  const int b = blockIdx.x;
  unsigned char* ws = a.ws;
  const int c16 = lane & 15, g = lane >> 4;
  if (b == K1_GRID - 1) {
    for (int i = threadIdx.x; i < 1024; i += 256) ((u32*)(ws + CTL_OFF))[i] = 0u;
    for (int i = threadIdx.x; i < 576; i += 256) {
      u32x4 v = *(const u32x4*)(a.mem0 + (i & 7) * DM + (i >> 3) * 8);
      if (v.x == POISON) v.x = 0x7FC07FC0u; if (v.y == POISON) v.y = 0x7FC07FC0u;
      if (v.z == POISON) v.z = 0x7FC07FC0u; if (v.w == POISON) v.w = 0x7FC07FC0u;
      *(u32x4*)(ws + M0_OFF + i * 16) = v;
    }
    return;
  }
  if (b >= K1_JOBS / 4) return;
  const int job = b * 4 + wave;
  auto poison = [&]() {
    const u32x4 pv = {POISON, POISON, POISON, POISON};
    for (u32 o = (job * 64 + lane) * 16; o < POISON_BYTES; o += K1_JOBS * 64 * 16) *(u32x4*)(ws + o) = pv;
  };
  if (b >= K1_LNB) {
    const int jx = ((b - K1_LNB) >> 3) * 4 + wave, h = (b & 7) * 4 + jx / 81, kp = (jx / 9) % 9, bb = jx % 9;
    u32x2 rw[4][8];
    u32x4 af[2][2][4];
#pragma unroll
    for (int es = 0; es < 4; ++es)
#pragma unroll
      for (int jj = 0; jj < 8; ++jj)
        rw[es][jj] = *(const u32x2*)(a.wo + (size_t)(h * DH + es * 32 + g * 8 + jj) * DM + bb * 64 + c16 * 4);
#pragma unroll
    for (int i = 0; i < 16; ++i) {
      const int k = (kp * 2 + (i >> 3)) * 32 + (c16 >> 2) * 8 + (c16 & 3) + ((i >> 2) & 1) * 4;
      af[i >> 3][(i >> 2) & 1][i & 3] = *(const u32x4*)(a.wv + (size_t)k * (NH * DH) + h * DH + (i & 3) * 32 + g * 8);
    }
    poison();
    __builtin_amdgcn_sched_barrier(0);
    u32x4 bf[4][4];
#pragma unroll
    for (int es = 0; es < 4; ++es)
#pragma unroll
      for (int p = 0; p < 4; ++p) {
        bf[es][0][p] = plo(rw[es][2 * p].x, rw[es][2 * p + 1].x); bf[es][1][p] = phi(rw[es][2 * p].x, rw[es][2 * p + 1].x);
        bf[es][2][p] = plo(rw[es][2 * p].y, rw[es][2 * p + 1].y); bf[es][3][p] = phi(rw[es][2 * p].y, rw[es][2 * p + 1].y);
      }
#pragma unroll
    for (int tt = 0; tt < 2; ++tt) {
      const int t = kp * 2 + tt;
      f32x4 acc[2][4];
#pragma unroll
      for (int rt = 0; rt < 2; ++rt)
#pragma unroll
        for (int i = 0; i < 4; ++i) {
          acc[rt][i] = f32x4{0.f, 0.f, 0.f, 0.f};
#pragma unroll
          for (int es = 0; es < 4; ++es) acc[rt][i] = mfma(af[tt][rt][es], bf[es][i], acc[rt][i]);
        }
      char* tl = tbuf + wave * 4096;
#pragma unroll
      for (int i = 0; i < 4; ++i) {
        u32x4 v = {pk(acc[0][i][0], acc[0][i][1]), pk(acc[0][i][2], acc[0][i][3]), pk(acc[1][i][0], acc[1][i][1]), pk(acc[1][i][2], acc[1][i][3])};
        *(u32x4*)(tl + ((c16 >> 1) * 4 + g) * 128 + ((c16 & 1) * 4 + i) * 16) = v;
      }
      __builtin_amdgcn_wave_barrier();
#pragma unroll
      for (int k = 0; k < 4; ++k) {
        const int ln = k * 8 + (lane >> 3);
        const u32x4 v = *(const u32x4*)(tl + k * 1024 + lane * 16);
        *(u32x4*)(ws + WVO_OFF + (size_t)(bb * 8 + (ln >> 2)) * WVO_J + ((h >> 1) * KS + t) * 1024 + ((ln & 3) * 16 + (h & 1) * 8 + (lane & 7)) * 16) = v;
      }
      __builtin_amdgcn_wave_barrier();
    }
  } else {
    u32x4 vv[2][5];
#pragma unroll
    for (int rr = 0; rr < 2; ++rr)
#pragma unroll
    for (int i = 0; i < 5; ++i) {
      vv[rr][i] = u32x4{0, 0, 0, 0};
      if (i < 4 || c16 < 8) vv[rr][i] = *(const u32x4*)(a.x + (size_t)(job * 8 + rr * 4 + g) * DM + (i * 16 + c16) * 8);
    }
    poison();
#pragma unroll
    for (int rr = 0; rr < 2; ++rr) {
    const int row = job * 8 + rr * 4 + g;
    const u32x4 (&v)[5] = vv[rr];
    float sm = 0.f;
#pragma unroll
    for (int i = 0; i < 20; ++i) sm += bflo(v[i >> 2][i & 3]) + bfhi(v[i >> 2][i & 3]);
#pragma unroll
    for (int o = 1; o < 16; o <<= 1) sm += __shfl_xor(sm, o);
    const float mean = sm * (1.f / DM);
    float q = 0.f;
#pragma unroll
    for (int i = 0; i < 20; ++i) {
      const float d0 = bflo(v[i >> 2][i & 3]) - mean, d1 = bfhi(v[i >> 2][i & 3]) - mean;
      q += (i < 16 || c16 < 8) ? d0 * d0 + d1 * d1 : 0.f;
    }
#pragma unroll
    for (int o = 1; o < 16; o <<= 1) q += __shfl_xor(q, o);
    const float mb = bfr(mean), den = bfr(sqrtf(bfr(bfr(q * (1.f / DM)) + 1e-5f)));
#pragma unroll
    for (int i = 0; i < 5; ++i) {
      u32x4 o;
#pragma unroll
      for (int k = 0; k < 4; ++k) o[k] = pk(bfr(bflo(v[i][k]) - mb) / den, bfr(bfhi(v[i][k]) - mb) / den);
      const int ch = i * 16 + c16;
      if (i < 4 || c16 < 8) *(u32x4*)(ws + XF_OFF + ((row >> 4) * KS + (ch >> 2)) * 1024 + ((ch & 3) * 16 + (row & 15)) * 16) = o;
    }
    }
  }
}

constexpr u32 L_X = 0;
constexpr u32 L_M = 2 * XF_SEG;
constexpr u32 L_QM = L_M + 9216;
constexpr u32 L_QX = L_QM + 8192;

template <bool WA, int ST>
__device__ __forceinline__ void proj2(const u32x4 (&W0)[KS], const u32x4 (&W1)[KS], const char* xb, f32x4& c0, f32x4& c1) {
  u32x4 xf[KS];
#pragma unroll
  for (int t = 0; t < KS; ++t) xf[t] = *(const u32x4*)(xb + t * ST);
  c0 = f32x4{0.f, 0.f, 0.f, 0.f}; c1 = c0;
#pragma unroll
  for (int t = 0; t < KS; ++t) {
    c0 = WA ? mfma(W0[t], xf[t], c0) : mfma(xf[t], W0[t], c0);
    c1 = WA ? mfma(W1[t], xf[t], c1) : mfma(xf[t], W1[t], c1);
  }
  __builtin_amdgcn_sched_group_barrier(0x100, 6, 0);
#pragma unroll
  for (int t = 0; t < KS - 6; ++t) { __builtin_amdgcn_sched_group_barrier(0x008, 2, 0); __builtin_amdgcn_sched_group_barrier(0x100, 1, 0); }
  __builtin_amdgcn_sched_group_barrier(0x008, 12, 0);
}
__device__ __forceinline__ void stage_x(const unsigned char* ws, int seg, int wave, int lane) {
  const unsigned char* src = ws + XF_OFF + seg * XF_SEG + lane * 16;
  const u32 dst = L_X + (seg & 1) * XF_SEG;
#pragma unroll
  for (int i = 0; i < 5; ++i) {
    const int p = i * 8 + wave;
    if (p < 36) glds16(src + p * 1024, dst + p * 1024);
  }
}
__device__ __forceinline__ u32 get_flips(__amdgpu_buffer_rsrc_t r, int lane, u32* tmo) {
  u32 f, spins = 0;
  for (;;) {
    f = __builtin_amdgcn_raw_buffer_load_b32(r, FLP_OFF + lane * 4, 0, SC1);
    if (allok(f)) break;
    if (spin_fail(spins, tmo)) break;
    __builtin_amdgcn_s_sleep(8);
  }
  return f & 8192u;
}
__device__ __forceinline__ void gather_m(__amdgpu_buffer_rsrc_t r, u32 base, u32 flip, bool first, char* smem, int wave, int lane, u32* tmo) {
  const u32 l0 = (wave * 64 + lane) * 16, l1 = (512 + wave * 8 + (lane & 7)) * 16;
  const u32 o0 = first ? M0_OFF + l0 : base + homap(HO_M + l0, flip), o1 = first ? M0_OFF + l1 : base + homap(HO_M + l1, flip);
  u32 spins = 0;
  u32x4 v, w;
  for (;;) {
    v = __builtin_amdgcn_raw_buffer_load_b128(r, o0, 0, SC1);
    w = __builtin_amdgcn_raw_buffer_load_b128(r, o1, 0, SC1);
    if (allok(mx(mx4(v), mx4(w)))) break;
    if (spin_fail(spins, tmo)) break;
  }
  *(u32x4*)(smem + L_M + l0) = v;
  if (lane < 8) *(u32x4*)(smem + L_M + l1) = w;
  wait_vm();
}

__device__ __forceinline__ void load_wqk(const Args& a, int h, u32x4 (&W)[2][KS], char* smem) {
  const int tid = threadIdx.x, lane = tid & 63, wave = __builtin_amdgcn_readfirstlane(tid >> 6);
  const int c16 = lane & 15, g = lane >> 4;
  {
    u32x4 st[18];
    auto ldw = [&](int hf) {
#pragma unroll
      for (int i = 0; i < 18; ++i) {
        const int pc = i * 512 + tid;
        st[i] = *(const u32x4*)(((pc & 16) ? a.wk : a.wq) + (size_t)(hf * 288 + (pc >> 5)) * (NH * DH) + h * DH + (pc & 15) * 8);
      }
    };
    ldw(0);
#pragma unroll
    for (int hf = 0; hf < 2; ++hf) {
#pragma unroll
      for (int i = 0; i < 18; ++i) { const int pc = i * 512 + tid; *(u32x4*)(smem + (pc >> 5) * 528 + (pc & 31) * 16) = st[i]; }
      if (hf == 0) ldw(1);
      bar();
      const char* wl = smem + g * 8 * 528 + wave * 64 + c16 * 4;
#pragma unroll
      for (int t = 0; t < 9; ++t) {
#pragma unroll
        for (int p = 0; p < 4; ++p) {
          const u32 d0 = *(const u32*)(wl + (t * 32 + 2 * p) * 528), d1 = *(const u32*)(wl + (t * 32 + 2 * p + 1) * 528);
          W[0][hf * 9 + t][p] = plo(d0, d1); W[1][hf * 9 + t][p] = phi(d0, d1);
        }
        asm volatile("" : "+v"(W[0][hf * 9 + t]), "+v"(W[1][hf * 9 + t]) :: "memory");
      }
      bar();
    }
  }
}

__device__ __forceinline__ void role_a(const Args& a, int h, char* smem) {
  const int tid = threadIdx.x, lane = tid & 63, wave = __builtin_amdgcn_readfirstlane(tid >> 6);
  const int c16 = lane & 15, g = lane >> 4;
  unsigned char* ws = a.ws;
  u32* tmo = (u32*)(ws + CTL_OFF) + 64;
  __amdgpu_buffer_rsrc_t r = __builtin_amdgcn_make_buffer_rsrc(ws, 0, WS_END, 0x00020000);
  u32x4 W[2][KS];
  load_wqk(a, h, W, smem);
  const u32 flips = get_flips(r, lane, tmo);
  const u32 lx = (((h >> 1) * 4 + g) * 16 + (h & 1) * 8 + (lane & 7)) * 16, lm = HO_PM + h * 128 + g * 64 + (lane & 7) * 8;
  u32x4 kf[8];
  u32 fl = POISON;
  auto q_load = [&](int seg) {
    const u32 fo = FLQ_OFF + seg * 128 + h * 4;
    u32 spins = 0;
    while (!allok(fl)) {
      fl = __builtin_amdgcn_raw_buffer_load_b32(r, fo, 0, SC1);
      if (spin_fail(spins, tmo)) break;
    }
    asm volatile("" ::: "memory");
    const u32 qo = QX_OFF + ((seg * NH + h) * 16 + (wave - 1) * 4) * 1024 + lane * 16;
#pragma unroll
    for (int w = 0; w < 4; ++w) kf[w] = __builtin_amdgcn_raw_buffer_load_b128(r, qo + w * 1024, 0, SC1);
    if (wave == 3) {
#pragma unroll
      for (int w = 4; w < 8; ++w) kf[w] = __builtin_amdgcn_raw_buffer_load_b128(r, qo + w * 1024, 0, SC1);
#pragma unroll
      for (int w = 0; w < 8; ++w) *(u32x4*)(smem + L_QX + (seg & 1) * 16384 + 8192 + w * 1024 + lane * 16) = kf[w];
    }
    fl = seg + 1 < NSEG ? __builtin_amdgcn_raw_buffer_load_b32(r, fo + 128, 0, SC1) : 0u;
  };
  stage_x(ws, 0, wave, lane);
  wait_vm();
  bar();
  if (wave >= 4) {
#pragma unroll
    for (int rt = 0; rt < 2; ++rt) {
      f32x4 c0, c1;
      proj2<true, 1024>(W[0], W[1], smem + L_X + rt * KS * 1024 + lane * 16, c0, c1);
      *(u32x4*)(smem + L_QX + 8192 + rt * 4096 + (wave & 3) * 1024 + lane * 16) = u32x4{pk(c0[0], c1[0]), pk(c0[1], c1[1]), pk(c0[2], c1[2]), pk(c0[3], c1[3])};
    }
  }
  for (int s = 0; s < NSEG; ++s) {
    const u32 flip = __builtin_amdgcn_readlane(flips, s), base = HO_OFF + s * 65536;
    gather_m(r, base, flip, s == 0, smem, wave, lane, tmo);
    bar();
    {
      f32x4 c0, c1;
      proj2<true, 512>(W[0], W[1], smem + L_M + g * 128 + (lane & 7) * 16, c0, c1);
      u32x4 o = {pk(c0[0], c1[0]), pk(c0[1], c1[1]), pk(c0[2], c1[2]), pk(c0[3], c1[3])};
      *(u32x4*)(smem + L_QM + wave * 1024 + lane * 16) = o;
    }
    if (wave == 0) {
      __builtin_amdgcn_s_setprio(3);
#pragma unroll
      for (int w = 0; w < 8; ++w) kf[w] = *(const u32x4*)(smem + L_QX + (s & 1) * 16384 + 8192 + lane * 16 + w * 1024);
    }
    bar();
    const float C2 = 0.08838834764831845f * 1.4426950408889634f;
    if (wave == 0) {
      if (s < NSEG - 1) {
        const char* qb = smem + L_QM + lane * 16;
        f32x4 s0 = {0.f, 0.f, 0.f, 0.f}, s1 = s0, s2 = s0;
#pragma unroll
        for (int w = 0; w < 4; ++w) {
          const u32x4 qf = *(const u32x4*)(qb + w * 1024);
          s2 = mfma(*(const u32x4*)(qb + 4096 + w * 1024), qf, s2);
          s0 = mfma(kf[w], qf, s0);
          s1 = mfma(kf[4 + w], qf, s1);
        }
        const bool hi = c16 >= 8;
        float v[6];
#pragma unroll
        for (int i = 0; i < 4; ++i) v[i] = hi ? s1[i] : s0[i];
        v[4] = g < 2 ? (hi ? s2[2] : s2[0]) : -INFINITY;
        v[5] = g < 2 ? (hi ? s2[3] : s2[1]) : -INFINITY;
        float m = vmax(vmax3(v[0], v[1], v[2]), vmax3(v[3], v[4], v[5]));
        m = xmax(m);
        m = vmax(m, ror8(m)) * C2;
#pragma unroll
        for (int i = 0; i < 6; ++i) v[i] = __builtin_amdgcn_exp2f(__builtin_fmaf(v[i], C2, -m));
        float sum = ((v[0] + v[1]) + (v[2] + v[3])) + (v[4] + v[5]);
        sum += ror8(sum);
        const float inv = __builtin_amdgcn_rcpf(xsum(sum));
        const u32x2 px = {pk(v[0] * inv, v[1] * inv) & 0x7FFF7FFFu, pk(v[2] * inv, v[3] * inv) & 0x7FFF7FFFu};
        const u32 pm = pk(v[4] * inv, v[5] * inv) & 0x7FFF7FFFu;
        __builtin_amdgcn_raw_buffer_store_b64(px, r, base + homap(lx + (c16 >> 3) * 8, flip), 0, SC1);
        if (g < 2) __builtin_amdgcn_raw_buffer_store_b32(pm, r, base + homap(lm + (c16 >> 3) * 4, flip), 0, SC1);
      }
      __builtin_amdgcn_s_setprio(0);
    } else if (wave < 3) {
      const char* qx = smem + L_QX + (s & 1) * 16384 + lane * 16;
      if (s == 0) q_load(0);
      f32x4 s0 = {0.f, 0.f, 0.f, 0.f}, s1 = s0, s2 = s0;
#pragma unroll
      for (int w = 0; w < 4; ++w) {
        s2 = mfma(*(const u32x4*)(smem + L_QM + 4096 + lane * 16 + w * 1024), kf[w], s2);
        s0 = mfma(*(const u32x4*)(qx + 8192 + w * 1024), kf[w], s0);
        s1 = mfma(*(const u32x4*)(qx + 12288 + w * 1024), kf[w], s1);
      }
      const int qi = (wave - 1) * 16 + c16;
      const float NEG = -1.0e9f * 1.4426950408889634f;
      float v[12];
#pragma unroll
      for (int i = 0; i < 4; ++i) {
        v[i] = (4 * g + i <= qi) ? s0[i] * C2 : NEG;
        v[4 + i] = (16 + 4 * g + i <= qi) ? s1[i] * C2 : NEG;
        v[8 + i] = g < 2 ? s2[i] * C2 : -INFINITY;
      }
      float m = v[0];
#pragma unroll
      for (int i = 1; i < 12; ++i) m = fmaxf(m, v[i]);
      m = xmax(m);
      float sum = 0.f;
#pragma unroll
      for (int i = 0; i < 12; ++i) { v[i] = __builtin_amdgcn_exp2f(v[i] - m); sum += v[i]; }
      const float inv = __builtin_amdgcn_rcpf(xsum(sum));
      u32x4 px; u32x2 pm;
#pragma unroll
      for (int i = 0; i < 4; ++i) px[i] = pk(v[2 * i] * inv, v[2 * i + 1] * inv) & 0x7FFF7FFFu;
#pragma unroll
      for (int i = 0; i < 2; ++i) pm[i] = pk(v[8 + 2 * i] * inv, v[9 + 2 * i] * inv) & 0x7FFF7FFFu;
      const u32 tb = PT_OFF + s * PT_STEP + ((wave - 1) * 2 + (c16 >> 3)) * 20480;
      __builtin_amdgcn_raw_buffer_store_b128(px, r, tb + lx, 0, SC1);
      if (g < 2) __builtin_amdgcn_raw_buffer_store_b64(pm, r, tb + lm, 0, SC1);
    }
    if (wave >= 1 && wave <= 3 && s + 1 < NSEG) q_load(s + 1);
    bar();
    __builtin_amdgcn_s_sleep(10);
  }
}

__device__ __forceinline__ void role_b(const Args& a, int j, char* smem) {
  const int tid = threadIdx.x, lane = tid & 63, wave = __builtin_amdgcn_readfirstlane(tid >> 6);
  const int c16 = lane & 15, g = lane >> 4;
  unsigned char* ws = a.ws;
  u32* tmo = (u32*)(ws + CTL_OFF) + 64;
  __amdgpu_buffer_rsrc_t r = __builtin_amdgcn_make_buffer_rsrc(ws, 0, WS_END, 0x00020000);
  u32x4 W[2][KS];
  {
    const unsigned char* wsrc = ws + WVO_OFF + (size_t)j * WVO_J + wave * 2 * KS * 1024 + lane * 16;
#pragma unroll
    for (int tt = 0; tt < 2; ++tt)
#pragma unroll
      for (int t = 0; t < KS; ++t) W[tt][t] = *(const u32x4*)(wsrc + (tt * KS + t) * 1024);
  }
  wait_vm();
  bar();
  const u32 flips = get_flips(r, lane, tmo);
  u32x4 nx0, nx1;
  u32 fl = POISON;
  auto ux_load = [&](int seg) {
    const u32 fo = FLG_OFF + seg * 128 + wave * 16 + (lane & 3) * 4;
    u32 spins = 0;
    while (!allok(fl)) {
      fl = __builtin_amdgcn_raw_buffer_load_b32(r, fo, 0, SC1);
      if (spin_fail(spins, tmo)) break;
    }
    asm volatile("" ::: "memory");
    const u32 uo = UX_OFF + ((seg * NB + j) * 16 + wave * 2) * 1024 + lane * 16;
    nx0 = __builtin_amdgcn_raw_buffer_load_b128(r, uo, 0, SC1);
    nx1 = __builtin_amdgcn_raw_buffer_load_b128(r, uo + 1024, 0, SC1);
    fl = seg + 1 < NSEG ? __builtin_amdgcn_raw_buffer_load_b32(r, fo + 128, 0, SC1) : 0u;
  };
  const u32 lx = wave * 2048 + lane * 16;
  const u32 lm = HO_PM + (wave * 4 + (c16 >> 3)) * 128 + (g & 1) * 64 + (lane & 7) * 8;
  const bool diag = (c16 >> 3) == (g >> 1);
  const u32 redo = ((wave * 2 + (g >> 1)) * 64 + (lane & 7) * 8 + (g & 1) * 4) * 4;
  for (int s = 0; s < NSEG; ++s) {
    const u32 flip = __builtin_amdgcn_readlane(flips, s), base = HO_OFF + s * 65536;
    gather_m(r, base, flip, s == 0, smem, wave, lane, tmo);
    bar();
    u32x4 um;
    {
      f32x4 c0, c1;
      proj2<false, 512>(W[0], W[1], smem + L_M + g * 128 + (lane & 7) * 16, c0, c1);
      um = u32x4{pk(c0[0], c0[1]), pk(c0[2], c0[3]), pk(c1[0], c1[1]), pk(c1[2], c1[3])};
      if (g < 2) __builtin_amdgcn_raw_buffer_store_b128(um, r, UM_OFF + ((s * NB + j) * 8 + wave) * 512 + lane * 16, 0, SC1);
      else um = u32x4{0, 0, 0, 0};
    }
    if (s + 1 < NSEG) {
      if (s == 0) ux_load(0);
      if (s > 0) __builtin_amdgcn_s_sleep(16);
      const u32 o0 = base + homap(lx, flip), o1 = o0 + 1024, o2 = base + homap(lm, flip), o3 = o2 + 256;
      u32x4 p0, p1; u32x2 q0, q1;
      u32 spins = 0;
      for (;;) {
        p0 = __builtin_amdgcn_raw_buffer_load_b128(r, o0, 0, SC1);
        p1 = __builtin_amdgcn_raw_buffer_load_b128(r, o1, 0, SC1);
        q0 = __builtin_amdgcn_raw_buffer_load_b64(r, o2, 0, SC1);
        q1 = __builtin_amdgcn_raw_buffer_load_b64(r, o3, 0, SC1);
        if (allok(mx(mx(mx4(p0), mx4(p1)), mx(mx2(q0), mx2(q1))))) break;
        if (spin_fail(spins, tmo)) break;
      }
      f32x4 d = {0.f, 0.f, 0.f, 0.f}, e = d;
      const u32x4 pmv = {q0.x, q0.y, q1.x, q1.y};
      d = mfma(nx0, p0, d); e = mfma(nx1, p1, e); d = mfma(um, pmv, d);
      d += e;
      if (diag) *(f32x4*)(smem + L_QM + redo) = d;
    }
    wait_vm();
    bar();
    if (wave == 0 && s + 1 < NSEG) {
      const char* rp = smem + L_QM + (lane >> 5) * 2048 + (lane & 31) * 8;
      f32x2 acc = *(const f32x2*)rp;
#pragma unroll
      for (int p = 1; p < 8; ++p) acc += *(const f32x2*)(rp + p * 256);
      auto t0 = __builtin_amdgcn_permlane32_swap(__float_as_uint(acc.x), __float_as_uint(acc.x), false, false);
      auto t1 = __builtin_amdgcn_permlane32_swap(__float_as_uint(acc.y), __float_as_uint(acc.y), false, false);
      const u32 o = pks(__uint_as_float(t0[0]) + __uint_as_float(t0[1]), __uint_as_float(t1[0]) + __uint_as_float(t1[1]));
      const u32 nflip = __builtin_amdgcn_readlane(flips, s + 1);
      if (lane < 32) __builtin_amdgcn_raw_buffer_store_b32(o, r, base + 65536 + homap(HO_M + j * 128 + lane * 4, nflip), 0, SC1);
    }
    if (tid == 64) __builtin_amdgcn_raw_buffer_store_b32(1u, r, FLM_OFF + s * 3072 + (j / 6) * 128 + (j % 6) * 4, 0, SC1);
    if (s + 2 < NSEG) ux_load(s + 1);
  }
}

__device__ __forceinline__ void role_o(const Args& a, int o24, char* smem) {
  const int tid = threadIdx.x, lane = tid & 63, wave = __builtin_amdgcn_readfirstlane(tid >> 6);
  const int c16 = lane & 15, g = lane >> 4, o = o24 >> 1;
  unsigned char* ws = a.ws;
  u32* tmo = (u32*)(ws + CTL_OFF) + 64;
  __amdgpu_buffer_rsrc_t r = __builtin_amdgcn_make_buffer_rsrc(ws, 0, WS_END, 0x00020000);
  if (wave < 3 && o24 * 3 + wave < NSEG) {
    const int blk = o24 * 3 + wave;
    u32 best[2] = {~0u, ~0u};
    for (int rep = 0; rep < 4; ++rep)
      for (int f = 0; f < 2; ++f) {
        u32* p = (u32*)(ws + HO_OFF + blk * 65536 + homap(32768 - 128 * (rep + 1), f * 8192));
        __hip_atomic_store(p, POISON, __ATOMIC_RELAXED, __HIP_MEMORY_SCOPE_AGENT);
        wait_vm();
        u64 t0, t1;
        asm volatile("s_memtime %0\n\ts_waitcnt lgkmcnt(0)" : "=s"(t0) :: "memory");
        u32 v = __hip_atomic_load(p, __ATOMIC_RELAXED, __HIP_MEMORY_SCOPE_AGENT);
        asm volatile("s_waitcnt vmcnt(0)" : "+v"(v) :: "memory");
        asm volatile("s_memtime %0\n\ts_waitcnt lgkmcnt(0)" : "=s"(t1) :: "memory");
        const u32 d = (u32)(t1 - t0);
        best[f] = d < best[f] ? d : best[f];
      }
    if (lane == 0) __builtin_amdgcn_raw_buffer_store_b32(best[1] < best[0] ? 8192u : 0u, r, FLP_OFF + blk * 4, 0, SC1);
  }
  const u32 lx = wave * 2048 + lane * 16;
  const u32 lm = HO_PM + (wave * 4 + (c16 >> 3)) * 128 + (g & 1) * 64 + (lane & 7) * 8;
  const bool diag = (c16 >> 3) == (g >> 1);
  const u32 redo = ((wave * 2 + (g >> 1)) * 64 + (lane & 7) * 8 + (g & 1) * 4) * 4;
  u32x4 x0[6], x1[6], um[6];
  auto ux_load = [&](int s) {
    u32 spins = 0;
    while (!allok(__builtin_amdgcn_raw_buffer_load_b32(r, FLG_OFF + s * 128 + wave * 16 + (lane & 3) * 4, 0, SC1))) {
      if (spin_fail(spins, tmo)) break;
      __builtin_amdgcn_s_sleep(16);
    }
    asm volatile("" ::: "memory");
#pragma unroll
    for (int c = 0; c < 6; ++c) {
      const u32 uo = UX_OFF + ((s * NB + o * 6 + c) * 16 + wave * 2) * 1024 + lane * 16;
      x0[c] = __builtin_amdgcn_raw_buffer_load_b128(r, uo, 0, SC1);
      x1[c] = __builtin_amdgcn_raw_buffer_load_b128(r, uo + 1024, 0, SC1);
    }
  };
  const u32 e0 = (((tid & 63) >> 1) * DM + (o * 6 + (tid >> 6)) * 8 + (tid & 1) * 4) * 2;
  u32x2 xv = {0, 0}, fv = {0, 0};
  ux_load(o24 & 1);
  for (int s = o24 & 1; s < NSEG; s += 2) {
    u32 spins = 0;
    if (wave == 0) {
      const u32 fo = FLM_OFF + s * 3072 + o * 128 + (lane % 6) * 4;
      while (!allok(__builtin_amdgcn_raw_buffer_load_b32(r, fo, 0, SC1))) {
        if (spin_fail(spins, tmo)) break;
        __builtin_amdgcn_s_sleep(4);
      }
    }
    if (s - 2 < SPLIT) wait_vm();
    bar();
    if (tid == 0 && s >= 2 && s - 2 < SPLIT) __builtin_amdgcn_raw_buffer_store_b32(1u, r, FLA_OFF + (s - 2) * 128 + o * 4, 0, SC1);
    const u32 eo = e0 + s * 32 * DM * 2;
#pragma unroll
    for (int c = 0; c < 6; ++c)
      um[c] = __builtin_amdgcn_raw_buffer_load_b128(r, UM_OFF + ((s * NB + o * 6 + c) * 8 + wave) * 512 + (lane & 31) * 16, 0, SC1);
    u32x4 p0[4], p1[4]; u32x2 q0[4], q1[4];
    for (;;) {
      u32 m = 0;
#pragma unroll
      for (int u = 0; u < 4; ++u) {
        const u32 tb = PT_OFF + s * PT_STEP + u * 20480;
        p0[u] = __builtin_amdgcn_raw_buffer_load_b128(r, tb + lx, 0, SC1);
        p1[u] = __builtin_amdgcn_raw_buffer_load_b128(r, tb + lx + 1024, 0, SC1);
        q0[u] = __builtin_amdgcn_raw_buffer_load_b64(r, tb + lm, 0, SC1);
        q1[u] = __builtin_amdgcn_raw_buffer_load_b64(r, tb + lm + 256, 0, SC1);
      }
#pragma unroll
      for (int u = 0; u < 4; ++u) m = mx(m, mx(mx(mx4(p0[u]), mx4(p1[u])), mx(mx2(q0[u]), mx2(q1[u]))));
      if (allok(m)) break;
      if (spin_fail(spins, tmo)) break;
    }
#pragma unroll
    for (int c = 0; c < 6; ++c) {
      if (g >= 2) um[c] = u32x4{0, 0, 0, 0};
#pragma unroll
      for (int u = 0; u < 4; ++u) {
        f32x4 d = {0.f, 0.f, 0.f, 0.f};
        const u32x4 pmv = {q0[u].x, q0[u].y, q1[u].x, q1[u].y};
        d = mfma(x0[c], p0[u], d); d = mfma(x1[c], p1[u], d); d = mfma(um[c], pmv, d);
        if (diag) *(f32x4*)(smem + (c * 4 + u) * 4096 + redo) = d;
      }
    }
    bar();
    if (tid < 384) {
      const char* rp = smem + (tid >> 4) * 4096 + (tid & 15) * 16;
      f32x4 acc = *(const f32x4*)rp;
#pragma unroll
      for (int p = 1; p < 16; ++p) acc += *(const f32x4*)(rp + p * 256);
      const u32x2 av = {pk(acc[0], acc[1]), pk(acc[2], acc[3])};
      if (s < SPLIT) __builtin_amdgcn_raw_buffer_store_b64(av, r, AO_OFF + eo, 0, SC1);
      else *(u32x2*)((char*)a.out + eo) = u32x2{fin(xv.x, av.x, fv.x), fin(xv.y, av.y, fv.y)};
      if (s + 2 >= SPLIT && s + 2 < NSEG) {
        while (!allok(__builtin_amdgcn_raw_buffer_load_b32(r, FLF_OFF + (((s + 2) >> 2) * 6 + (o >> 1)) * 128, 0, SC1))) {
          if (spin_fail(spins, tmo)) break;
          __builtin_amdgcn_s_sleep(16);
        }
        asm volatile("" ::: "memory");
        xv = *(const u32x2*)((const char*)a.x + eo + 64 * DM * 2);
        fv = __builtin_amdgcn_raw_buffer_load_b64(r, FO_OFF + eo + 64 * DM * 2, 0, SC1);
      }
    }
    if (s + 2 < NSEG) ux_load(s + 2);
  }
}

__device__ __forceinline__ void role_v(const Args& a, int h, char* smem) {
  const int tid = threadIdx.x, lane = tid & 63, wave = __builtin_amdgcn_readfirstlane(tid >> 6);
  const int c16 = lane & 15, g = lane >> 4;
  unsigned char* ws = a.ws;
  __amdgpu_buffer_rsrc_t r = __builtin_amdgcn_make_buffer_rsrc(ws, 0, WS_END, 0x00020000);
  u32x4 WV[KS], WO[5][4];
  {
    const u32 sel = (c16 & 1) ? 0x07060302u : 0x05040100u;
    u32x4 st[18];
#pragma unroll
    for (int i = 0; i < 18; ++i) { const int pc = i * 512 + tid; st[i] = *(const u32x4*)(a.wv + (size_t)(pc >> 4) * (NH * DH) + h * DH + (pc & 15) * 8); }
#pragma unroll
    for (int i = 0; i < 18; ++i) { const int pc = i * 512 + tid; *(u32x4*)(smem + (pc >> 4) * 272 + (pc & 15) * 16) = st[i]; }
#pragma unroll
    for (int i = 0; i < 18; ++i) st[i] = *(const u32x4*)(a.wo + (size_t)h * DH * DM + (i * 512 + tid) * 8);
    bar();
    {
      const char* wl = smem + g * 8 * 272 + wave * 32 + (c16 >> 1) * 4;
#pragma unroll
      for (int t = 0; t < KS; ++t) {
#pragma unroll
        for (int p = 0; p < 4; ++p)
          WV[t][p] = __builtin_amdgcn_perm(*(const u32*)(wl + (t * 32 + 2 * p + 1) * 272), *(const u32*)(wl + (t * 32 + 2 * p) * 272), sel);
        asm volatile("" : "+v"(WV[t]) :: "memory");
      }
    }
    bar();
#pragma unroll
    for (int i = 0; i < 18; ++i) { const int pc = i * 512 + tid; *(u32x4*)(smem + (pc / 72) * 1168 + (pc % 72) * 16) = st[i]; }
    bar();
#pragma unroll
    for (int i = 0; i < 5; ++i) {
      const int ct = wave + 8 * i < 36 ? wave + 8 * i : 0;
      const char* wl = smem + g * 4 * 1168 + ct * 32 + (c16 >> 1) * 4;
#pragma unroll
      for (int es = 0; es < 4; ++es) {
#pragma unroll
        for (int p = 0; p < 4; ++p) {
          const int e0 = 32 * es + 16 * (p >> 1) + 2 * (p & 1);
          WO[i][es][p] = __builtin_amdgcn_perm(*(const u32*)(wl + (e0 + 1) * 1168), *(const u32*)(wl + e0 * 1168), sel);
        }
        asm volatile("" : "+v"(WO[i][es]) :: "memory");
      }
    }
    bar();
  }
  stage_x(ws, 0, wave, lane);
  for (int sg = 0; sg < NSEG; ++sg) {
    wait_vm();
    bar();
    if (sg > 0 && tid == 0) __builtin_amdgcn_raw_buffer_store_b32(1u, r, FLG_OFF + (sg - 1) * 128 + h * 4, 0, SC1);
    {
      const char* xb = smem + L_X + (sg & 1) * XF_SEG + lane * 16;
      f32x4 c0 = {0.f, 0.f, 0.f, 0.f}, c1 = c0;
#pragma unroll
      for (int t = 0; t < KS; ++t) {
        c0 = mfma(WV[t], *(const u32x4*)(xb + t * 1024), c0);
        c1 = mfma(WV[t], *(const u32x4*)(xb + (KS + t) * 1024), c1);
      }
      __builtin_amdgcn_sched_group_barrier(0x100, 8, 0);
#pragma unroll
      for (int t = 0; t < KS - 4; ++t) { __builtin_amdgcn_sched_group_barrier(0x008, 2, 0); __builtin_amdgcn_sched_group_barrier(0x100, 2, 0); }
      __builtin_amdgcn_sched_group_barrier(0x008, 8, 0);
      if (sg + 1 < NSEG) stage_x(ws, sg + 1, wave, lane);
      char* vt = smem + L_QM + (wave >> 1) * 1024 + lane * 16 + (wave & 1) * 8;
      *(u32x2*)vt = u32x2{pk(c0[0], c0[1]), pk(c0[2], c0[3])};
      *(u32x2*)(vt + 4096) = u32x2{pk(c1[0], c1[1]), pk(c1[2], c1[3])};
    }
    bar();
    u32x4 vf[2][4];
#pragma unroll
    for (int rt = 0; rt < 2; ++rt)
#pragma unroll
      for (int es = 0; es < 4; ++es) vf[rt][es] = *(const u32x4*)(smem + L_QM + rt * 4096 + es * 1024 + lane * 16);
#pragma unroll
    for (int i = 0; i < 5; ++i) {
      const int ct = wave + 8 * i;
      if (ct < 36) {
        f32x4 d0 = {0.f, 0.f, 0.f, 0.f}, d1 = d0;
#pragma unroll
        for (int es = 0; es < 4; ++es) { d0 = mfma(vf[0][es], WO[i][es], d0); d1 = mfma(vf[1][es], WO[i][es], d1); }
        const u32x4 o = {pk(d0[0], d0[1]), pk(d0[2], d0[3]), pk(d1[0], d1[1]), pk(d1[2], d1[3])};
        __builtin_amdgcn_raw_buffer_store_b128(o, r, UX_OFF + ((sg * NB + ct * 2 + (c16 >> 3)) * 16 + (h >> 1)) * 1024 + (g * 16 + (h & 1) * 8 + (lane & 7)) * 16, 0, SC1);
      }
    }
  }
  wait_vm();
  bar();
  if (tid == 0) __builtin_amdgcn_raw_buffer_store_b32(1u, r, FLG_OFF + (NSEG - 1) * 128 + h * 4, 0, SC1);
}

__device__ __forceinline__ float gelu_tanh(float x) {
  const float u = 0.7978845608028654f * (x + 0.044715f * x * x * x);
  const float e = __expf(2.f * u);
  return 0.5f * x * (2.f - 2.f / (e + 1.f));
}

__device__ __forceinline__ void role_f(const Args& a, int f, char* smem) {
  const int tid = threadIdx.x, lane = tid & 63, wave = __builtin_amdgcn_readfirstlane(tid >> 6);
  const int c16 = lane & 15, g = lane >> 4;
  unsigned char* ws = a.ws;
  u32* tmo = (u32*)(ws + CTL_OFF) + 64;
  u32* cnt = (u32*)(ws + CTL_OFF) + 128;
  __amdgpu_buffer_rsrc_t r = __builtin_amdgcn_make_buffer_rsrc(ws, 0, WS_END, 0x00020000);
  auto stage = [&](int sg) {
#pragma unroll
    for (int i = 0; i < 5; ++i) {
      const int p = min(i * 8 + wave, 35);
      glds16(ws + XF_OFF + sg * XF_SEG + p * 1024 + lane * 16, (sg & 3) * XF_SEG + p * 1024);
    }
  };
  if (f >= 64) {
    const int h = f - 64;
    u32x4 WW[2][KS];
    load_wqk(a, h, WW, smem);
    wait_vm();
    stage(0); stage(1);
    for (int sg = 0; sg < NSEG; ++sg) {
      if (sg < 4) wait_vm();
      else asm volatile("s_waitcnt vmcnt(5)" ::: "memory");
      bar();
      if (sg >= 1 && tid == 0) __builtin_amdgcn_raw_buffer_store_b32(1u, r, FLQ_OFF + (sg < 4 ? sg - 1 : sg - 2) * 128 + h * 4, 0, SC1);
#pragma unroll
      for (int rt = 0; rt < 2; ++rt) {
        f32x4 c0, c1;
        proj2<true, 1024>(WW[0], WW[1], smem + (sg & 3) * XF_SEG + rt * KS * 1024 + lane * 16, c0, c1);
        if (rt == 0) stage(min(sg + 2, NSEG - 1));
        const u32x4 o = {pk(c0[0], c1[0]), pk(c0[1], c1[1]), pk(c0[2], c1[2]), pk(c0[3], c1[3])};
        __builtin_amdgcn_raw_buffer_store_b128(o, r, QX_OFF + ((sg * NH + h) * 16 + (wave >> 2) * 8 + rt * 4 + (wave & 3)) * 1024 + lane * 16, 0, SC1);
      }
    }
    wait_vm();
    bar();
    if (tid < 2) __builtin_amdgcn_raw_buffer_store_b32(1u, r, FLQ_OFF + (NSEG - 2 + tid) * 128 + h * 4, 0, SC1);
  }
  for (int it = f * 41 + wave; it < min(f * 41 + 41, 2592) && f < 64; it += 8) {
    const int ks = it / 18, bb = it % 18;
    u32 d[8];
#pragma unroll
    for (int jj = 0; jj < 8; ++jj)
      d[jj] = *(const u32*)(a.wd + (size_t)(ks * 32 + (jj >> 2) * 16 + g * 4 + (jj & 3)) * DM + bb * 32 + c16 * 2);
    const u32x4 lo = {plo(d[0], d[1]), plo(d[2], d[3]), plo(d[4], d[5]), plo(d[6], d[7])};
    const u32x4 hi = {phi(d[0], d[1]), phi(d[2], d[3]), phi(d[4], d[5]), phi(d[6], d[7])};
    __builtin_amdgcn_raw_buffer_store_b128(lo, r, WDF_OFF + (bb * 144 + ks) * 2048 + lane * 16, 0, SC1);
    __builtin_amdgcn_raw_buffer_store_b128(hi, r, WDF_OFF + (bb * 144 + ks) * 2048 + 1024 + lane * 16, 0, SC1);
  }
#pragma unroll 1
  for (int job = 0; job < 3 && f < 64; ++job) {
    int tid = threadIdx.x;
    asm volatile("" : "+v"(tid));
    const int hv = wave & 3, rt = wave >> 2;
    const int u0 = f * 72, sl = (u0 >> 6) + job;
    const int q0 = job == 0 ? (u0 & 63) : 0, q1 = min(u0 + 72 - sl * 64, 64);
    if (q1 <= q0) break;
    u32x4 WW[2][KS];
#pragma unroll
    for (int mat = 0; mat < 2; ++mat) {
      __syncthreads();
      u32x4 st[9];
#pragma unroll
      for (int i = 0; i < 9; ++i) {
        const int pc = i * 512 + tid;
        st[i] = *(const u32x4*)((mat ? a.wu : a.wg) + (size_t)(pc >> 3) * FF + sl * 64 + (pc & 7) * 8);
      }
#pragma unroll
      for (int i = 0; i < 9; ++i) { const int pc = i * 512 + tid; *(u32x4*)(smem + (pc >> 3) * 144 + (pc & 7) * 16) = st[i]; }
      __syncthreads();
      const char* wl = smem + g * 8 * 144 + hv * 32 + (c16 >> 1) * 4;
      const u32 sel = (c16 & 1) ? 0x07060302u : 0x05040100u;
#pragma unroll
      for (int t = 0; t < KS; ++t) {
#pragma unroll
        for (int p = 0; p < 4; ++p) {
          const int o = (t * 32 + 2 * p) * 144;
          WW[mat][t][p] = __builtin_amdgcn_perm(*(const u32*)(wl + o + 144), *(const u32*)(wl + o), sel);
        }
        asm volatile("" : "+v"(WW[mat][t]) :: "memory");
      }
    }
    __syncthreads();
    stage(q0); stage(min(q0 + 1, q1 - 1));
    for (int sg = q0; sg < q1; ++sg) {
      asm volatile("s_waitcnt vmcnt(5)" ::: "memory");
      bar();
      f32x4 cg, cu;
      proj2<true, 1024>(WW[0], WW[1], smem + (sg & 3) * XF_SEG + rt * KS * 1024 + lane * 16, cg, cu);
      stage(min(sg + 2, q1 - 1));
      float hh[4];
#pragma unroll
      for (int i = 0; i < 4; ++i) hh[i] = bfr(gelu_tanh(bfr(cg[i]))) * bfr(cu[i]);
      const u32x2 o = {pk(hh[0], hh[1]), pk(hh[2], hh[3])};
      __builtin_amdgcn_raw_buffer_store_b64(o, r, H_OFF + ((sg * 2 + rt) * 144 + sl * 2 + (hv >> 1)) * 1024 + lane * 16 + (hv & 1) * 8, 0, 0);
    }
    wait_vm();
  }
  wait_vm();
  __syncthreads();
  if (tid == 0) {
    if (f < 64) {
      __builtin_amdgcn_fence(__ATOMIC_RELEASE, "agent");
      wait_vm();
      __hip_atomic_fetch_add(cnt, 1u, __ATOMIC_RELAXED, __HIP_MEMORY_SCOPE_AGENT);
    }
    u32 spins = 0;
    while (__hip_atomic_load(cnt, __ATOMIC_RELAXED, __HIP_MEMORY_SCOPE_AGENT) < 64u) {
      __builtin_amdgcn_s_sleep(8);
      if (spin_fail(spins, tmo)) break;
    }
    __builtin_amdgcn_fence(__ATOMIC_ACQUIRE, "agent");
    wait_vm();
  }
  __syncthreads();
  {
    const int ft = (f + 32) % NF;
    const int tm = ft / 6, b3 = (ft % 6) * 3, tg = wave & 3, kh = wave >> 2;
    const unsigned char* hb = ws + H_OFF + ((size_t)(tm * 8 + tg * 2) * 144 + kh * 72) * 1024 + lane * 16;
    const unsigned char* wb = ws + WDF_OFF + (size_t)(b3 * 144 + kh * 72) * 2048 + lane * 16;
    f32x4 acc[2][6];
#pragma unroll
    for (int u = 0; u < 2; ++u)
#pragma unroll
      for (int i = 0; i < 6; ++i) acc[u][i] = f32x4{0.f, 0.f, 0.f, 0.f};
    u32x4 hf[2][2][2], wf[2][2][6];
    auto ld = [&](int st, int t0) {
#pragma unroll
      for (int t = 0; t < 2; ++t) {
        hf[st][t][0] = *(const u32x4*)(hb + (t0 + t) * 1024); hf[st][t][1] = *(const u32x4*)(hb + (144 + t0 + t) * 1024);
#pragma unroll
        for (int i = 0; i < 6; ++i) wf[st][t][i] = *(const u32x4*)(wb + ((i >> 1) * 144 + t0 + t) * 2048 + (i & 1) * 1024);
      }
    };
    auto mm = [&](int st) {
#pragma unroll
      for (int t = 0; t < 2; ++t)
#pragma unroll
        for (int i = 0; i < 6; ++i) { acc[0][i] = mfma(wf[st][t][i], hf[st][t][0], acc[0][i]); acc[1][i] = mfma(wf[st][t][i], hf[st][t][1], acc[1][i]); }
    };
    ld(0, 0);
#pragma unroll 1
    for (int t0 = 0; t0 < 72; t0 += 4) {
      ld(1, t0 + 2);
      __builtin_amdgcn_sched_barrier(0);
      mm(0);
      __builtin_amdgcn_sched_barrier(0);
      ld(0, min(t0 + 4, 70));
      __builtin_amdgcn_sched_barrier(0);
      mm(1);
      __builtin_amdgcn_sched_barrier(0);
    }
    if (kh == 1) {
#pragma unroll
      for (int u = 0; u < 2; ++u)
#pragma unroll
        for (int i = 0; i < 6; ++i) *(f32x4*)(smem + ((tg * 12 + u * 6 + i) * 64 + lane) * 16) = acc[u][i];
    }
    const bool mine = tm * 4 < SPLIT;
    if (mine && wave == 0) {
      u32 spins = 0;
      while (!allok(__builtin_amdgcn_raw_buffer_load_b32(r, FLA_OFF + (tm * 4 + ((lane >> 1) & 3)) * 128 + ((ft % 6) * 2 + (lane & 1)) * 4, 0, SC1))) {
        if (spin_fail(spins, tmo)) break;
        __builtin_amdgcn_s_sleep(16);
      }
    }
    __syncthreads();
    if (kh == 0) {
#pragma unroll
      for (int u = 0; u < 2; ++u)
#pragma unroll
        for (int b = 0; b < 3; ++b) {
          const f32x4 e0 = acc[u][2 * b] + *(const f32x4*)(smem + ((tg * 12 + u * 6 + 2 * b) * 64 + lane) * 16);
          const f32x4 e1 = acc[u][2 * b + 1] + *(const f32x4*)(smem + ((tg * 12 + u * 6 + 2 * b + 1) * 64 + lane) * 16);
          const u32x4 o = {pk(e0[0], e1[0]), pk(e0[1], e1[1]), pk(e0[2], e1[2]), pk(e0[3], e1[3])};
          const u32 eo = ((tm * 128 + tg * 32 + u * 16 + c16) * DM + (b3 + b) * 32 + g * 8) * 2;
          if (mine) {
            const u32x4 xv = *(const u32x4*)((const char*)a.x + eo), av = __builtin_amdgcn_raw_buffer_load_b128(r, AO_OFF + eo, 0, SC1);
            *(u32x4*)((char*)a.out + eo) = u32x4{fin(xv.x, av.x, o.x), fin(xv.y, av.y, o.y), fin(xv.z, av.z, o.z), fin(xv.w, av.w, o.w)};
          } else {
            __builtin_amdgcn_raw_buffer_store_b128(o, r, FO_OFF + eo, 0, SC1);
          }
        }
    }
    wait_vm();
    __syncthreads();
    if (!mine && tid == 0) __builtin_amdgcn_raw_buffer_store_b32(1u, r, FLF_OFF + ft * 128, 0, SC1);
  }
}

__global__ void __launch_bounds__(512, 2) k2_main(Args a) {
  extern __shared__ __attribute__((aligned(16))) char smem[];
  if (threadIdx.x == 0) {
    u32* ctl = (u32*)(a.ws + CTL_OFF);
    const u32 x = __builtin_amdgcn_s_getreg((4 - 1) << 11 | 0 << 6 | 20) & 7u;
    const u32 rank = __hip_atomic_fetch_add(ctl + 256 + x * 32, 1u, __ATOMIC_RELAXED, __HIP_MEMORY_SCOPE_AGENT);
    u32 slot = rank * 8 + x;
    if (rank >= 32) {
      u32 k = __hip_atomic_fetch_add(ctl + 192, 1u, __ATOMIC_RELAXED, __HIP_MEMORY_SCOPE_AGENT), spins = 0;
      for (;;) {
        u32 tot = 0;
        for (int i = 0; i < 8; ++i) tot += __hip_atomic_load(ctl + 256 + i * 32, __ATOMIC_RELAXED, __HIP_MEMORY_SCOPE_AGENT);
        if (tot >= 256u || spin_fail(spins, ctl + 64)) break;
      }
      slot = 256;
      for (int i = 0; i < 8 && slot == 256; ++i) {
        const u32 c = __hip_atomic_load(ctl + 256 + i * 32, __ATOMIC_RELAXED, __HIP_MEMORY_SCOPE_AGENT), fr = c < 32u ? 32u - c : 0u;
        if (k < fr) slot = (c + k) * 8 + i; else k -= fr;
      }
    }
    *(u32*)smem = slot;
  }
  __syncthreads();
  const int b = __builtin_amdgcn_readfirstlane(*(const u32*)smem), rr = b & 7, q = b >> 3;
  __syncthreads();
  if (b >= 256) return;
  if (rr == 0) role_a(a, q, smem);
  else if (rr < 4 && q < 24) role_b(a, (rr - 1) * 24 + q, smem);
  else if (rr == 4) role_v(a, q, smem);
  else if (rr > 4) role_f(a, (rr - 5) * 32 + q, smem);
  else role_o(a, (rr - 1) * 8 + (q - 24), smem);
}

extern "C" void kernel_launch(void* const* d_in, const int* in_sizes, int n_in, void* d_out, int out_size, void* d_ws,
                              size_t ws_size, hipStream_t stream) {
  Args a;
  a.x = (const u16*)d_in[0]; a.mem0 = (const u16*)d_in[1]; a.wq = (const u16*)d_in[2]; a.wk = (const u16*)d_in[3];
  a.wv = (const u16*)d_in[4]; a.wo = (const u16*)d_in[5]; a.wg = (const u16*)d_in[8]; a.wu = (const u16*)d_in[9];
  a.wd = (const u16*)d_in[10];
  a.ws = (unsigned char*)(((uintptr_t)d_ws + 65535) & ~(uintptr_t)65535);
  a.out = (u16*)d_out;
  (void)hipFuncSetAttribute((const void*)k2_main, hipFuncAttributeMaxDynamicSharedMemorySize, LDS_BYTES);
  k1_prep<<<K1_GRID, 256, 0, stream>>>(a);
  k2_main<<<256, 512, LDS_BYTES, stream>>>(a);
}
```
